```python
import torch

def _softmax(x, axis=-1):
    return torch.softmax(x, dim=axis)
def _gelu(x):
    return torch.nn.functional.gelu(x, approximate="tanh")
def _silu(x):
    return torch.nn.functional.silu(x)
def _sigmoid(x):
    return torch.sigmoid(x)
def _layernorm(x, axis=-1, eps=1e-5):
    m = x.mean(dim=axis, keepdim=True)
    v = x.var(dim=axis, keepdim=True, unbiased=False)
    return (x - m) / torch.sqrt(v + eps)
def _scatter_add(tgt, idx, src, axis=0):
    return tgt.clone().index_add_(axis, idx, src)
def _segment_sum(src, seg, n):
    out = torch.zeros((n,) + src.shape[1:], dtype=src.dtype)
    return out.index_add_(0, seg, src)
def _pad(x, pw):
    flat = tuple(v for pair in reversed(pw) for v in pair)
    return torch.nn.functional.pad(x, flat)

kernel_name = 'sequential_preLN__grouped_kv_mqa__mixer_channel_mlp__prompt'

def setup_inputs():

    _w = (lambda a: (lambda t: t.to(torch.bfloat16) if t.is_floating_point() else t)(torch.as_tensor(a)))
    def _A_setup():
        B, S, D, H = 1, 2048, 640, 32
        Dh = D // H
        return {
            "x": _w((lambda *s: torch.randn(*s, dtype=torch.bfloat16) * 0.05)(B, S, D)),
            "wq": _w((lambda *s: torch.randn(*s, dtype=torch.bfloat16) * 0.05)(D, H * Dh)),
            "wk": _w((lambda *s: torch.randn(*s, dtype=torch.bfloat16) * 0.05)(D, Dh)),
            "wv": _w((lambda *s: torch.randn(*s, dtype=torch.bfloat16) * 0.05)(D, Dh)),
            "wo": _w((lambda *s: torch.randn(*s, dtype=torch.bfloat16) * 0.05)(H * Dh, D)),
        }
    _A_in = _A_setup()

    def _B_setup():
        B, S, D, F = 1, 2048, 640, 1728
        return {
            "x": _w((lambda *s: torch.randn(*s, dtype=torch.bfloat16) * 0.05)(B, S, D)),
            "w1": _w((lambda *s: torch.randn(*s, dtype=torch.bfloat16) * 3.2)(D, F) * (D ** -0.5)),
            "b1": _w((lambda *s: torch.randn(*s, dtype=torch.bfloat16) * 1.6)(F)),
            "w2": _w((lambda *s: torch.randn(*s, dtype=torch.bfloat16) * 0.2)(F, D) * (F ** -0.5)),
            "b2": _w((lambda *s: torch.randn(*s, dtype=torch.bfloat16) * 0.4)(D)),
        }
    _B_in = _B_setup()

    return {
        "A_x": _A_in["x"],
        "A_wq": _A_in["wq"],
        "A_wk": _A_in["wk"],
        "A_wv": _A_in["wv"],
        "A_wo": _A_in["wo"],
        "B_x": _B_in["x"],
        "B_w1": _B_in["w1"],
        "B_b1": _B_in["b1"],
        "B_w2": _B_in["w2"],
        "B_b2": _B_in["b2"],
    }

def reference(A_x, A_wq, A_wk, A_wv, A_wo, B_x, B_w1, B_b1, B_w2, B_b2):
    def _A_ref(x, wq, wk, wv, wo):
        B, S, D = x.shape
        H = 32
        Dh = D // H
        q = (lambda x, *d: x.permute(*d))((x @ wq).reshape(B, S, H, Dh), 0, 2, 1, 3)
        k = x @ wk
        v = x @ wv
        scores = torch.einsum("bhqd,bkd->bhqk", q, k) * (Dh ** -0.5)
        p = _softmax(scores, axis=-1)
        o = torch.einsum("bhqk,bkd->bhqd", p, v)
        return (lambda x, *d: x.permute(*d))(o, 0, 2, 1, 3).reshape(B, S, H * Dh) @ wo

    def _B_ref(x, w1, b1, w2, b2):
        y = _layernorm(x, axis=-1)
        h = _gelu(torch.einsum("bsd,df->bsf", y, w1) + b1)
        return torch.einsum("bsf,fd->bsd", h, w2) + b2

    x = A_x
    h = x + _A_ref(_layernorm(x, axis=-1), A_wq, A_wk, A_wv, A_wo)
    h2 = h + _B_ref(_layernorm(h, axis=-1), B_w1, B_b1, B_w2, B_b2)
    return h2
```

```cpp
#include <hip/hip_runtime.h>
#include <hip/hip_bf16.h>
typedef unsigned short u16;
typedef __bf16 bf16x8 __attribute__((ext_vector_type(8)));
typedef __bf16 bf16x16 __attribute__((ext_vector_type(16)));
typedef short s16x4 __attribute__((ext_vector_type(4)));
typedef float f32x4 __attribute__((ext_vector_type(4)));
typedef float f32x8 __attribute__((ext_vector_type(8)));
typedef float f32x16 __attribute__((ext_vector_type(16)));
typedef unsigned u32x4 __attribute__((ext_vector_type(4)));
typedef unsigned u32x2 __attribute__((ext_vector_type(2)));
#define LAS __attribute__((address_space(3)))
constexpr int S = 2048, D = 640, H = 32, DH = 20, F = 1728;
__device__ __forceinline__ float b2f(u16 b) { return __uint_as_float((unsigned)b << 16); }
__device__ __forceinline__ u16 f2b(float f) { __bf16 h = (__bf16)f; return __builtin_bit_cast(u16, h); }
__device__ __forceinline__ unsigned pk2(float a, float b) { return (unsigned)f2b(a) | ((unsigned)f2b(b) << 16); }

enum { M_Q = 0, M_KV = 1, M_WO = 2, M_F1 = 3, M_F2 = 4 };
struct GArgs { const u16* A; const u16* W; const u16* W2; const u16* bias; const u16* res; u16* out; u16* out2; };

template <int MODE, int NT, int K, int LDW>
__device__ __forceinline__ void gemm_body(const GArgs& a, int tg, int n0, int nvalid, char* lds) {
  const int tid = threadIdx.x, wv = tid >> 6, lane = tid & 63, tk = lane & 15, g = lane >> 4;
  const int tok = tg * 64 + wv * 16 + tk;
  constexpr int KS = K / 32, PITCH = NT * 32 + 32;
  constexpr bool LN = (MODE == M_Q || MODE == M_KV || MODE == M_F1);
  float mean = 0.f, rstd = 1.f;
  if (LN) {
    float s = 0.f;
    for (int ks = 0; ks < KS; ++ks) { u32x4 v = *(const u32x4*)(a.A + (size_t)tok * K + ks * 32 + g * 8);
      for (int j = 0; j < 4; ++j) s += __uint_as_float(v[j] << 16) + __uint_as_float(v[j] & 0xffff0000u); }
    s += __shfl_xor(s, 16); s += __shfl_xor(s, 32); mean = s * (1.f / K);
    float q = 0.f;
    for (int ks = 0; ks < KS; ++ks) { u32x4 v = *(const u32x4*)(a.A + (size_t)tok * K + ks * 32 + g * 8);
      for (int j = 0; j < 4; ++j) { float x0 = __uint_as_float(v[j] << 16) - mean, x1 = __uint_as_float(v[j] & 0xffff0000u) - mean; q += x0 * x0 + x1 * x1; } }
    q += __shfl_xor(q, 16); q += __shfl_xor(q, 32);
    const float var = q * (1.f / K);
    rstd = rsqrtf(var + 1e-5f);
    if (MODE == M_F1) rstd *= rsqrtf(var * rstd * rstd + 1e-5f);
  }
  f32x4 acc[NT];
  for (int t = 0; t < NT; ++t) acc[t] = f32x4{0, 0, 0, 0};
  constexpr int KC = 64;
  for (int k0 = 0; k0 < K; k0 += KC) {
    __syncthreads();
    for (int c = tid; c < KC * NT * 4; c += 256) {
      const int r = c / (NT * 4), p = c % (NT * 4), n = p * 4;
      u32x2 v = {0, 0};
      if (MODE == M_KV) { if (n < 20) v = *(const u32x2*)(a.W + (size_t)(k0 + r) * 20 + n); else if (n < 40) v = *(const u32x2*)(a.W2 + (size_t)(k0 + r) * 20 + n - 20); }
      else if (n < nvalid) v = *(const u32x2*)(a.W + (size_t)(k0 + r) * LDW + n0 + n);
      *(u32x2*)(lds + r * PITCH + p * 8) = v;
    }
    __syncthreads();
#pragma unroll
    for (int kk = 0; kk < KC / 32; ++kk) {
      u32x4 v = *(const u32x4*)(a.A + (size_t)tok * K + k0 + kk * 32 + g * 8);
      if (LN) for (int j = 0; j < 4; ++j) v[j] = pk2((__uint_as_float(v[j] << 16) - mean) * rstd, (__uint_as_float(v[j] & 0xffff0000u) - mean) * rstd);
      const bf16x8 bf = __builtin_bit_cast(bf16x8, v);
#pragma unroll
      for (int t = 0; t < NT; ++t) {
        const LAS char* p = (const LAS char*)lds + (kk * 32 + g * 8 + (tk >> 2)) * PITCH + t * 32 + (tk & 3) * 8;
        s16x4 lo = __builtin_amdgcn_ds_read_tr16_b64_v4i16((LAS s16x4*)p), hi = __builtin_amdgcn_ds_read_tr16_b64_v4i16((LAS s16x4*)(p + 4 * PITCH));
        const bf16x8 wf = __builtin_bit_cast(bf16x8, __builtin_shufflevector(lo, hi, 0, 1, 2, 3, 4, 5, 6, 7));
        acc[t] = __builtin_amdgcn_mfma_f32_16x16x32_bf16(wf, bf, acc[t], 0, 0, 0);
      }
    }
  }
#pragma unroll
  for (int t = 0; t < NT; ++t) {
    const int nl = t * 16 + g * 4, n = n0 + nl;
    if (nl >= nvalid) continue;
    float o[4] = {acc[t][0], acc[t][1], acc[t][2], acc[t][3]};
    if (MODE == M_Q) { const float c = 0.22360679774997896f * 1.4426950408889634f;
      *(u32x2*)(a.out + (size_t)tok * D + n) = u32x2{pk2(o[0] * c, o[1] * c), pk2(o[2] * c, o[3] * c)}; }
    if (MODE == M_KV) { if (nl < 20) *(u32x2*)(a.out + (size_t)tok * 20 + nl) = u32x2{pk2(o[0], o[1]), pk2(o[2], o[3])};
      else for (int r = 0; r < 4; ++r) a.out2[(size_t)(nl - 20 + r) * S + tok] = f2b(o[r]); }
    if (MODE == M_WO || MODE == M_F2) { u32x2 rv = *(const u32x2*)(a.res + (size_t)tok * D + n);
      if (MODE == M_F2) for (int r = 0; r < 4; ++r) o[r] += b2f(a.bias[n + r]);
      o[0] += __uint_as_float(rv[0] << 16); o[1] += __uint_as_float(rv[0] & 0xffff0000u); o[2] += __uint_as_float(rv[1] << 16); o[3] += __uint_as_float(rv[1] & 0xffff0000u);
      *(u32x2*)(a.out + (size_t)tok * D + n) = u32x2{pk2(o[0], o[1]), pk2(o[2], o[3])}; }
    if (MODE == M_F1) { for (int r = 0; r < 4; ++r) { const float x = o[r] + b2f(a.bias[n + r]);
        const float u = x * (0.7978845608028654f + 0.7978845608028654f * 0.044715f * x * x);
        o[r] = x / (1.f + __expf(-2.f * u)); }
      *(u32x2*)(a.out + (size_t)tok * F + n) = u32x2{pk2(o[0], o[1]), pk2(o[2], o[3])}; }
  }
}

__global__ void __launch_bounds__(256) k_qkv(GArgs q, GArgs kv) {
  __shared__ __attribute__((aligned(16))) char lds[64 * (5 * 32 + 32)];
  const int tg = blockIdx.x / 9, j = blockIdx.x % 9;
  if (j < 8) gemm_body<M_Q, 5, D, D>(q, tg, j * 80, 80, lds); else gemm_body<M_KV, 3, D, 20>(kv, tg, 0, 40, lds);
}
template <int MODE, int NT, int K, int LDW, int NTOT>
__global__ void __launch_bounds__(256) k_gemm(GArgs a) {
  __shared__ __attribute__((aligned(16))) char lds[64 * (NT * 32 + 32)];
  const int tg = blockIdx.x >> 3, j = blockIdx.x & 7, n0 = j * NT * 16;
  gemm_body<MODE, NT, K, LDW>(a, tg, n0, min(NT * 16, NTOT - n0), lds);
}

__global__ void __launch_bounds__(256) k_attn(const u16* Q, const u16* Kb, const u16* Vt, u16* O) {
  const int tid = threadIdx.x, wv = tid >> 6, lane = tid & 63, r = lane & 31, h = lane >> 5;
  const int unit = blockIdx.x * 4 + wv, head = unit & 31, tok = (unit >> 5) * 32 + r;
  const bf16x8 q0 = *(const bf16x8*)(Q + (size_t)tok * D + head * DH + h * 8);
  s16x4 q1 = *(const s16x4*)(Q + (size_t)tok * D + head * DH + 16); if (h) q1 = s16x4{0, 0, 0, 0};
  f32x16 o = {}; float m = -1e30f;
  const int vr = r < 20 ? r : 0;
  for (int k0 = 0; k0 < S; k0 += 32) {
    const bf16x8 kf0 = *(const bf16x8*)(Kb + (size_t)(k0 + r) * DH + h * 8);
    const s16x4 kf1 = *(const s16x4*)(Kb + (size_t)(k0 + r) * DH + 16);
    u32x4 v0 = *(const u32x4*)(Vt + (size_t)vr * S + k0 + h * 8), v1 = *(const u32x4*)(Vt + (size_t)vr * S + k0 + 16 + h * 8);
    if (r >= 20) { v0 = u32x4{0x3f803f80u, 0x3f803f80u, 0x3f803f80u, 0x3f803f80u}; v1 = v0; }
    f32x16 s = {};
    s = __builtin_amdgcn_mfma_f32_32x32x16_bf16(kf0, q0, s, 0, 0, 0);
    s = __builtin_amdgcn_mfma_f32_32x32x8bf16_1k(kf1, q1, s, 0, 0, 0);
    float mx = s[0];
    for (int i = 1; i < 16; ++i) mx = fmaxf(mx, s[i]);
    mx = fmaxf(mx, __shfl_xor(mx, 32));
    const float mn = fmaxf(m, mx), al = __builtin_amdgcn_exp2f(m - mn); m = mn;
    for (int i = 0; i < 16; ++i) { s[i] = __builtin_amdgcn_exp2f(s[i] - mn); o[i] *= al; }
    const f32x8 p0 = __builtin_shufflevector(s, s, 0, 1, 2, 3, 4, 5, 6, 7), p1 = __builtin_shufflevector(s, s, 8, 9, 10, 11, 12, 13, 14, 15);
    o = __builtin_amdgcn_smfmac_f32_32x32x32_bf16(__builtin_bit_cast(bf16x8, v0), __builtin_bit_cast(bf16x16, p0), o, 0xDDDD, 0, 0);
    o = __builtin_amdgcn_smfmac_f32_32x32x32_bf16(__builtin_bit_cast(bf16x8, v1), __builtin_bit_cast(bf16x16, p1), o, 0xDDDD, 0, 0);
  }
  const float il = 1.f / o[12];
  u16* op = O + (size_t)tok * D + head * DH + h * 4;
  *(u32x2*)(op) = u32x2{pk2(o[0] * il, o[1] * il), pk2(o[2] * il, o[3] * il)};
  *(u32x2*)(op + 8) = u32x2{pk2(o[4] * il, o[5] * il), pk2(o[6] * il, o[7] * il)};
  if (!h) *(u32x2*)(op + 16) = u32x2{pk2(o[8] * il, o[9] * il), pk2(o[10] * il, o[11] * il)};
}
#define NDUMMY 0
__global__ void __launch_bounds__(256) k_dummy(unsigned* p) { if (threadIdx.x == 0) p[blockIdx.x * 32] = 1; }

extern "C" void kernel_launch(void* const* d_in, const int* in_sizes, int n_in, void* d_out, int out_size, void* d_ws, size_t ws_size, hipStream_t stream) {
  const u16 *x = (const u16*)d_in[0], *wq = (const u16*)d_in[1], *wk = (const u16*)d_in[2], *wv = (const u16*)d_in[3], *wo = (const u16*)d_in[4];
  const u16 *w1 = (const u16*)d_in[6], *b1 = (const u16*)d_in[7], *w2 = (const u16*)d_in[8], *b2 = (const u16*)d_in[9];
  char* ws = (char*)d_ws;
  u16 *Q = (u16*)ws, *O = (u16*)(ws + (4 << 20)), *Hh = (u16*)(ws + (8 << 20)), *Hid = (u16*)(ws + (12 << 20)), *Kb = (u16*)(ws + (24 << 20)), *Vt = (u16*)(ws + (25 << 20));
  for (int i = 0; i < NDUMMY; ++i) k_dummy<<<256, 256, 0, stream>>>((unsigned*)(ws + (26 << 20)));
  k_qkv<<<32 * 9, 256, 0, stream>>>(GArgs{x, wq, wq, wq, wq, Q, Q}, GArgs{x, wk, wv, wk, wk, Kb, Vt});
  k_attn<<<512, 256, 0, stream>>>(Q, Kb, Vt, O);
  k_gemm<M_WO, 5, D, D, D><<<256, 256, 0, stream>>>(GArgs{O, wo, wo, wo, x, Hh, Hh});
  k_gemm<M_F1, 14, D, F, F><<<256, 256, 0, stream>>>(GArgs{Hh, w1, w1, b1, x, Hid, Hid});
  k_gemm<M_F2, 5, F, D, D><<<256, 256, 0, stream>>>(GArgs{Hid, w2, w2, b2, Hh, (u16*)d_out, (u16*)d_out});
}
```

```cpp
#include <hip/hip_runtime.h>

namespace {
constexpr unsigned KB = 1024, OOB = 0x40000000u, REC = 6656;
constexpr unsigned W_F = 0x1000, W_KV = 0x20000, W_ST = 0x60000, W_O = 0x100000, W_H = 0x400000, W_HID = 0x700000,
                   W_WO = 0xE00000, W_W1 = 0xF00000, W_W2 = 0x1200000;
struct Tab { unsigned v[1024][48]; };
constexpr Tab make_tab() {
  Tab T{};
  for (unsigned b = 0; b < 256; ++b) for (unsigned w = 0; w < 4; ++w) {
    unsigned* e = T.v[b * 4 + w];
    const unsigned g = (b & 7) * 4 + (b >> 6), j = (b >> 3) & 7, tt = 4 * g + w, c = w & 1, hh = w >> 1, x3 = w ^ 3;
    const unsigned kt = (j >> 1) & 1, half = j & 1, bp = 5 * (j >> 1) + 3 * half, bs = 5 * (j >> 1) + 2;
    unsigned r0 = 8 * w, r1 = 4, pit = 1280, s0 = 0, step = 128, d0 = 0, js = 0, bo = 0;
    const bool w1 = b < 108;
    if (w1) {
      const unsigned strip = b >> 2, qd = b & 3, B = 2 * strip + hh, P = B + (B >= 27);
      pit = 3456; s0 = qd * 160 * 3456 + strip * 128; step = 32 * 3456; bo = B * 64 + c * 8;
      d0 = W_W1 + (P * 42 + c * 21 + 5 * qd) * KB;
    } else if (b < 216) {
      const unsigned B = (b - 108) >> 1, P = B + (B >= 27), jj = P / 7, pp = P % 7;
      r0 += 32 * B; s0 = (b & 1) * 640; js = 280 * KB;
      d0 = W_W2 + ((jj >> 1) * 14 + 2 * pp + (jj & 1)) * 5 * KB + (b & 1) * 4 * js;
    } else {
      const unsigned ks = (b - 216) >> 1, ps = ks / 10, s = ks % 10;
      s0 = (b & 1) * 640; js = 100 * KB; d0 = W_WO + ks * 5 * KB + (b & 1) * 4 * js;
      if (s < 8) { r0 = (4 * s + 2 * ps + hh) * 20 + 4 * c; r1 = 8; } else { r0 = (16 * (s - 8) + 4 * w + 2 * ps) * 20 + 16; r1 = 20; }
    }
    const unsigned ob = W_O + (4 * g + 2 * c) * 20 * KB, sl = (w - j) & 3;
    e[0] = W_KV + g * REC + (j < 4 ? (c ? 6144 + half * 256 + kt * 8 : kt * KB + half * 256) : 2048 + (j & 3) * KB + c * 256);
    e[1] = w >= 2 ? 0 : (j < 4 && c ? 16 : 64);
    e[2] = j >= 4 && c ? 4 : 16;
    e[3] = r0; e[4] = r1; e[5] = pit; e[6] = b < 216 ? 1728 * 640 * 2 : 640 * 640 * 2;
    e[7] = sl ? (20 + 20 * sl) * KB : 128 * KB;
    e[8] = W_KV + w * 8 * REC;
    e[9] = ob + hh * 512 + j * KB; e[10] = ob + 8 * KB + hh * 8 + j * 256;
    e[11] = W_O + tt * 20 * KB; e[12] = W_H + tt * 20 * KB; e[13] = e[12] + j * KB; e[14] = e[12] + (16 + (j >> 1)) * KB + half * 512;
    e[15] = W_ST + tt * KB;
    for (unsigned u = 0; u < 4; ++u) e[16 + u] = W_HID + ((4 * g + (u ^ x3)) * 56 + w * 14) * KB;
    e[20] = e[15] + (j >> 1) * 256 + half * 8;
    e[21] = W_HID + (tt * 56 + (j >> 1) * 14 + half) * KB;
    for (unsigned i = 0; i < 3; ++i) {
      const unsigned B = i < 2 ? bp + i : bs, r = B >> 2;
      e[22] |= ((r < 4 ? r : 16) << 3 | (B & 3) << 1 | (i == 2 ? half : 0)) << (10 * i);
    }
    e[23] = x3;
    e[24] = W_W1 + (j * 294 + w * 4) * KB; e[25] = W_WO + (j * 100 + w * 4) * KB; e[26] = W_W2 + (j * 280 + w * 70) * KB; e[27] = w * 4 * KB;
    e[30] = bp * 64; e[31] = bs * 64 + half * 32; e[28] = e[30] + tt * 16 * 1280; e[29] = e[31] + tt * 16 * 1280;
    e[32] = W_F + (g * 8 + j) * 128; e[33] = W_F + w * 64 * 128;
    for (unsigned i = 0; i < 5; ++i) {
      const unsigned m = 2 * i + hh, r = m % 5;
      const unsigned jl = r == 2 ? 2 * (m / 5) + c : 2 * (m / 5) + r / 3, tile = r == 2 ? 4 : 2 * (r % 3) + c;
      e[34 + i] = w1 ? d0 + i * KB : d0 + jl * js + tile * KB;
    }
    e[39] = w1 ? d0 + 20 * KB - (b & 3) * (5 * KB - 256) : 0;
    e[40] = s0; e[41] = step; e[42] = bo;
    e[43] = ((j & 3) == 3) | c << 1 | (!w1 || (b & 3)) << 2 | !w1 << 3 | (!w1 && hh == 0) << 4 | (!w1 && hh == 1) << 5 | (j >= 4 || w) << 6;
  }
  return T;
}
__device__ const Tab kTab = make_tab();
}

__global__ void __launch_bounds__(256, 1) __attribute__((aligned(4096)))
fused_block(const void* x, const void* wq, const void* wk, const void* wv, const void* wo,
            const void* w1, const void* b1, const void* w2, const void* b2, void* out, void* ws) {
  __shared__ __attribute__((aligned(16))) char lds[163840];
  asm volatile(
    "Lk0:\n"
    "s_load_dwordx2 s[8:9], %0, 0x0\n"
    "s_mov_b32 s40, %1\n"
    "v_lshrrev_b32 v16, 6, v0\n"
    "v_and_b32 v249, 63, v0\n"
    "s_bfe_u32 s42, s40, 0x30003\n"
    "v_readfirstlane_b32 s41, v16\n"
    "s_lshr_b32 s49, s42, 2\n"
    "s_lshl_b32 s49, s49, 3\n"
    "s_load_dwordx2 s[16:17], %0, s49 offset:0x10\n"
    "s_cmp_lt_u32 s40, 0xd8\n"
    "s_cselect_b32 s50, 24, 0\n"
    "s_cmp_lt_u32 s40, 0x6c\n"
    "s_cselect_b32 s50, 8, s50\n"
    "s_load_dwordx2 s[20:21], %0, s50 offset:0x20\n"
    "s_load_dwordx2 s[12:13], %0, 0x8\n"
    "s_load_dwordx2 s[24:25], %0, 0x50\n"
    "s_load_dwordx2 s[32:33], %0, 0x30\n"
    "s_load_dwordx2 s[36:37], %0, 0x40\n"
    "s_load_dwordx2 s[28:29], %0, 0x48\n"
    "s_getpc_b64 s[52:53]\n"
    "s_and_b32 s52, s52, 0xfffff000\n"
    "s_mov_b32 s54, 0xbc6c\n"
    "s_mov_b32 s55, 0x20000\n"
    "s_bfe_u32 s49, s40, 0x10003\n"
    "s_bfe_u32 s50, s40, 0x40003\n"
    "s_add_u32 s50, s50, 1\n"
    "s_cmp_lt_u32 s41, 2\n"
    "s_cselect_b32 s49, s49, s50\n"
    "s_lshl_b32 s49, s49, 1\n"
    "s_add_u32 s49, s49, s41\n"
    "s_lshl_b32 s49, s49, 11\n"
    "v_lshrrev_b32 v251, 4, v249\n"
    "v_lshlrev_b32 v1, 4, v249\n"
    "v_and_b32 v250, 15, v249\n"
    "v_lshlrev_b32 v16, 7, v250\n"
    "v_add_u32 v16, s49, v16\n"
    "v_lshl_add_u32 v16, v251, 28, v16\n"
    "buffer_load_dword v248, v16, s[52:55], 0 offen\n"
    "s_mov_b32 s10, 0x280000\n"
    "s_mov_b32 s11, 0x20000\n"
    "s_mov_b32 s18, 0x6400\n"
    "s_mov_b32 s19, 0x20000\n"
    "s_lshr_b32 s44, s40, 6\n"
    "s_and_b32 s48, s40, 7\n"
    "s_lshl_b32 s48, s48, 2\n"
    "s_add_u32 s48, s48, s44\n"
    "s_lshl_b32 s48, s48, 2\n"
    "s_mul_i32 s45, s41, 0x1400\n"
    "s_add_u32 s30, s44, 0\n"
    "s_sub_u32 s50, s30, 5\n"
    "s_min_u32 s30, s30, s50\n"
    "s_mul_i32 s30, s30, 0x1400\n"
    "s_add_u32 s31, s44, 1\n"
    "s_sub_u32 s50, s31, 5\n"
    "s_min_u32 s31, s31, s50\n"
    "s_mul_i32 s31, s31, 0x1400\n"
    "s_add_u32 s38, s44, 2\n"
    "s_sub_u32 s50, s38, 5\n"
    "s_min_u32 s38, s38, s50\n"
    "s_mul_i32 s38, s38, 0x1400\n"
    "s_add_u32 s39, s44, 3\n"
    "s_sub_u32 s50, s39, 5\n"
    "s_min_u32 s39, s39, s50\n"
    "s_mul_i32 s39, s39, 0x1400\n"
    "s_add_u32 s34, s44, 4\n"
    "s_sub_u32 s50, s34, 5\n"
    "s_min_u32 s34, s34, s50\n"
    "s_mul_i32 s34, s34, 0x1400\n"
    "s_lshl_b32 s23, s41, 10\n"
    "s_lshl_b32 s49, s41, 6\n"
    "v_xor_b32 v92, s49, v1\n"
    "v_xor_b32 v93, 16, v92\n"
    "v_add_u32 v93, 0x100, v93\n"
    "v_xor_b32 v94, 32, v92\n"
    "v_add_u32 v94, 0x200, v94\n"
    "v_xor_b32 v95, 48, v92\n"
    "v_add_u32 v95, 0x300, v95\n"
    "v_xor_b32 v18, v251, v250\n"
    "v_lshlrev_b32 v18, 4, v18\n"
    "s_mov_b32 s51, 0x400\n"
    "v_xor_b32 v17, s49, v18\n"
    "v_mul_u32_u24 v16, 0x500, v251\n"
    "v_add3_u32 v96, v16, v17, s51\n"
    "s_mov_b32 s14, 0xc8000\n"
    "s_mov_b32 s15, 0x20000\n"
    "s_mov_b32 s26, 0x1900000\n"
    "s_mov_b32 s27, 0x20000\n"
    "s_mov_b32 s43, 0x8000\n"
    "s_mul_i32 s47, s41, 0xa000\n"
    "s_mul_i32 s49, s42, 0xa0\n"
    "s_add_u32 s47, s47, s49\n"
    "v_bfe_u32 v16, v249, 2, 2\n"
    "v_and_b32 v17, 3, v249\n"
    "v_mul_u32_u24 v22, 0x1400, v16\n"
    "v_add_u32 v22, v22, v18\n"
    "v_lshl_add_u32 v20, v17, 10, v22\n"
    "v_lshl_add_u32 v21, v17, 8, v22\n"
    "v_add_u32 v21, 0x1000, v21\n"
    "s_lshl_b32 s49, s41, 6\n"
    "v_xor_b32 v72, s49, v20\n"
    "v_xor_b32 v73, s49, v21\n"
    "v_add_u32 v72, 0x20000, v72\n"
    "v_add_u32 v73, 0x20000, v73\n"
    "v_mov_b32 v252, 0x3f803f80\n"
    "v_mov_b32 v253, 0x3f803f80\n"
    "v_mov_b32 v254, 0x3f803f80\n"
    "v_mov_b32 v255, 0x3f803f80\n"
    "s_lshl_b32 s49, s40, 2\n"
    "s_add_u32 s49, s49, s41\n"
    "s_mul_i32 s49, s49, 0x320\n"
    "v_min_u32 v106, 6, v249\n"
    "v_lshlrev_b32 v106, 7, v106\n"
    "s_waitcnt lgkmcnt(0)\n"
    "buffer_load_dword v107, v106, s[12:15], s49 offen\n"
    "s_add_u32 s51, s45, 0x1000\n"
    "s_add_u32 m0, s51, 0x19000\n"
    "s_nop 0\n"
    "buffer_load_dwordx4 v1, s[16:19], s51 offen  lds\n"
    "s_mov_b32 s51, 0x6000\n"
    "s_add_u32 m0, s51, 0x19000\n"
    "s_nop 0\n"
    "buffer_load_dwordx4 v1, s[16:19], s51 offen  lds\n"
    "s_add_u32 s51, s30, s23\n"
    "s_add_u32 m0, s51, 0x19000\n"
    "s_nop 0\n"
    "buffer_load_dwordx4 v1, s[16:19], s51 offen  lds\n"
    "s_add_u32 s51, s31, s23\n"
    "s_add_u32 m0, s51, 0x19000\n"
    "s_nop 0\n"
    "buffer_load_dwordx4 v1, s[16:19], s51 offen  lds\n"
    "s_add_u32 m0, s45, 0x20000\n"
    "s_add_u32 s46, s42, 0\n"
    "s_and_b32 s46, s46, 3\n"
    "s_add_u32 s46, s46, s48\n"
    "s_mul_i32 s46, s46, 0x5000\n"
    "s_add_u32 s46, s46, s45\n"
    "buffer_load_dwordx4 v92, s[8:11], s46 offen offset:0 nt lds\n"
    "buffer_load_dwordx4 v93, s[8:11], s46 offen offset:1024 nt lds\n"
    "buffer_load_dwordx4 v94, s[8:11], s46 offen offset:2048 nt lds\n"
    "buffer_load_dwordx4 v95, s[8:11], s46 offen offset:3072 nt lds\n"
    "s_add_u32 m0, s45, 0x21000\n"
    "s_nop 0\n"
    "buffer_load_dwordx4 v96, s[8:11], s46 offen offset:0 nt lds\n"
    "s_lshl_b32 s49, s40, 2\n"
    "s_add_u32 s49, s49, s41\n"
    "s_mul_i32 s49, s49, 0xc0\n"
    "s_load_dwordx16 s[52:67], %4, s49 offset:0x0\n"
    "s_load_dwordx16 s[68:83], %4, s49 offset:0x40\n"
    "s_load_dwordx8 s[84:91], %4, s49 offset:0x80\n"
    "s_load_dwordx4 s[92:95], %4, s49 offset:0xa0\n"
    "s_lshl_b32 s49, s41, 12\n"
    "s_add_u32 s49, s49, 0x19000\n"
    "v_add_u32 v77, s49, v1\n"
    "s_and_b32 s49, s41, 1\n"
    "s_lshl_b32 s49, s49, 11\n"
    "s_add_u32 s49, s49, 0x19000\n"
    "v_add_u32 v78, s49, v1\n"
    "v_and_b32 v23, 1, v251\n"
    "v_lshlrev_b32 v23, 3, v23\n"
    "v_lshl_add_u32 v23, v250, 4, v23\n"
    "v_lshrrev_b32 v22, 1, v251\n"
    "v_lshl_add_u32 v247, v22, 9, v23\n"
    "v_lshl_add_u32 v7, v22, 8, v23\n"
    "v_mov_b32 v2, 0xdddddddd\n"
    "v_and_b32 v13, 0x1ff, v1\n"
    "v_add_u32 v3, 0x10000, v1\n"
    "v_add_u32 v4, 0x20000, v1\n"
    "v_lshrrev_b32 v22, 2, v249\n"
    "v_mul_u32_u24 v22, 0x500, v22\n"
    "v_lshl_add_u32 v11, v17, 4, v22\n"
    "v_lshl_add_u32 v12, v17, 3, v22\n"
    "v_and_b32 v22, 1, v251\n"
    "v_mul_u32_u24 v22, 0x5000, v22\n"
    "v_lshl_add_u32 v22, v250, 4, v22\n"
    "v_lshrrev_b32 v23, 5, v249\n"
    "v_lshl_add_u32 v5, v23, 8, v22\n"
    "v_lshl_add_u32 v6, v23, 30, v22\n"
    "v_min_u32 v23, 1, v251\n"
    "v_lshl_add_u32 v8, v23, 30, v1\n"
    "v_min_u32 v23, 1, v249\n"
    "v_lshlrev_b32 v14, 30, v23\n"
    "v_lshl_add_u32 v22, v251, 2, v16\n"
    "v_mul_u32_u24 v22, 0xa0, v22\n"
    "v_mul_u32_u24 v23, 40, v17\n"
    "v_add3_u32 v23, v23, v22, 32\n"
    "v_lshl_add_u32 v22, v17, 3, v22\n"
    "s_add_u32 s49, s41, 0\n"
    "s_and_b32 s49, s49, 3\n"
    "s_mul_i32 s49, s49, 0x1400\n"
    "v_add_u32 v239, s49, v22\n"
    "v_add_u32 v243, s49, v23\n"
    "s_add_u32 s49, s41, 1\n"
    "s_and_b32 s49, s49, 3\n"
    "s_mul_i32 s49, s49, 0x1400\n"
    "v_add_u32 v240, s49, v22\n"
    "v_add_u32 v244, s49, v23\n"
    "s_add_u32 s49, s41, 2\n"
    "s_and_b32 s49, s49, 3\n"
    "s_mul_i32 s49, s49, 0x1400\n"
    "v_add_u32 v241, s49, v22\n"
    "v_add_u32 v245, s49, v23\n"
    "s_add_u32 s49, s41, 3\n"
    "s_and_b32 s49, s49, 3\n"
    "s_mul_i32 s49, s49, 0x1400\n"
    "v_add_u32 v242, s49, v22\n"
    "v_add_u32 v246, s49, v23\n"
    "s_mul_i32 s49, s41, 0x500\n"
    "s_add_u32 s49, s49, 0x19000\n"
    "v_lshl_add_u32 v22, v251, 3, v16\n"
    "v_mul_u32_u24 v22, 40, v22\n"
    "v_lshl_add_u32 v22, v17, 3, v22\n"
    "v_add_u32 v74, s49, v22\n"
    "s_lshl_b32 s49, s41, 3\n"
    "s_add_u32 s49, s49, 0x27000\n"
    "v_lshl_add_u32 v22, v250, 5, s49\n"
    "v_cmp_eq_u32 vcc, v16, v251\n"
    "v_lshlrev_b32 v23, 3, v249\n"
    "v_add_u32 v23, 0x27200, v23\n"
    "v_cndmask_b32 v75, v23, v22, vcc\n"
    "s_cmp_lt_u32 s42, 4\n"
    "s_cselect_b64 vcc, -1, 0\n"
    "v_lshl_add_u32 v22, v251, 2, v17\n"
    "v_cndmask_b32 v22, v22, v250, vcc\n"
    "v_lshlrev_b32 v22, 5, v22\n"
    "v_add_u32 v76, 0x27000, v22\n"
    "v_mov_b32 v122, 0\n"
    "v_mov_b32 v123, 0\n"
    "v_mov_b32 v126, 0\n"
    "v_mov_b32 v127, 0\n"
    "v_mov_b32 v130, 0\n"
    "v_mov_b32 v131, 0\n"
    "v_mov_b32 v134, 0\n"
    "v_mov_b32 v135, 0\n"
    "v_mov_b32 v138, 0\n"
    "v_mov_b32 v139, 0\n"
    "v_mov_b32 v90, 0\n"
    "v_mov_b32 v91, 0\n"
    "v_add_u32 v22, 0, v249\n"
    "v_mul_u32_u24 v22, 0x199a, v22\n"
    "v_lshrrev_b32 v22, 16, v22\n"
    "v_and_b32 v23, 12, v22\n"
    "v_lshrrev_b32 v19, 4, v22\n"
    "v_mul_u32_u24 v19, 12, v19\n"
    "v_sub_u32 v23, v23, v19\n"
    "v_mul_u32_u24 v22, 0x460, v22\n"
    "v_mul_i32_i24 v23, 0x500, v23\n"
    "v_add3_u32 v97, v22, v23, v1\n"
    "v_add_u32 v22, 64, v249\n"
    "v_mul_u32_u24 v22, 0x199a, v22\n"
    "v_lshrrev_b32 v22, 16, v22\n"
    "v_and_b32 v23, 12, v22\n"
    "v_lshrrev_b32 v19, 4, v22\n"
    "v_mul_u32_u24 v19, 12, v19\n"
    "v_sub_u32 v23, v23, v19\n"
    "v_mul_u32_u24 v22, 0x460, v22\n"
    "v_mul_i32_i24 v23, 0x500, v23\n"
    "v_add3_u32 v98, v22, v23, v1\n"
    "v_add_u32 v22, 0x80, v249\n"
    "v_mul_u32_u24 v22, 0x199a, v22\n"
    "v_lshrrev_b32 v22, 16, v22\n"
    "v_and_b32 v23, 12, v22\n"
    "v_lshrrev_b32 v19, 4, v22\n"
    "v_mul_u32_u24 v19, 12, v19\n"
    "v_sub_u32 v23, v23, v19\n"
    "v_mul_u32_u24 v22, 0x460, v22\n"
    "v_mul_i32_i24 v23, 0x500, v23\n"
    "v_add3_u32 v99, v22, v23, v1\n"
    "v_add_u32 v22, 0xc0, v249\n"
    "v_mul_u32_u24 v22, 0x199a, v22\n"
    "v_lshrrev_b32 v22, 16, v22\n"
    "v_and_b32 v23, 12, v22\n"
    "v_lshrrev_b32 v19, 4, v22\n"
    "v_mul_u32_u24 v19, 12, v19\n"
    "v_sub_u32 v23, v23, v19\n"
    "v_mul_u32_u24 v22, 0x460, v22\n"
    "v_mul_i32_i24 v23, 0x500, v23\n"
    "v_add3_u32 v100, v22, v23, v1\n"
    "v_add_u32 v22, 0x100, v249\n"
    "v_mul_u32_u24 v22, 0x199a, v22\n"
    "v_lshrrev_b32 v22, 16, v22\n"
    "v_and_b32 v23, 12, v22\n"
    "v_lshrrev_b32 v19, 4, v22\n"
    "v_mul_u32_u24 v19, 12, v19\n"
    "v_sub_u32 v23, v23, v19\n"
    "v_mul_u32_u24 v22, 0x460, v22\n"
    "v_mul_i32_i24 v23, 0x500, v23\n"
    "v_add3_u32 v101, v22, v23, v1\n"
    "v_add_u32 v101, 0x1000, v101\n"
    "s_mov_b64 exec, 0\n"
    "s_cmp_eq_u32 s41, 0\n"
    "s_cbranch_scc1 Lhop1\n"
    "s_cmp_eq_u32 s41, 1\n"
    "s_cbranch_scc1 Lhop2\n"
    "s_cmp_eq_u32 s41, 2\n"
    "s_cbranch_scc1 Lhop3\n"
    "s_branch Lhop4\n"
    "LhopA:\n"
    "s_mov_b64 exec, -1\n"
    "s_waitcnt lgkmcnt(0)\n"
    "s_mov_b32 s22, s58\n"
    "v_cmp_gt_u32 vcc, s53, v249\n"
    "v_mov_b32 v23, 0x40000000\n"
    "v_lshrrev_b32 v22, 5, v249\n"
    "v_cndmask_b32 v79, v23, v247, vcc\n"
    "v_mul_u32_u24 v22, s56, v22\n"
    "v_bfe_u32 v23, v249, 3, 2\n"
    "v_add3_u32 v22, v22, v23, s55\n"
    "v_mul_u32_u24 v22, s57, v22\n"
    "v_and_b32 v23, 7, v249\n"
    "v_lshl_add_u32 v232, v23, 4, v22\n"
    "v_lshl_add_u32 v22, v16, 3, v17\n"
    "s_lshl_b32 s49, s95, 28\n"
    "s_and_b32 s49, s49, 0x40000000\n"
    "v_lshl_or_b32 v236, v22, 1, s49\n"
    "s_add_u32 s49, s41, 0\n"
    "s_and_b32 s49, s49, 3\n"
    "s_lshl_b32 s49, s49, 6\n"
    "v_xor_b32 v64, s49, v20\n"
    "v_xor_b32 v68, s49, v21\n"
    "v_add_u32 v64, s59, v64\n"
    "v_add_u32 v68, s59, v68\n"
    "s_add_u32 s49, s41, 1\n"
    "s_and_b32 s49, s49, 3\n"
    "s_lshl_b32 s49, s49, 6\n"
    "v_xor_b32 v65, s49, v20\n"
    "v_xor_b32 v69, s49, v21\n"
    "v_add_u32 v65, s59, v65\n"
    "v_add_u32 v69, s59, v69\n"
    "s_add_u32 s49, s41, 2\n"
    "s_and_b32 s49, s49, 3\n"
    "s_lshl_b32 s49, s49, 6\n"
    "v_xor_b32 v66, s49, v20\n"
    "v_xor_b32 v70, s49, v21\n"
    "v_add_u32 v66, s59, v66\n"
    "v_add_u32 v70, s59, v70\n"
    "s_add_u32 s49, s41, 3\n"
    "s_and_b32 s49, s49, 3\n"
    "s_lshl_b32 s49, s49, 6\n"
    "v_xor_b32 v67, s49, v20\n"
    "v_xor_b32 v71, s49, v21\n"
    "v_add_u32 v67, s59, v67\n"
    "v_add_u32 v71, s59, v71\n"
    "s_add_u32 s51, s38, s23\n"
    "s_add_u32 m0, s51, 0x19000\n"
    "s_nop 0\n"
    "buffer_load_dwordx4 v1, s[16:19], s51 offen  lds\n"
    "s_add_u32 s51, s39, s23\n"
    "s_add_u32 m0, s51, 0x19000\n"
    "s_nop 0\n"
    "buffer_load_dwordx4 v1, s[16:19], s51 offen  lds\n"
    "s_add_u32 s51, s34, s23\n"
    "s_add_u32 m0, s51, 0x19000\n"
    "s_nop 0\n"
    "buffer_load_dwordx4 v1, s[16:19], s51 offen  lds\n"
    "v_mov_b32 v102, 0\n"
    "buffer_load_dwordx2 v[102:103], v102, s[24:27], 0 offen offset:0 sc1\n"
    "v_lshrrev_b32 v22, 1, v251\n"
    "v_xor_b32 v22, v22, v250\n"
    "v_lshlrev_b32 v22, 4, v22\n"
    "v_and_b32 v23, 1, v251\n"
    "v_lshl_add_u32 v22, v23, 3, v22\n"
    "v_mul_u32_u24 v23, 0x1400, v16\n"
    "s_bfe_u32 s49, s74, 0x80000\n"
    "s_and_b32 s55, s49, 7\n"
    "s_lshl_b32 s55, s55, 5\n"
    "v_xor_b32 v80, s55, v18\n"
    "s_lshr_b32 s49, s49, 3\n"
    "s_lshl_b32 s55, s49, 8\n"
    "s_add_u32 s55, s55, s59\n"
    "v_add3_u32 v80, v80, v23, s55\n"
    "s_lshr_b32 s49, s49, 3\n"
    "s_sub_u32 s49, 10, s49\n"
    "v_lshl_add_u32 v80, v17, s49, v80\n"
    "s_bfe_u32 s49, s74, 0x8000a\n"
    "s_and_b32 s55, s49, 7\n"
    "s_lshl_b32 s55, s55, 5\n"
    "v_xor_b32 v81, s55, v18\n"
    "s_lshr_b32 s49, s49, 3\n"
    "s_lshl_b32 s55, s49, 8\n"
    "s_add_u32 s55, s55, s59\n"
    "v_add3_u32 v81, v81, v23, s55\n"
    "s_lshr_b32 s49, s49, 3\n"
    "s_sub_u32 s49, 10, s49\n"
    "v_lshl_add_u32 v81, v17, s49, v81\n"
    "s_bfe_u32 s49, s74, 0x80014\n"
    "s_and_b32 s55, s49, 7\n"
    "s_lshl_b32 s55, s55, 5\n"
    "v_xor_b32 v82, s55, v22\n"
    "s_lshr_b32 s49, s49, 3\n"
    "s_lshl_b32 s55, s49, 8\n"
    "s_add_u32 s55, s55, s59\n"
    "v_add3_u32 v82, v82, v23, s55\n"
    "s_lshr_b32 s49, s49, 3\n"
    "s_sub_u32 s49, 10, s49\n"
    "v_lshl_add_u32 v82, v17, s49, v82\n"
    "s_lshl_b32 s49, s95, 30\n"
    "s_and_b32 s49, s49, 0x40000000\n"
    "v_or_b32 v9, s49, v1\n"
    "s_lshl_b32 s49, s95, 29\n"
    "s_and_b32 s49, s49, 0x40000000\n"
    "v_or_b32 v10, s49, v1\n"
    "s_lshl_b32 s49, s95, 27\n"
    "s_and_b32 s49, s49, 0x40000000\n"
    "v_or_b32 v15, s49, v8\n"
    "s_lshl_b32 s49, s75, 10\n"
    "s_add_u32 s49, s49, 0x1d000\n"
    "v_add_u32 v237, s49, v1\n"
    "s_lshl_b32 s49, s75, 8\n"
    "v_lshlrev_b32 v22, 3, v249\n"
    "v_xor_b32 v22, s49, v22\n"
    "v_add_u32 v238, 0x1c800, v22\n"
    "v_lshl_add_u32 v22, v251, 3, v16\n"
    "v_lshlrev_b32 v22, 7, v22\n"
    "s_lshl_b32 s49, s41, 5\n"
    "s_add_u32 s49, s49, 0x1f000\n"
    "v_lshl_add_u32 v23, v17, 3, v22\n"
    "v_add_u32 v23, s49, v23\n"
    "s_and_b32 s56, s41, 1\n"
    "s_mul_i32 s56, s56, 24\n"
    "s_sub_u32 s49, s49, s56\n"
    "v_lshl_add_u32 v22, v17, 4, v22\n"
    "v_add_u32 v233, s49, v22\n"
    "s_bfe_i32 s16, s95, 0x10004\n"
    "s_mov_b32 s17, s16\n"
    "v_cndmask_b32 v234, v233, v23, s[16:17]\n"
    "s_bfe_i32 s16, s95, 0x10005\n"
    "s_mov_b32 s17, s16\n"
    "v_cndmask_b32 v235, v233, v23, s[16:17]\n"
    "s_lshr_b32 s49, s41, 1\n"
    "s_mul_i32 s49, s49, 0x200\n"
    "s_lshl_b32 s56, s41, 8\n"
    "s_add_u32 s49, s49, s56\n"
    "s_add_u32 s49, s49, 0x1d000\n"
    "v_add_u32 v247, s49, v247\n"
    "s_lshl_b32 s49, s41, 7\n"
    "s_add_u32 s56, s49, 0x1c800\n"
    "s_waitcnt vmcnt(14)\n"
    "v_lshlrev_b32 v248, 9, v251\n"
    "v_lshl_add_u32 v248, v250, 3, v248\n"
    "v_add_u32 v248, s56, v248\n"
    "s_add_u32 s49, s49, 0x20000\n"
    "v_lshl_add_u32 v110, v250, 3, s49\n"
    "v_lshlrev_b32 v111, 3, v249\n"
    "v_add_u32 v111, 0x20000, v111\n"
    "s_add_u32 m0, s45, 0xa000\n"
    "s_add_u32 s46, s42, 1\n"
    "s_and_b32 s46, s46, 3\n"
    "s_add_u32 s46, s46, s48\n"
    "s_mul_i32 s46, s46, 0x5000\n"
    "s_add_u32 s46, s46, s45\n"
    "buffer_load_dwordx4 v92, s[8:11], s46 offen offset:0  lds\n"
    "buffer_load_dwordx4 v93, s[8:11], s46 offen offset:1024  lds\n"
    "buffer_load_dwordx4 v94, s[8:11], s46 offen offset:2048  lds\n"
    "buffer_load_dwordx4 v95, s[8:11], s46 offen offset:3072  lds\n"
    "s_add_u32 m0, s45, 0xb000\n"
    "s_nop 0\n"
    "buffer_load_dwordx4 v96, s[8:11], s46 offen offset:0  lds\n"
    "s_waitcnt vmcnt(6)\n"
    "s_barrier\n"
    "ds_read_b128 v[172:175], v72 offset:0\n"
    "ds_read_b128 v[176:179], v72 offset:256\n"
    "ds_read_b128 v[180:183], v72 offset:512\n"
    "ds_read_b128 v[184:187], v72 offset:768\n"
    "ds_read_b128 v[188:191], v73 offset:0\n"
    "s_add_u32 m0, s45, 0xf000\n"
    "s_add_u32 s46, s42, 2\n"
    "s_and_b32 s46, s46, 3\n"
    "s_add_u32 s46, s46, s48\n"
    "s_mul_i32 s46, s46, 0x5000\n"
    "s_add_u32 s46, s46, s45\n"
    "buffer_load_dwordx4 v92, s[8:11], s46 offen offset:0  lds\n"
    "buffer_load_dwordx4 v93, s[8:11], s46 offen offset:1024  lds\n"
    "s_waitcnt lgkmcnt(4)\n"
    "v_mfma_f32_16x16x32_bf16 v[84:87], v[252:255], v[172:175], 0\n"
    "ds_read_b64_tr_b16 v[24:25], v74 offset:0\n"
    "ds_read_b64_tr_b16 v[26:27], v74 offset:160\n"
    "v_mfma_f32_16x16x32_bf16 v[168:171], v[172:175], v[172:175], 0\n"
    "ds_read_b64_tr_b16 v[28:29], v74 offset:5120\n"
    "ds_read_b64_tr_b16 v[30:31], v74 offset:5280\n"
    "s_waitcnt lgkmcnt(7)\n"
    "v_mfma_f32_16x16x32_bf16 v[84:87], v[252:255], v[176:179], v[84:87]\n"
    "ds_read_b64_tr_b16 v[32:33], v74 offset:10240\n"
    "ds_read_b64_tr_b16 v[34:35], v74 offset:10400\n"
    "buffer_load_dwordx4 v94, s[8:11], s46 offen offset:2048  lds\n"
    "v_mfma_f32_16x16x32_bf16 v[168:171], v[176:179], v[176:179], v[168:171]\n"
    "ds_read_b64_tr_b16 v[36:37], v74 offset:15360\n"
    "ds_read_b64_tr_b16 v[38:39], v74 offset:15520\n"
    "s_waitcnt lgkmcnt(10)\n"
    "v_mfma_f32_16x16x32_bf16 v[84:87], v[252:255], v[180:183], v[84:87]\n"
    "ds_read_b64_tr_b16 v[40:41], v74 offset:20480\n"
    "ds_read_b64_tr_b16 v[42:43], v74 offset:20640\n"
    "v_mfma_f32_16x16x32_bf16 v[168:171], v[180:183], v[180:183], v[168:171]\n"
    "ds_read_b64_tr_b16 v[44:45], v74 offset:32\n"
    "ds_read_b64_tr_b16 v[46:47], v74 offset:192\n"
    "buffer_load_dwordx4 v95, s[8:11], s46 offen offset:3072  lds\n"
    "s_add_u32 m0, s45, 0x10000\n"
    "s_waitcnt lgkmcnt(13)\n"
    "v_mfma_f32_16x16x32_bf16 v[84:87], v[252:255], v[184:187], v[84:87]\n"
    "ds_read_b64_tr_b16 v[48:49], v74 offset:5152\n"
    "ds_read_b64_tr_b16 v[50:51], v74 offset:5312\n"
    "v_mfma_f32_16x16x32_bf16 v[168:171], v[184:187], v[184:187], v[168:171]\n"
    "s_waitcnt lgkmcnt(14)\n"
    "ds_read_b64_tr_b16 v[52:53], v74 offset:10272\n"
    "s_waitcnt lgkmcnt(14)\n"
    "ds_read_b64_tr_b16 v[54:55], v74 offset:10432\n"
    "v_mfma_f32_16x16x32_bf16 v[84:87], v[252:255], v[188:191], v[84:87]\n"
    "s_waitcnt lgkmcnt(14)\n"
    "ds_read_b64_tr_b16 v[56:57], v74 offset:15392\n"
    "s_waitcnt lgkmcnt(14)\n"
    "ds_read_b64_tr_b16 v[58:59], v74 offset:15552\n"
    "buffer_load_dwordx4 v96, s[8:11], s46 offen offset:0  lds\n"
    "v_mfma_f32_16x16x32_bf16 v[168:171], v[188:191], v[188:191], v[168:171]\n"
    "s_waitcnt lgkmcnt(14)\n"
    "ds_read_b64_tr_b16 v[60:61], v74 offset:20512\n"
    "s_waitcnt lgkmcnt(14)\n"
    "ds_read_b64_tr_b16 v[62:63], v74 offset:20672\n"
    "s_cmp_lt_u32 s42, 4\n"
    "s_cbranch_scc0 Lvrole1\n"
    "v_mfma_f32_16x16x32_bf16 v[192:195], v[24:27], v[172:175], 0\n"
    "s_add_u32 m0, s45, 0x14000\n"
    "s_add_u32 s46, s42, 3\n"
    "s_and_b32 s46, s46, 3\n"
    "s_add_u32 s46, s46, s48\n"
    "s_mul_i32 s46, s46, 0x5000\n"
    "s_add_u32 s46, s46, s45\n"
    "buffer_load_dwordx4 v92, s[8:11], s46 offen offset:0  lds\n"
    "v_mfma_f32_16x16x32_bf16 v[196:199], v[24:27], v[252:255], 0\n"
    "v_mfma_f32_16x16x32_bf16 v[192:195], v[28:31], v[176:179], v[192:195]\n"
    "s_mov_b32 s16, 0x22222222\n"
    "s_mov_b32 s17, s16\n"
    "v_cndmask_b32 v168, v168, v169, s[16:17]\n"
    "s_mov_b32 s16, 0x44444444\n"
    "v_mfma_f32_16x16x32_bf16 v[196:199], v[28:31], v[252:255], v[196:199]\n"
    "buffer_load_dwordx4 v93, s[8:11], s46 offen offset:1024  lds\n"
    "s_waitcnt lgkmcnt(14)\n"
    "v_mfma_f32_16x16x32_bf16 v[192:195], v[32:35], v[180:183], v[192:195]\n"
    "s_mov_b32 s17, s16\n"
    "v_cndmask_b32 v168, v168, v170, s[16:17]\n"
    "s_mov_b32 s16, 0x88888888\n"
    "v_mfma_f32_16x16x32_bf16 v[196:199], v[32:35], v[252:255], v[196:199]\n"
    "s_mov_b32 s17, s16\n"
    "v_cndmask_b32 v85, v168, v171, s[16:17]\n"
    "s_waitcnt lgkmcnt(12)\n"
    "v_mfma_f32_16x16x32_bf16 v[192:195], v[36:39], v[184:187], v[192:195]\n"
    "buffer_load_dwordx4 v94, s[8:11], s46 offen offset:2048  lds\n"
    "v_mfma_f32_16x16x32_bf16 v[196:199], v[36:39], v[252:255], v[196:199]\n"
    "ds_write_b64 v75, v[84:85]\n"
    "s_waitcnt lgkmcnt(1)\n"
    "v_mfma_f32_16x16x32_bf16 v[192:195], v[40:43], v[188:191], v[192:195]\n"
    "s_barrier\n"
    "s_waitcnt vmcnt(13)\n"
    "v_readfirstlane_b32 s6, v102\n"
    "v_readfirstlane_b32 s7, v103\n"
    "s_add_u32 s6, s6, 1\n"
    "s_addc_u32 s7, s7, 0\n"
    "v_mfma_f32_16x16x32_bf16 v[196:199], v[40:43], v[252:255], v[196:199]\n"
    "buffer_load_dwordx4 v95, s[8:11], s46 offen offset:3072  lds\n"
    "s_add_u32 m0, s45, 0x15000\n"
    "v_mfma_f32_16x16x32_bf16 v[200:203], v[44:47], v[172:175], 0\n"
    "s_lshl_b32 s49, s41, 3\n"
    "s_lshl_b32 s57, s41, 29\n"
    "s_and_b32 s57, s57, 0x40000000\n"
    "v_mfma_f32_16x16x32_bf16 v[204:207], v[44:47], v[252:255], 0\n"
    "s_or_b32 s49, s49, s57\n"
    "v_or_b32 v102, s49, v14\n"
    "s_min_u32 s49, s41, 1\n"
    "v_mfma_f32_16x16x32_bf16 v[200:203], v[48:51], v[176:179], v[200:203]\n"
    "buffer_load_dwordx4 v96, s[8:11], s46 offen offset:0  lds\n"
    "v_mfma_f32_16x16x32_bf16 v[204:207], v[48:51], v[252:255], v[204:207]\n"
    "s_lshl_b32 s49, s49, 30\n"
    "v_or_b32 v14, s49, v14\n"
    "s_mov_b32 s74, 0\n"
    "v_mfma_f32_16x16x32_bf16 v[200:203], v[52:55], v[180:183], v[200:203]\n"
    "v_mov_b32 v113, s6\n"
    "v_mov_b32 v115, s6\n"
    "s_lshl_b32 s49, s95, 24\n"
    "v_mfma_f32_16x16x32_bf16 v[204:207], v[52:55], v[252:255], v[204:207]\n"
    "s_add_u32 m0, s45, 0\n"
    "s_add_u32 s51, s47, 0\n"
    "buffer_load_dwordx4 v97, s[12:15], s51 offen offset:0 lds\n"
    "v_mfma_f32_16x16x32_bf16 v[200:203], v[56:59], v[184:187], v[200:203]\n"
    "s_and_b32 s49, s49, 0x40000000\n"
    "v_or_b32 v116, s49, v8\n"
    "v_and_b32 v117, 0xff, v1\n"
    "s_sub_u32 s55, s67, 0x60000\n"
    "v_mfma_f32_16x16x32_bf16 v[204:207], v[56:59], v[252:255], v[204:207]\n"
    "s_lshr_b32 s55, s55, 2\n"
    "s_add_u32 s55, s55, 0x58000\n"
    "s_lshl_b32 s56, s42, 8\n"
    "v_mfma_f32_16x16x32_bf16 v[200:203], v[60:63], v[188:191], v[200:203]\n"
    "buffer_load_dwordx4 v98, s[12:15], s51 offen offset:1024 lds\n"
    "v_mfma_f32_16x16x32_bf16 v[204:207], v[60:63], v[252:255], v[204:207]\n"
    "s_add_u32 s56, s56, s55\n"
    "ds_write_b128 v77, v[192:195] offset:0\n"
    "ds_write_b128 v77, v[196:199] offset:1024\n"
    "s_branch Lkvjoin2\n"
    "Lvrole1:\n"
    "v_mfma_f32_16x16x32_bf16 v[192:195], v[172:175], v[24:27], 0\n"
    "s_add_u32 m0, s45, 0x14000\n"
    "s_add_u32 s46, s42, 3\n"
    "s_and_b32 s46, s46, 3\n"
    "s_add_u32 s46, s46, s48\n"
    "s_mul_i32 s46, s46, 0x5000\n"
    "s_add_u32 s46, s46, s45\n"
    "buffer_load_dwordx4 v92, s[8:11], s46 offen offset:0  lds\n"
    "v_mfma_f32_16x16x32_bf16 v[196:199], v[252:255], v[24:27], 0\n"
    "v_mfma_f32_16x16x32_bf16 v[192:195], v[176:179], v[28:31], v[192:195]\n"
    "s_mov_b32 s16, 0x22222222\n"
    "s_mov_b32 s17, s16\n"
    "v_cndmask_b32 v168, v168, v169, s[16:17]\n"
    "s_mov_b32 s16, 0x44444444\n"
    "v_mfma_f32_16x16x32_bf16 v[196:199], v[252:255], v[28:31], v[196:199]\n"
    "buffer_load_dwordx4 v93, s[8:11], s46 offen offset:1024  lds\n"
    "s_waitcnt lgkmcnt(14)\n"
    "v_mfma_f32_16x16x32_bf16 v[192:195], v[180:183], v[32:35], v[192:195]\n"
    "s_mov_b32 s17, s16\n"
    "v_cndmask_b32 v168, v168, v170, s[16:17]\n"
    "s_mov_b32 s16, 0x88888888\n"
    "v_mfma_f32_16x16x32_bf16 v[196:199], v[252:255], v[32:35], v[196:199]\n"
    "s_mov_b32 s17, s16\n"
    "v_cndmask_b32 v85, v168, v171, s[16:17]\n"
    "s_waitcnt lgkmcnt(12)\n"
    "v_mfma_f32_16x16x32_bf16 v[192:195], v[184:187], v[36:39], v[192:195]\n"
    "buffer_load_dwordx4 v94, s[8:11], s46 offen offset:2048  lds\n"
    "v_mfma_f32_16x16x32_bf16 v[196:199], v[252:255], v[36:39], v[196:199]\n"
    "ds_write_b64 v75, v[84:85]\n"
    "s_waitcnt lgkmcnt(1)\n"
    "v_mfma_f32_16x16x32_bf16 v[192:195], v[188:191], v[40:43], v[192:195]\n"
    "s_barrier\n"
    "s_waitcnt vmcnt(13)\n"
    "v_readfirstlane_b32 s6, v102\n"
    "v_readfirstlane_b32 s7, v103\n"
    "s_add_u32 s6, s6, 1\n"
    "s_addc_u32 s7, s7, 0\n"
    "v_mfma_f32_16x16x32_bf16 v[196:199], v[252:255], v[40:43], v[196:199]\n"
    "buffer_load_dwordx4 v95, s[8:11], s46 offen offset:3072  lds\n"
    "s_add_u32 m0, s45, 0x15000\n"
    "v_mfma_f32_16x16x32_bf16 v[200:203], v[172:175], v[44:47], 0\n"
    "s_lshl_b32 s49, s41, 3\n"
    "s_lshl_b32 s57, s41, 29\n"
    "s_and_b32 s57, s57, 0x40000000\n"
    "v_mfma_f32_16x16x32_bf16 v[204:207], v[252:255], v[44:47], 0\n"
    "s_or_b32 s49, s49, s57\n"
    "v_or_b32 v102, s49, v14\n"
    "s_min_u32 s49, s41, 1\n"
    "v_mfma_f32_16x16x32_bf16 v[200:203], v[176:179], v[48:51], v[200:203]\n"
    "buffer_load_dwordx4 v96, s[8:11], s46 offen offset:0  lds\n"
    "v_mfma_f32_16x16x32_bf16 v[204:207], v[252:255], v[48:51], v[204:207]\n"
    "s_lshl_b32 s49, s49, 30\n"
    "v_or_b32 v14, s49, v14\n"
    "s_mov_b32 s74, 0\n"
    "v_mfma_f32_16x16x32_bf16 v[200:203], v[180:183], v[52:55], v[200:203]\n"
    "v_mov_b32 v113, s6\n"
    "v_mov_b32 v115, s6\n"
    "s_lshl_b32 s49, s95, 24\n"
    "v_mfma_f32_16x16x32_bf16 v[204:207], v[252:255], v[52:55], v[204:207]\n"
    "s_add_u32 m0, s45, 0\n"
    "s_add_u32 s51, s47, 0\n"
    "buffer_load_dwordx4 v97, s[12:15], s51 offen offset:0 lds\n"
    "v_mfma_f32_16x16x32_bf16 v[200:203], v[184:187], v[56:59], v[200:203]\n"
    "s_and_b32 s49, s49, 0x40000000\n"
    "v_or_b32 v116, s49, v8\n"
    "v_and_b32 v117, 0xff, v1\n"
    "s_sub_u32 s55, s67, 0x60000\n"
    "v_mfma_f32_16x16x32_bf16 v[204:207], v[252:255], v[56:59], v[204:207]\n"
    "s_lshr_b32 s55, s55, 2\n"
    "s_add_u32 s55, s55, 0x58000\n"
    "s_lshl_b32 s56, s42, 8\n"
    "v_mfma_f32_16x16x32_bf16 v[200:203], v[188:191], v[60:63], v[200:203]\n"
    "buffer_load_dwordx4 v98, s[12:15], s51 offen offset:1024 lds\n"
    "v_mfma_f32_16x16x32_bf16 v[204:207], v[252:255], v[60:63], v[204:207]\n"
    "s_add_u32 s56, s56, s55\n"
    "ds_write_b128 v77, v[192:195] offset:0\n"
    "ds_write_b128 v77, v[196:199] offset:1024\n"
    "Lkvjoin2:\n"
    "buffer_load_dwordx4 v99, s[12:15], s51 offen offset:2048 lds\n"
    "buffer_load_dwordx4 v100, s[12:15], s51 offen offset:3072 lds\n"
    "s_add_u32 m0, s45, 0x1000\n"
    "ds_write_b128 v77, v[200:203] offset:2048\n"
    "s_nop 0\n"
    "ds_write_b128 v77, v[204:207] offset:3072\n"
    "buffer_load_dwordx4 v101, s[12:15], s51 offen offset:0 lds\n"
    "s_waitcnt vmcnt(5)\n"
    "s_waitcnt lgkmcnt(0)\n"
    "s_barrier\n"
    "ds_read_b128 v[56:59], v76\n"
    "ds_read_b128 v[60:63], v76 offset:16\n"
    "ds_read_b128 v[24:27], v78 offset:0\n"
    "ds_read_b128 v[28:31], v78 offset:1024\n"
    "ds_read_b128 v[32:35], v78 offset:4096\n"
    "ds_read_b128 v[36:39], v78 offset:5120\n"
    "ds_read_b128 v[40:43], v78 offset:8192\n"
    "ds_read_b128 v[44:47], v78 offset:9216\n"
    "ds_read_b128 v[48:51], v78 offset:12288\n"
    "ds_read_b128 v[52:55], v78 offset:13312\n"
    "ds_read_b128 a[144:147], v64 offset:0\n"
    "ds_read_b128 a[148:151], v65 offset:0\n"
    "ds_read_b128 a[152:155], v66 offset:0\n"
    "ds_read_b128 a[156:159], v67 offset:0\n"
    "ds_read_b128 a[160:163], v64 offset:256\n"
    "Lhop1:\n"
    "s_cbranch_execz Lhop5\n"
    "s_add_u32 m0, s45, 0x5000\n"
    "s_add_u32 s51, s47, 0x28000\n"
    "buffer_load_dwordx4 v97, s[12:15], s51 offen offset:0 lds\n"
    "buffer_load_dwordx4 v98, s[12:15], s51 offen offset:1024 lds\n"
    "s_waitcnt lgkmcnt(14)\n"
    "ds_read_b128 a[164:167], v65 offset:256\n"
    "s_waitcnt lgkmcnt(14)\n"
    "ds_read_b128 a[168:171], v66 offset:256\n"
    "s_waitcnt lgkmcnt(14)\n"
    "ds_read_b128 a[172:175], v67 offset:256\n"
    "s_waitcnt lgkmcnt(14)\n"
    "ds_read_b128 a[176:179], v64 offset:512\n"
    "s_waitcnt lgkmcnt(14)\n"
    "ds_read_b128 a[180:183], v65 offset:512\n"
    "s_waitcnt lgkmcnt(14)\n"
    "ds_read_b128 a[184:187], v66 offset:512\n"
    "s_waitcnt lgkmcnt(14)\n"
    "ds_read_b128 a[188:191], v67 offset:512\n"
    "s_waitcnt lgkmcnt(14)\n"
    "ds_read_b128 a[192:195], v64 offset:768\n"
    "s_waitcnt lgkmcnt(14)\n"
    "ds_read_b128 a[196:199], v65 offset:768\n"
    "buffer_load_dwordx4 v99, s[12:15], s51 offen offset:2048 lds\n"
    "buffer_load_dwordx4 v100, s[12:15], s51 offen offset:3072 lds\n"
    "s_add_u32 m0, s45, 0x6000\n"
    "s_waitcnt lgkmcnt(14)\n"
    "ds_read_b128 a[200:203], v66 offset:768\n"
    "s_waitcnt lgkmcnt(14)\n"
    "ds_read_b128 a[204:207], v67 offset:768\n"
    "s_waitcnt lgkmcnt(14)\n"
    "ds_read_b128 a[208:211], v68 offset:0\n"
    "s_waitcnt lgkmcnt(14)\n"
    "ds_read_b128 a[212:215], v69 offset:0\n"
    "s_waitcnt lgkmcnt(14)\n"
    "ds_read_b128 a[216:219], v70 offset:0\n"
    "s_waitcnt lgkmcnt(14)\n"
    "ds_read_b128 a[220:223], v71 offset:0\n"
    "s_waitcnt lgkmcnt(14)\n"
    "ds_read_b128 v[212:215], v80\n"
    "s_waitcnt lgkmcnt(14)\n"
    "ds_read_b128 v[216:219], v81\n"
    "s_waitcnt lgkmcnt(14)\n"
    "ds_read_b64 v[220:221], v82\n"
    "buffer_load_dwordx4 v101, s[12:15], s51 offen offset:0 lds\n"
    "v_add_f32 v56, v56, v60\n"
    "v_add_f32 v57, v57, v61\n"
    "v_add_f32 v58, v58, v62\n"
    "v_add_f32 v59, v59, v63\n"
    "v_add_f32 v56, v56, v58\n"
    "v_add_f32 v57, v57, v59\n"
    "v_mul_f32 v56, 0xbacccccd, v56\n"
    "v_mul_f32 v57, 0x3acccccd, v57\n"
    "v_fma_f32 v57, -v56, v56, v57\n"
    "v_max_f32 v57, 0, v57\n"
    "v_add_f32 v57, 0x3727c5ac, v57\n"
    "v_rsq_f32 v57, v57\n"
    "v_mov_b32 v112, v56\n"
    "v_mov_b32 v114, v57\n"
    "buffer_store_dwordx4 v[112:115], v116, s[24:27], s56 offen offset:0 sc1\n"
    "v_pk_add_f32 v[24:25], v[24:25], v[32:33]\n"
    "v_pk_add_f32 v[26:27], v[26:27], v[34:35]\n"
    "v_pk_add_f32 v[28:29], v[28:29], v[36:37]\n"
    "v_pk_add_f32 v[30:31], v[30:31], v[38:39]\n"
    "v_pk_add_f32 v[40:41], v[40:41], v[48:49]\n"
    "v_pk_add_f32 v[42:43], v[42:43], v[50:51]\n"
    "v_pk_add_f32 v[44:45], v[44:45], v[52:53]\n"
    "v_pk_add_f32 v[46:47], v[46:47], v[54:55]\n"
    "v_pk_add_f32 v[24:25], v[24:25], v[40:41]\n"
    "v_pk_add_f32 v[26:27], v[26:27], v[42:43]\n"
    "v_pk_add_f32 v[28:29], v[28:29], v[44:45]\n"
    "v_pk_add_f32 v[30:31], v[30:31], v[46:47]\n"
    "s_waitcnt lgkmcnt(0)\n"
    "s_barrier\n"
    "s_add_u32 m0, s45, 0xa000\n"
    "s_add_u32 s51, s47, 0x50000\n"
    "buffer_load_dwordx4 v97, s[12:15], s51 offen offset:0 lds\n"
    "s_cmp_lt_u32 s42, 4\n"
    "s_cbranch_scc1 Lfxk3\n"
    "v_fmac_f32_dpp v24, v56, v28 quad_perm:[0,0,0,0] row_mask:0xf bank_mask:0xf\n"
    "v_fmac_f32_dpp v25, v56, v29 quad_perm:[1,1,1,1] row_mask:0xf bank_mask:0xf\n"
    "v_fmac_f32_dpp v26, v56, v30 quad_perm:[2,2,2,2] row_mask:0xf bank_mask:0xf\n"
    "v_fmac_f32_dpp v27, v56, v31 quad_perm:[3,3,3,3] row_mask:0xf bank_mask:0xf\n"
    "v_mul_f32_dpp v24, v57, v24 quad_perm:[0,0,0,0] row_mask:0xf bank_mask:0xf\n"
    "v_mul_f32_dpp v25, v57, v25 quad_perm:[1,1,1,1] row_mask:0xf bank_mask:0xf\n"
    "v_mul_f32_dpp v26, v57, v26 quad_perm:[2,2,2,2] row_mask:0xf bank_mask:0xf\n"
    "v_mul_f32_dpp v27, v57, v27 quad_perm:[3,3,3,3] row_mask:0xf bank_mask:0xf\n"
    "s_branch Lfxo4\n"
    "Lfxk3:\n"
    "v_fmac_f32 v24, v56, v28\n"
    "v_fmac_f32 v25, v56, v29\n"
    "v_fmac_f32 v26, v56, v30\n"
    "v_fmac_f32 v27, v56, v31\n"
    "v_mul_f32 v24, v57, v24\n"
    "v_mul_f32 v25, v57, v25\n"
    "v_mul_f32 v26, v57, v26\n"
    "v_mul_f32 v27, v57, v27\n"
    "Lfxo4:\n"
    "v_cmp_eq_u32 vcc, s54, v250\n"
    "v_cmp_lt_u32 s[16:17], s54, v250\n"
    "v_cvt_pk_bf16_f32 v24, v24, v25\n"
    "v_cvt_pk_bf16_f32 v25, v26, v27\n"
    "v_cndmask_b32 v24, v24, v252, vcc\n"
    "v_cndmask_b32 v24, v24, 0, s[16:17]\n"
    "v_cndmask_b32 v25, v25, v252, vcc\n"
    "v_cndmask_b32 v25, v25, 0, s[16:17]\n"
    "buffer_store_dwordx2 v[24:25], v79, s[24:27], s52 offen offset:0 sc1\n"
    "buffer_load_dwordx4 v98, s[12:15], s51 offen offset:1024 lds\n"
    "buffer_load_dwordx4 v99, s[12:15], s51 offen offset:2048 lds\n"
    "buffer_load_dwordx4 v100, s[12:15], s51 offen offset:3072 lds\n"
    "s_add_u32 m0, s45, 0xb000\n"
    "s_waitcnt vmcnt(11)\n"
    "s_barrier\n"
    "ds_read_b64_tr_b16 v[24:25], v239 offset:0\n"
    "ds_read_b64_tr_b16 v[26:27], v239 offset:2560\n"
    "ds_read_b64_tr_b16 v[28:29], v239 offset:40\n"
    "ds_read_b64_tr_b16 v[30:31], v239 offset:2600\n"
    "ds_read_b64_tr_b16 v[32:33], v239 offset:80\n"
    "ds_read_b64_tr_b16 v[34:35], v239 offset:2640\n"
    "ds_read_b64_tr_b16 v[36:37], v239 offset:120\n"
    "ds_read_b64_tr_b16 v[38:39], v239 offset:2680\n"
    "ds_read_b64_tr_b16 v[40:41], v243 offset:0\n"
    "ds_read_b64_tr_b16 v[42:43], v243 offset:2560\n"
    "ds_read_b64_tr_b16 v[44:45], v240 offset:0\n"
    "ds_read_b64_tr_b16 v[46:47], v240 offset:2560\n"
    "ds_read_b64_tr_b16 v[48:49], v240 offset:40\n"
    "ds_read_b64_tr_b16 v[50:51], v240 offset:2600\n"
    "s_waitcnt lgkmcnt(10)\n"
    "v_mfma_f32_16x16x32_bf16 v[64:67], v[24:27], a[144:147], 0\n"
    "ds_read_b64_tr_b16 v[52:53], v240 offset:80\n"
    "ds_read_b64_tr_b16 v[54:55], v240 offset:2640\n"
    "v_mfma_f32_16x16x32_bf16 v[192:195], v[252:255], v[24:27], 0\n"
    "buffer_load_dwordx4 v101, s[12:15], s51 offen offset:0 lds\n"
    "v_mfma_f32_16x16x32_bf16 v[68:71], v[28:31], a[144:147], 0\n"
    "s_waitcnt lgkmcnt(8)\n"
    "ds_read_b64_tr_b16 v[56:57], v240 offset:120\n"
    "ds_read_b64_tr_b16 v[58:59], v240 offset:2680\n"
    "v_mfma_f32_16x16x32_bf16 v[72:75], v[32:35], a[144:147], 0\n"
    "s_add_u32 m0, s45, 0xf000\n"
    "ds_read_b64_tr_b16 v[60:61], v244 offset:0\n"
    "ds_read_b64_tr_b16 v[62:63], v244 offset:2560\n"
    "v_mfma_f32_16x16x32_bf16 v[76:79], v[36:39], a[144:147], 0\n"
    "s_waitcnt lgkmcnt(8)\n"
    "ds_read_b64_tr_b16 v[24:25], v241 offset:0\n"
    "ds_read_b64_tr_b16 v[26:27], v241 offset:2560\n"
    "v_mfma_f32_16x16x32_bf16 v[80:83], v[40:43], a[144:147], 0\n"
    "s_add_u32 s51, s47, 0x78000\n"
    "ds_read_b64_tr_b16 v[28:29], v241 offset:40\n"
    "ds_read_b64_tr_b16 v[30:31], v241 offset:2600\n"
    "v_mfma_f32_16x16x32_bf16 v[208:211], v[252:255], v[40:43], 0\n"
    "buffer_load_dwordx4 v97, s[12:15], s51 offen offset:0 lds\n"
    "v_mfma_f32_16x16x32_bf16 v[64:67], v[44:47], a[148:151], v[64:67]\n"
    "s_waitcnt lgkmcnt(8)\n"
    "ds_read_b64_tr_b16 v[32:33], v241 offset:80\n"
    "ds_read_b64_tr_b16 v[34:35], v241 offset:2640\n"
    "v_mfma_f32_16x16x32_bf16 v[68:71], v[48:51], a[148:151], v[68:71]\n"
    "ds_read_b64_tr_b16 v[36:37], v241 offset:120\n"
    "ds_read_b64_tr_b16 v[38:39], v241 offset:2680\n"
    "v_mfma_f32_16x16x32_bf16 v[72:75], v[52:55], a[148:151], v[72:75]\n"
    "s_waitcnt lgkmcnt(8)\n"
    "ds_read_b64_tr_b16 v[40:41], v245 offset:0\n"
    "ds_read_b64_tr_b16 v[42:43], v245 offset:2560\n"
    "v_mfma_f32_16x16x32_bf16 v[76:79], v[56:59], a[148:151], v[76:79]\n"
    "ds_read_b64_tr_b16 v[44:45], v242 offset:0\n"
    "ds_read_b64_tr_b16 v[46:47], v242 offset:2560\n"
    "v_mfma_f32_16x16x32_bf16 v[204:207], v[252:255], v[56:59], 0\n"
    "buffer_load_dwordx4 v98, s[12:15], s51 offen offset:1024 lds\n"
    "v_mfma_f32_16x16x32_bf16 v[80:83], v[60:63], a[148:151], v[80:83]\n"
    "s_waitcnt lgkmcnt(8)\n"
    "ds_read_b64_tr_b16 v[48:49], v242 offset:40\n"
    "ds_read_b64_tr_b16 v[50:51], v242 offset:2600\n"
    "v_mfma_f32_16x16x32_bf16 v[64:67], v[24:27], a[152:155], v[64:67]\n"
    "ds_read_b64_tr_b16 v[52:53], v242 offset:80\n"
    "ds_read_b64_tr_b16 v[54:55], v242 offset:2640\n"
    "v_mfma_f32_16x16x32_bf16 v[68:71], v[28:31], a[152:155], v[68:71]\n"
    "s_waitcnt lgkmcnt(8)\n"
    "ds_read_b64_tr_b16 v[56:57], v242 offset:120\n"
    "ds_read_b64_tr_b16 v[58:59], v242 offset:2680\n"
    "v_mfma_f32_16x16x32_bf16 v[72:75], v[32:35], a[152:155], v[72:75]\n"
    "s_waitcnt vmcnt(9)\n"
    "s_barrier\n"
    "ds_read_b64_tr_b16 v[60:61], v246 offset:0\n"
    "ds_read_b64_tr_b16 v[62:63], v246 offset:2560\n"
    "v_mfma_f32_16x16x32_bf16 v[200:203], v[252:255], v[32:35], 0\n"
    "buffer_load_dwordx4 v99, s[12:15], s51 offen offset:2048 lds\n"
    "v_mfma_f32_16x16x32_bf16 v[76:79], v[36:39], a[152:155], v[76:79]\n"
    "s_waitcnt lgkmcnt(8)\n"
    "ds_read_b64_tr_b16 v[24:25], v239 offset:20480\n"
    "ds_read_b64_tr_b16 v[26:27], v239 offset:23040\n"
    "v_mfma_f32_16x16x32_bf16 v[80:83], v[40:43], a[152:155], v[80:83]\n"
    "ds_read_b64_tr_b16 v[28:29], v239 offset:20520\n"
    "ds_read_b64_tr_b16 v[30:31], v239 offset:23080\n"
    "v_mfma_f32_16x16x32_bf16 v[64:67], v[44:47], a[156:159], v[64:67]\n"
    "s_waitcnt lgkmcnt(8)\n"
    "ds_read_b64_tr_b16 v[32:33], v239 offset:20560\n"
    "ds_read_b64_tr_b16 v[34:35], v239 offset:23120\n"
    "v_mfma_f32_16x16x32_bf16 v[68:71], v[48:51], a[156:159], v[68:71]\n"
    "ds_read_b64_tr_b16 v[36:37], v239 offset:20600\n"
    "ds_read_b64_tr_b16 v[38:39], v239 offset:23160\n"
    "v_mfma_f32_16x16x32_bf16 v[196:199], v[252:255], v[48:51], 0\n"
    "buffer_load_dwordx4 v100, s[12:15], s51 offen offset:3072 lds\n"
    "v_mfma_f32_16x16x32_bf16 v[72:75], v[52:55], a[156:159], v[72:75]\n"
    "s_waitcnt lgkmcnt(8)\n"
    "ds_read_b64_tr_b16 v[40:41], v243 offset:20480\n"
    "ds_read_b64_tr_b16 v[42:43], v243 offset:23040\n"
    "v_mfma_f32_16x16x32_bf16 v[76:79], v[56:59], a[156:159], v[76:79]\n"
    "s_add_u32 m0, s45, 0x10000\n"
    "ds_read_b64_tr_b16 v[44:45], v240 offset:20480\n"
    "ds_read_b64_tr_b16 v[46:47], v240 offset:23040\n"
    "v_mfma_f32_16x16x32_bf16 v[80:83], v[60:63], a[156:159], v[80:83]\n"
    "s_waitcnt lgkmcnt(8)\n"
    "ds_read_b64_tr_b16 v[48:49], v240 offset:20520\n"
    "ds_read_b64_tr_b16 v[50:51], v240 offset:23080\n"
    "v_mfma_f32_16x16x32_bf16 v[64:67], v[24:27], a[160:163], v[64:67]\n"
    "ds_read_b64_tr_b16 v[52:53], v240 offset:20560\n"
    "ds_read_b64_tr_b16 v[54:55], v240 offset:23120\n"
    "v_mfma_f32_16x16x32_bf16 v[192:195], v[252:255], v[24:27], v[192:195]\n"
    "buffer_load_dwordx4 v101, s[12:15], s51 offen offset:0 lds\n"
    "v_mov_b32 v104, s6\n"
    "v_mov_b32 v105, s7\n"
    "v_mfma_f32_16x16x32_bf16 v[68:71], v[28:31], a[160:163], v[68:71]\n"
    "s_waitcnt lgkmcnt(8)\n"
    "ds_read_b64_tr_b16 v[56:57], v240 offset:20600\n"
    "ds_read_b64_tr_b16 v[58:59], v240 offset:23160\n"
    "s_waitcnt vmcnt(9)\n"
    "buffer_store_dwordx2 v[104:105], v102, s[24:27], s84 offen offset:0 sc1\n"
    "v_mfma_f32_16x16x32_bf16 v[72:75], v[32:35], a[160:163], v[72:75]\n"
    "s_add_u32 m0, s45, 0x14000\n"
    "ds_read_b64_tr_b16 v[60:61], v244 offset:20480\n"
    "ds_read_b64_tr_b16 v[62:63], v244 offset:23040\n"
    "v_mfma_f32_16x16x32_bf16 v[76:79], v[36:39], a[160:163], v[76:79]\n"
    "s_waitcnt lgkmcnt(8)\n"
    "ds_read_b64_tr_b16 v[24:25], v241 offset:20480\n"
    "ds_read_b64_tr_b16 v[26:27], v241 offset:23040\n"
    "v_mfma_f32_16x16x32_bf16 v[80:83], v[40:43], a[160:163], v[80:83]\n"
    "s_add_u32 s51, s47, 0xa0000\n"
    "ds_read_b64_tr_b16 v[28:29], v241 offset:20520\n"
    "ds_read_b64_tr_b16 v[30:31], v241 offset:23080\n"
    "v_mfma_f32_16x16x32_bf16 v[208:211], v[252:255], v[40:43], v[208:211]\n"
    "buffer_load_dwordx4 v97, s[12:15], s51 offen offset:0 lds\n"
    "v_mfma_f32_16x16x32_bf16 v[64:67], v[44:47], a[164:167], v[64:67]\n"
    "s_waitcnt lgkmcnt(8)\n"
    "ds_read_b64_tr_b16 v[32:33], v241 offset:20560\n"
    "ds_read_b64_tr_b16 v[34:35], v241 offset:23120\n"
    "v_mfma_f32_16x16x32_bf16 v[68:71], v[48:51], a[164:167], v[68:71]\n"
    "ds_read_b64_tr_b16 v[36:37], v241 offset:20600\n"
    "ds_read_b64_tr_b16 v[38:39], v241 offset:23160\n"
    "v_mfma_f32_16x16x32_bf16 v[72:75], v[52:55], a[164:167], v[72:75]\n"
    "s_waitcnt lgkmcnt(8)\n"
    "ds_read_b64_tr_b16 v[40:41], v245 offset:20480\n"
    "ds_read_b64_tr_b16 v[42:43], v245 offset:23040\n"
    "v_mfma_f32_16x16x32_bf16 v[76:79], v[56:59], a[164:167], v[76:79]\n"
    "ds_read_b64_tr_b16 v[44:45], v242 offset:20480\n"
    "ds_read_b64_tr_b16 v[46:47], v242 offset:23040\n"
    "v_mfma_f32_16x16x32_bf16 v[204:207], v[252:255], v[56:59], v[204:207]\n"
    "buffer_load_dwordx4 v98, s[12:15], s51 offen offset:1024 lds\n"
    "v_mfma_f32_16x16x32_bf16 v[80:83], v[60:63], a[164:167], v[80:83]\n"
    "s_waitcnt lgkmcnt(8)\n"
    "ds_read_b64_tr_b16 v[48:49], v242 offset:20520\n"
    "ds_read_b64_tr_b16 v[50:51], v242 offset:23080\n"
    "v_mfma_f32_16x16x32_bf16 v[64:67], v[24:27], a[168:171], v[64:67]\n"
    "ds_read_b64_tr_b16 v[52:53], v242 offset:20560\n"
    "ds_read_b64_tr_b16 v[54:55], v242 offset:23120\n"
    "v_mfma_f32_16x16x32_bf16 v[68:71], v[28:31], a[168:171], v[68:71]\n"
    "s_waitcnt lgkmcnt(8)\n"
    "ds_read_b64_tr_b16 v[56:57], v242 offset:20600\n"
    "ds_read_b64_tr_b16 v[58:59], v242 offset:23160\n"
    "v_mfma_f32_16x16x32_bf16 v[72:75], v[32:35], a[168:171], v[72:75]\n"
    "s_waitcnt vmcnt(8)\n"
    "s_barrier\n"
    "ds_read_b64_tr_b16 v[60:61], v246 offset:20480\n"
    "ds_read_b64_tr_b16 v[62:63], v246 offset:23040\n"
    "v_mfma_f32_16x16x32_bf16 v[200:203], v[252:255], v[32:35], v[200:203]\n"
    "buffer_load_dwordx4 v99, s[12:15], s51 offen offset:2048 lds\n"
    "v_mfma_f32_16x16x32_bf16 v[76:79], v[36:39], a[168:171], v[76:79]\n"
    "s_waitcnt lgkmcnt(8)\n"
    "ds_read_b64_tr_b16 v[24:25], v239 offset:40960\n"
    "ds_read_b64_tr_b16 v[26:27], v239 offset:43520\n"
    "v_mfma_f32_16x16x32_bf16 v[80:83], v[40:43], a[168:171], v[80:83]\n"
    "ds_read_b64_tr_b16 v[28:29], v239 offset:41000\n"
    "ds_read_b64_tr_b16 v[30:31], v239 offset:43560\n"
    "v_mfma_f32_16x16x32_bf16 v[64:67], v[44:47], a[172:175], v[64:67]\n"
    "s_waitcnt lgkmcnt(8)\n"
    "ds_read_b64_tr_b16 v[32:33], v239 offset:41040\n"
    "ds_read_b64_tr_b16 v[34:35], v239 offset:43600\n"
    "v_mfma_f32_16x16x32_bf16 v[68:71], v[48:51], a[172:175], v[68:71]\n"
    "ds_read_b64_tr_b16 v[36:37], v239 offset:41080\n"
    "ds_read_b64_tr_b16 v[38:39], v239 offset:43640\n"
    "v_mfma_f32_16x16x32_bf16 v[196:199], v[252:255], v[48:51], v[196:199]\n"
    "buffer_load_dwordx4 v100, s[12:15], s51 offen offset:3072 lds\n"
    "v_mfma_f32_16x16x32_bf16 v[72:75], v[52:55], a[172:175], v[72:75]\n"
    "s_waitcnt lgkmcnt(8)\n"
    "ds_read_b64_tr_b16 v[40:41], v243 offset:40960\n"
    "ds_read_b64_tr_b16 v[42:43], v243 offset:43520\n"
    "v_mfma_f32_16x16x32_bf16 v[76:79], v[56:59], a[172:175], v[76:79]\n"
    "s_add_u32 m0, s45, 0x15000\n"
    "ds_read_b64_tr_b16 v[44:45], v240 offset:40960\n"
    "ds_read_b64_tr_b16 v[46:47], v240 offset:43520\n"
    "v_mfma_f32_16x16x32_bf16 v[80:83], v[60:63], a[172:175], v[80:83]\n"
    "s_waitcnt lgkmcnt(8)\n"
    "ds_read_b64_tr_b16 v[48:49], v240 offset:41000\n"
    "ds_read_b64_tr_b16 v[50:51], v240 offset:43560\n"
    "v_mfma_f32_16x16x32_bf16 v[64:67], v[24:27], a[176:179], v[64:67]\n"
    "ds_read_b64_tr_b16 v[52:53], v240 offset:41040\n"
    "ds_read_b64_tr_b16 v[54:55], v240 offset:43600\n"
    "v_mfma_f32_16x16x32_bf16 v[192:195], v[252:255], v[24:27], v[192:195]\n"
    "buffer_load_dwordx4 v101, s[12:15], s51 offen offset:0 lds\n"
    "v_mfma_f32_16x16x32_bf16 v[68:71], v[28:31], a[176:179], v[68:71]\n"
    "s_waitcnt lgkmcnt(8)\n"
    "ds_read_b64_tr_b16 v[56:57], v240 offset:41080\n"
    "ds_read_b64_tr_b16 v[58:59], v240 offset:43640\n"
    "v_mfma_f32_16x16x32_bf16 v[72:75], v[32:35], a[176:179], v[72:75]\n"
    "ds_read_b64_tr_b16 v[60:61], v244 offset:40960\n"
    "ds_read_b64_tr_b16 v[62:63], v244 offset:43520\n"
    "v_mfma_f32_16x16x32_bf16 v[76:79], v[36:39], a[176:179], v[76:79]\n"
    "s_waitcnt lgkmcnt(8)\n"
    "ds_read_b64_tr_b16 v[24:25], v241 offset:40960\n"
    "ds_read_b64_tr_b16 v[26:27], v241 offset:43520\n"
    "v_mfma_f32_16x16x32_bf16 v[80:83], v[40:43], a[176:179], v[80:83]\n"
    "ds_read_b64_tr_b16 v[28:29], v241 offset:41000\n"
    "ds_read_b64_tr_b16 v[30:31], v241 offset:43560\n"
    "v_mfma_f32_16x16x32_bf16 v[208:211], v[252:255], v[40:43], v[208:211]\n"
    "buffer_load_dwordx4 v[112:115], v117, s[24:27], s55 offen offset:0 sc1\n"
    "v_mfma_f32_16x16x32_bf16 v[64:67], v[44:47], a[180:183], v[64:67]\n"
    "s_waitcnt lgkmcnt(8)\n"
    "ds_read_b64_tr_b16 v[32:33], v241 offset:41040\n"
    "ds_read_b64_tr_b16 v[34:35], v241 offset:43600\n"
    "v_mfma_f32_16x16x32_bf16 v[68:71], v[48:51], a[180:183], v[68:71]\n"
    "s_mov_b32 s30, 0x280000\n"
    "ds_read_b64_tr_b16 v[36:37], v241 offset:41080\n"
    "ds_read_b64_tr_b16 v[38:39], v241 offset:43640\n"
    "v_mfma_f32_16x16x32_bf16 v[72:75], v[52:55], a[180:183], v[72:75]\n"
    "s_waitcnt lgkmcnt(8)\n"
    "ds_read_b64_tr_b16 v[40:41], v245 offset:40960\n"
    "ds_read_b64_tr_b16 v[42:43], v245 offset:43520\n"
    "v_mfma_f32_16x16x32_bf16 v[76:79], v[56:59], a[180:183], v[76:79]\n"
    "s_mov_b32 s31, 0x20000\n"
    "ds_read_b64_tr_b16 v[44:45], v242 offset:40960\n"
    "ds_read_b64_tr_b16 v[46:47], v242 offset:43520\n"
    "v_mfma_f32_16x16x32_bf16 v[204:207], v[252:255], v[56:59], v[204:207]\n"
    "s_mov_b32 s34, 0xd80\n"
    "s_mov_b32 s35, 0x20000\n"
    "v_mfma_f32_16x16x32_bf16 v[80:83], v[60:63], a[180:183], v[80:83]\n"
    "s_waitcnt lgkmcnt(8)\n"
    "ds_read_b64_tr_b16 v[48:49], v242 offset:41000\n"
    "ds_read_b64_tr_b16 v[50:51], v242 offset:43560\n"
    "v_mfma_f32_16x16x32_bf16 v[64:67], v[24:27], a[184:187], v[64:67]\n"
    "s_mov_b32 s38, 0x500\n"
    "ds_read_b64_tr_b16 v[52:53], v242 offset:41040\n"
    "ds_read_b64_tr_b16 v[54:55], v242 offset:43600\n"
    "v_mfma_f32_16x16x32_bf16 v[68:71], v[28:31], a[184:187], v[68:71]\n"
    "s_waitcnt lgkmcnt(8)\n"
    "ds_read_b64_tr_b16 v[56:57], v242 offset:41080\n"
    "ds_read_b64_tr_b16 v[58:59], v242 offset:43640\n"
    "v_mfma_f32_16x16x32_bf16 v[72:75], v[32:35], a[184:187], v[72:75]\n"
    "s_waitcnt vmcnt(7)\n"
    "s_barrier\n"
    "ds_read_b64_tr_b16 v[60:61], v246 offset:40960\n"
    "ds_read_b64_tr_b16 v[62:63], v246 offset:43520\n"
    "v_mfma_f32_16x16x32_bf16 v[200:203], v[252:255], v[32:35], v[200:203]\n"
    "s_mov_b32 s39, 0x20000\n"
    "s_mov_b32 s23, 0x20000\n"
    "v_mfma_f32_16x16x32_bf16 v[76:79], v[36:39], a[184:187], v[76:79]\n"
    "s_waitcnt lgkmcnt(8)\n"
    "ds_read_b64_tr_b16 v[24:25], v239 offset:61440\n"
    "ds_read_b64_tr_b16 v[26:27], v239 offset:64000\n"
    "v_mfma_f32_16x16x32_bf16 v[80:83], v[40:43], a[184:187], v[80:83]\n"
    "ds_read_b64_tr_b16 v[28:29], v239 offset:61480\n"
    "ds_read_b64_tr_b16 v[30:31], v239 offset:64040\n"
    "v_mfma_f32_16x16x32_bf16 v[64:67], v[44:47], a[188:191], v[64:67]\n"
    "s_waitcnt lgkmcnt(8)\n"
    "ds_read_b64_tr_b16 v[32:33], v239 offset:61520\n"
    "ds_read_b64_tr_b16 v[34:35], v239 offset:64080\n"
    "v_mfma_f32_16x16x32_bf16 v[68:71], v[48:51], a[188:191], v[68:71]\n"
    "ds_read_b64_tr_b16 v[36:37], v239 offset:61560\n"
    "ds_read_b64_tr_b16 v[38:39], v239 offset:64120\n"
    "v_mfma_f32_16x16x32_bf16 v[196:199], v[252:255], v[48:51], v[196:199]\n"
    "v_mfma_f32_16x16x32_bf16 v[72:75], v[52:55], a[188:191], v[72:75]\n"
    "s_waitcnt lgkmcnt(8)\n"
    "ds_read_b64_tr_b16 v[40:41], v243 offset:61440\n"
    "ds_read_b64_tr_b16 v[42:43], v243 offset:64000\n"
    "v_mfma_f32_16x16x32_bf16 v[76:79], v[56:59], a[188:191], v[76:79]\n"
    "ds_read_b64_tr_b16 v[44:45], v240 offset:61440\n"
    "ds_read_b64_tr_b16 v[46:47], v240 offset:64000\n"
    "v_mfma_f32_16x16x32_bf16 v[80:83], v[60:63], a[188:191], v[80:83]\n"
    "s_waitcnt lgkmcnt(8)\n"
    "ds_read_b64_tr_b16 v[48:49], v240 offset:61480\n"
    "ds_read_b64_tr_b16 v[50:51], v240 offset:64040\n"
    "v_mfma_f32_16x16x32_bf16 v[64:67], v[24:27], a[192:195], v[64:67]\n"
    "ds_read_b64_tr_b16 v[52:53], v240 offset:61520\n"
    "ds_read_b64_tr_b16 v[54:55], v240 offset:64080\n"
    "v_mfma_f32_16x16x32_bf16 v[192:195], v[252:255], v[24:27], v[192:195]\n"
    "v_add_u32 v239, 0x14000, v239\n"
    "v_add_u32 v243, 0x14000, v243\n"
    "v_mfma_f32_16x16x32_bf16 v[68:71], v[28:31], a[192:195], v[68:71]\n"
    "s_waitcnt lgkmcnt(8)\n"
    "ds_read_b64_tr_b16 v[56:57], v240 offset:61560\n"
    "ds_read_b64_tr_b16 v[58:59], v240 offset:64120\n"
    "v_mfma_f32_16x16x32_bf16 v[72:75], v[32:35], a[192:195], v[72:75]\n"
    "ds_read_b64_tr_b16 v[60:61], v244 offset:61440\n"
    "ds_read_b64_tr_b16 v[62:63], v244 offset:64000\n"
    "v_mfma_f32_16x16x32_bf16 v[76:79], v[36:39], a[192:195], v[76:79]\n"
    "s_waitcnt lgkmcnt(8)\n"
    "ds_read_b64_tr_b16 v[24:25], v241 offset:61440\n"
    "ds_read_b64_tr_b16 v[26:27], v241 offset:64000\n"
    "v_mfma_f32_16x16x32_bf16 v[80:83], v[40:43], a[192:195], v[80:83]\n"
    "ds_read_b64_tr_b16 v[28:29], v241 offset:61480\n"
    "ds_read_b64_tr_b16 v[30:31], v241 offset:64040\n"
    "v_mfma_f32_16x16x32_bf16 v[208:211], v[252:255], v[40:43], v[208:211]\n"
    "v_add_u32 v240, 0x14000, v240\n"
    "v_add_u32 v244, 0x14000, v244\n"
    "v_mfma_f32_16x16x32_bf16 v[64:67], v[44:47], a[196:199], v[64:67]\n"
    "s_waitcnt lgkmcnt(8)\n"
    "ds_read_b64_tr_b16 v[32:33], v241 offset:61520\n"
    "ds_read_b64_tr_b16 v[34:35], v241 offset:64080\n"
    "v_mfma_f32_16x16x32_bf16 v[68:71], v[48:51], a[196:199], v[68:71]\n"
    "ds_read_b64_tr_b16 v[36:37], v241 offset:61560\n"
    "ds_read_b64_tr_b16 v[38:39], v241 offset:64120\n"
    "v_mfma_f32_16x16x32_bf16 v[72:75], v[52:55], a[196:199], v[72:75]\n"
    "s_waitcnt lgkmcnt(8)\n"
    "ds_read_b64_tr_b16 v[40:41], v245 offset:61440\n"
    "ds_read_b64_tr_b16 v[42:43], v245 offset:64000\n"
    "v_mfma_f32_16x16x32_bf16 v[76:79], v[56:59], a[196:199], v[76:79]\n"
    "ds_read_b64_tr_b16 v[44:45], v242 offset:61440\n"
    "ds_read_b64_tr_b16 v[46:47], v242 offset:64000\n"
    "v_mfma_f32_16x16x32_bf16 v[204:207], v[252:255], v[56:59], v[204:207]\n"
    "v_add_u32 v241, 0x14000, v241\n"
    "v_add_u32 v245, 0x14000, v245\n"
    "v_mfma_f32_16x16x32_bf16 v[80:83], v[60:63], a[196:199], v[80:83]\n"
    "s_waitcnt lgkmcnt(8)\n"
    "ds_read_b64_tr_b16 v[48:49], v242 offset:61480\n"
    "ds_read_b64_tr_b16 v[50:51], v242 offset:64040\n"
    "v_mfma_f32_16x16x32_bf16 v[64:67], v[24:27], a[200:203], v[64:67]\n"
    "ds_read_b64_tr_b16 v[52:53], v242 offset:61520\n"
    "ds_read_b64_tr_b16 v[54:55], v242 offset:64080\n"
    "s_waitcnt vmcnt(0)\n"
    "v_cmp_ne_u32 vcc, s6, v113\n"
    "v_cmp_ne_u32 s[16:17], s6, v115\n"
    "s_nop 0\n"
    "s_or_b64 vcc, vcc, s[16:17]\n"
    "s_cbranch_vccnz Lxsp5\n"
    "Lxok6:\n"
    "v_cvt_pk_bf16_f32 v88, v112, v112\n"
    "v_lshlrev_b32 v18, 16, v88\n"
    "v_mfma_f32_16x16x32_bf16 v[68:71], v[28:31], a[200:203], v[68:71]\n"
    "s_waitcnt lgkmcnt(8)\n"
    "ds_read_b64_tr_b16 v[56:57], v242 offset:61560\n"
    "ds_read_b64_tr_b16 v[58:59], v242 offset:64120\n"
    "v_sub_f32 v18, v112, v18\n"
    "v_cvt_pk_bf16_f32 v89, v18, 0\n"
    "v_mfma_f32_16x16x32_bf16 v[72:75], v[32:35], a[200:203], v[72:75]\n"
    "s_barrier\n"
    "ds_read_b64_tr_b16 v[60:61], v246 offset:61440\n"
    "ds_read_b64_tr_b16 v[62:63], v246 offset:64000\n"
    "v_mfma_f32_16x16x32_bf16 v[200:203], v[252:255], v[32:35], v[200:203]\n"
    "v_add_u32 v242, 0x14000, v242\n"
    "v_add_u32 v246, 0x14000, v246\n"
    "v_mul_f32 v108, 0x3ea52b5c, v114\n"
    "v_mfma_f32_16x16x32_bf16 v[76:79], v[36:39], a[200:203], v[76:79]\n"
    "s_waitcnt lgkmcnt(8)\n"
    "ds_read_b64_tr_b16 v[24:25], v239 offset:0\n"
    "ds_read_b64_tr_b16 v[26:27], v239 offset:2560\n"
    "v_mfma_f32_16x16x32_bf16 v[80:83], v[40:43], a[200:203], v[80:83]\n"
    "ds_read_b64_tr_b16 v[28:29], v239 offset:40\n"
    "ds_read_b64_tr_b16 v[30:31], v239 offset:2600\n"
    "v_mfma_f32_16x16x32_bf16 v[64:67], v[44:47], a[204:207], v[64:67]\n"
    "s_waitcnt lgkmcnt(8)\n"
    "ds_read_b64_tr_b16 v[32:33], v239 offset:80\n"
    "ds_read_b64_tr_b16 v[34:35], v239 offset:2640\n"
    "v_mfma_f32_16x16x32_bf16 v[68:71], v[48:51], a[204:207], v[68:71]\n"
    "ds_read_b64_tr_b16 v[36:37], v239 offset:120\n"
    "ds_read_b64_tr_b16 v[38:39], v239 offset:2680\n"
    "v_mfma_f32_16x16x32_bf16 v[196:199], v[252:255], v[48:51], v[196:199]\n"
    "v_mfma_f32_16x16x32_bf16 v[72:75], v[52:55], a[204:207], v[72:75]\n"
    "s_waitcnt lgkmcnt(8)\n"
    "ds_read_b64_tr_b16 v[40:41], v243 offset:0\n"
    "ds_read_b64_tr_b16 v[42:43], v243 offset:2560\n"
    "v_mfma_f32_16x16x32_bf16 v[76:79], v[56:59], a[204:207], v[76:79]\n"
    "ds_read_b64_tr_b16 v[44:45], v240 offset:0\n"
    "ds_read_b64_tr_b16 v[46:47], v240 offset:2560\n"
    "v_mfma_f32_16x16x32_bf16 v[80:83], v[60:63], a[204:207], v[80:83]\n"
    "s_waitcnt lgkmcnt(8)\n"
    "ds_read_b64_tr_b16 v[48:49], v240 offset:40\n"
    "ds_read_b64_tr_b16 v[50:51], v240 offset:2600\n"
    "v_mfma_f32_16x16x32_bf16 v[64:67], v[24:27], a[208:211], v[64:67]\n"
    "ds_read_b64_tr_b16 v[52:53], v240 offset:80\n"
    "ds_read_b64_tr_b16 v[54:55], v240 offset:2640\n"
    "v_mfma_f32_16x16x32_bf16 v[192:195], v[252:255], v[24:27], v[192:195]\n"
    "v_lshlrev_b32 v23, 3, v1\n"
    "v_mfma_f32_16x16x32_bf16 v[68:71], v[28:31], a[208:211], v[68:71]\n"
    "s_waitcnt lgkmcnt(8)\n"
    "ds_read_b64_tr_b16 v[56:57], v240 offset:120\n"
    "ds_read_b64_tr_b16 v[58:59], v240 offset:2680\n"
    "v_mfma_f32_16x16x32_bf16 v[196:199], v[252:255], v[28:31], v[196:199]\n"
    "buffer_load_dwordx4 v[104:107], v23, s[24:27], s85 offen offset:0 sc1\n"
    "v_mfma_f32_16x16x32_bf16 v[72:75], v[32:35], a[208:211], v[72:75]\n"
    "ds_read_b64_tr_b16 v[60:61], v244 offset:0\n"
    "ds_read_b64_tr_b16 v[62:63], v244 offset:2560\n"
    "v_mfma_f32_16x16x32_bf16 v[200:203], v[252:255], v[32:35], v[200:203]\n"
    "v_mfma_f32_16x16x32_bf16 v[76:79], v[36:39], a[208:211], v[76:79]\n"
    "s_waitcnt lgkmcnt(8)\n"
    "ds_read_b64_tr_b16 v[24:25], v241 offset:0\n"
    "ds_read_b64_tr_b16 v[26:27], v241 offset:2560\n"
    "v_mfma_f32_16x16x32_bf16 v[204:207], v[252:255], v[36:39], v[204:207]\n"
    "v_mfma_f32_16x16x32_bf16 v[80:83], v[40:43], a[208:211], v[80:83]\n"
    "ds_read_b64_tr_b16 v[28:29], v241 offset:40\n"
    "ds_read_b64_tr_b16 v[30:31], v241 offset:2600\n"
    "v_mfma_f32_16x16x32_bf16 v[208:211], v[252:255], v[40:43], v[208:211]\n"
    "v_mfma_f32_16x16x32_bf16 v[64:67], v[44:47], a[212:215], v[64:67]\n"
    "s_waitcnt lgkmcnt(8)\n"
    "ds_read_b64_tr_b16 v[32:33], v241 offset:80\n"
    "ds_read_b64_tr_b16 v[34:35], v241 offset:2640\n"
    "v_cvt_pk_bf16_f32 v195, v192, 0\n"
    "v_lshlrev_b32 v193, 16, v195\n"
    "v_sub_f32 v193, v192, v193\n"
    "v_cvt_pk_bf16_f32 v194, v192, v193\n"
    "v_mfma_f32_16x16x32_bf16 v[68:71], v[48:51], a[212:215], v[68:71]\n"
    "ds_read_b64_tr_b16 v[36:37], v241 offset:120\n"
    "ds_read_b64_tr_b16 v[38:39], v241 offset:2680\n"
    "ds_write_b64 v110, v[194:195] offset:0\n"
    "v_cvt_pk_bf16_f32 v199, v196, 0\n"
    "v_lshlrev_b32 v197, 16, v199\n"
    "v_sub_f32 v197, v196, v197\n"
    "v_cvt_pk_bf16_f32 v198, v196, v197\n"
    "v_mfma_f32_16x16x32_bf16 v[72:75], v[52:55], a[212:215], v[72:75]\n"
    "s_waitcnt lgkmcnt(9)\n"
    "ds_read_b64_tr_b16 v[40:41], v245 offset:0\n"
    "ds_read_b64_tr_b16 v[42:43], v245 offset:2560\n"
    "ds_write_b64 v110, v[198:199] offset:512\n"
    "v_cvt_pk_bf16_f32 v203, v200, 0\n"
    "v_lshlrev_b32 v201, 16, v203\n"
    "v_sub_f32 v201, v200, v201\n"
    "v_cvt_pk_bf16_f32 v202, v200, v201\n"
    "v_mfma_f32_16x16x32_bf16 v[76:79], v[56:59], a[212:215], v[76:79]\n"
    "ds_read_b64_tr_b16 v[44:45], v242 offset:0\n"
    "ds_read_b64_tr_b16 v[46:47], v242 offset:2560\n"
    "ds_write_b64 v110, v[202:203] offset:1024\n"
    "v_cvt_pk_bf16_f32 v207, v204, 0\n"
    "v_lshlrev_b32 v205, 16, v207\n"
    "v_sub_f32 v205, v204, v205\n"
    "v_cvt_pk_bf16_f32 v206, v204, v205\n"
    "v_mfma_f32_16x16x32_bf16 v[80:83], v[60:63], a[212:215], v[80:83]\n"
    "s_waitcnt lgkmcnt(11)\n"
    "ds_read_b64_tr_b16 v[48:49], v242 offset:40\n"
    "ds_read_b64_tr_b16 v[50:51], v242 offset:2600\n"
    "ds_write_b64 v110, v[206:207] offset:1536\n"
    "v_cvt_pk_bf16_f32 v211, v208, 0\n"
    "v_lshlrev_b32 v209, 16, v211\n"
    "v_sub_f32 v209, v208, v209\n"
    "v_cvt_pk_bf16_f32 v210, v208, v209\n"
    "v_mfma_f32_16x16x32_bf16 v[64:67], v[24:27], a[216:219], v[64:67]\n"
    "ds_read_b64_tr_b16 v[52:53], v242 offset:80\n"
    "s_waitcnt lgkmcnt(14)\n"
    "ds_read_b64_tr_b16 v[54:55], v242 offset:2640\n"
    "s_waitcnt lgkmcnt(14)\n"
    "ds_write_b64 v110, v[210:211] offset:2048\n"
    "v_mfma_f32_16x16x32_bf16 v[68:71], v[28:31], a[216:219], v[68:71]\n"
    "s_waitcnt lgkmcnt(13)\n"
    "ds_read_b64_tr_b16 v[56:57], v242 offset:120\n"
    "ds_read_b64_tr_b16 v[58:59], v242 offset:2680\n"
    "s_waitcnt lgkmcnt(2)\n"
    "s_barrier\n"
    "ds_read_b64 v[120:121], v111 offset:0\n"
    "ds_read_b64 v[124:125], v111 offset:512\n"
    "v_mfma_f32_16x16x32_bf16 v[72:75], v[32:35], a[216:219], v[72:75]\n"
    "ds_read_b64_tr_b16 v[60:61], v246 offset:0\n"
    "ds_read_b64_tr_b16 v[62:63], v246 offset:2560\n"
    "ds_read_b64 v[128:129], v111 offset:1024\n"
    "ds_read_b64 v[132:133], v111 offset:1536\n"
    "v_mfma_f32_16x16x32_bf16 v[76:79], v[36:39], a[216:219], v[76:79]\n"
    "ds_read_b64 v[136:137], v111 offset:2048\n"
    "v_mfma_f32_16x16x32_bf16 v[80:83], v[40:43], a[216:219], v[80:83]\n"
    "v_mfma_f32_16x16x32_bf16 v[64:67], v[44:47], a[220:223], v[64:67]\n"
    "Lhop2:\n"
    "s_cbranch_execz Lhop6\n"
    "v_mfma_f32_16x16x32_bf16 v[68:71], v[48:51], a[220:223], v[68:71]\n"
    "v_mfma_f32_16x16x32_bf16 v[72:75], v[52:55], a[220:223], v[72:75]\n"
    "s_waitcnt lgkmcnt(3)\n"
    "v_mfma_f32_16x16x32_bf16 v[76:79], v[56:59], a[220:223], v[76:79]\n"
    "v_mfma_f32_16x16x32_bf16 v[80:83], v[60:63], a[220:223], v[80:83]\n"
    "s_waitcnt vmcnt(0)\n"
    "v_cmp_ne_u64 vcc, s[6:7], v[104:105]\n"
    "v_cmp_ne_u64 s[16:17], s[6:7], v[106:107]\n"
    "s_nop 0\n"
    "s_or_b64 vcc, vcc, s[16:17]\n"
    "s_cbranch_vccnz Lspin7\n"
    "Lseen8:\n"
    "v_mfma_f32_16x16x32_bf16 v[64:67], v[120:123], v[88:91], v[64:67]\n"
    "s_add_u32 s57, s60, 0\n"
    "buffer_load_dwordx4 a[0:3], v1, s[24:27], s57 offen offset:0 sc1\n"
    "v_mfma_f32_16x16x32_bf16 v[68:71], v[124:127], v[88:91], v[68:71]\n"
    "s_waitcnt lgkmcnt(2)\n"
    "v_mfma_f32_16x16x32_bf16 v[72:75], v[128:131], v[88:91], v[72:75]\n"
    "buffer_load_dwordx4 a[4:7], v1, s[24:27], s57 offen offset:1024 sc1\n"
    "s_waitcnt lgkmcnt(1)\n"
    "v_mfma_f32_16x16x32_bf16 v[76:79], v[132:135], v[88:91], v[76:79]\n"
    "s_waitcnt lgkmcnt(0)\n"
    "v_mfma_f32_16x16x32_bf16 v[80:83], v[136:139], v[88:91], v[80:83]\n"
    "buffer_load_dwordx4 a[8:11], v1, s[24:27], s57 offen offset:2048 sc1\n"
    "v_mov_b32 v20, 0\n"
    "v_mov_b32 v21, 0\n"
    "v_pk_mul_f32 v[64:65], v[64:65], v[108:109] op_sel_hi:[1,0]\n"
    "v_pk_mul_f32 v[66:67], v[66:67], v[108:109] op_sel_hi:[1,0]\n"
    "v_cvt_pk_bf16_f32 v64, v64, v65\n"
    "v_cvt_pk_bf16_f32 v65, v66, v67\n"
    "v_pk_mul_f32 v[68:69], v[68:69], v[108:109] op_sel_hi:[1,0]\n"
    "v_pk_mul_f32 v[70:71], v[70:71], v[108:109] op_sel_hi:[1,0]\n"
    "v_cvt_pk_bf16_f32 v68, v68, v69\n"
    "v_cvt_pk_bf16_f32 v69, v70, v71\n"
    "v_pk_mul_f32 v[72:73], v[72:73], v[108:109] op_sel_hi:[1,0]\n"
    "v_pk_mul_f32 v[74:75], v[74:75], v[108:109] op_sel_hi:[1,0]\n"
    "v_cvt_pk_bf16_f32 v72, v72, v73\n"
    "v_cvt_pk_bf16_f32 v73, v74, v75\n"
    "v_pk_mul_f32 v[76:77], v[76:77], v[108:109] op_sel_hi:[1,0]\n"
    "v_pk_mul_f32 v[78:79], v[78:79], v[108:109] op_sel_hi:[1,0]\n"
    "v_cvt_pk_bf16_f32 v76, v76, v77\n"
    "v_cvt_pk_bf16_f32 v77, v78, v79\n"
    "v_pk_mul_f32 v[80:81], v[80:81], v[108:109] op_sel_hi:[1,0]\n"
    "v_pk_mul_f32 v[82:83], v[82:83], v[108:109] op_sel_hi:[1,0]\n"
    "v_cvt_pk_bf16_f32 v80, v80, v81\n"
    "v_cvt_pk_bf16_f32 v81, v82, v83\n"
    "ds_write_b64 v247, v[64:65] offset:0\n"
    "ds_write_b64 v247, v[68:69] offset:2048\n"
    "ds_write_b64 v247, v[72:73] offset:4096\n"
    "ds_write_b64 v247, v[76:77] offset:6144\n"
    "ds_write_b64 v248, v[80:81] offset:0\n"
    "buffer_load_dwordx4 a[12:15], v1, s[24:27], s57 offen offset:3072 sc1\n"
    "s_waitcnt lgkmcnt(0)\n"
    "s_barrier\n"
    "v_xor_b32 v17, 0x400, v237\n"
    "ds_read_b128 v[152:155], v237 offset:0\n"
    "ds_read_b128 v[156:159], v17 offset:0\n"
    "ds_read_b64 v[168:169], v238 offset:0\n"
    "v_xor_b32 v16, 0x800, v237\n"
    "v_xor_b32 v18, 0x200, v238\n"
    "v_xor_b32 v17, 0xc00, v237\n"
    "ds_read_b128 v[160:163], v16 offset:0\n"
    "ds_read_b128 v[164:167], v17 offset:0\n"
    "ds_read_b64 v[170:171], v18 offset:0\n"
    "v_mfma_f32_32x32x4_2b_bf16 v[88:119], v[20:21], v[20:21], 0\n"
    "s_add_u32 s57, s60, 0x1000\n"
    "buffer_load_dwordx4 a[24:27], v13, s[24:27], s57 offen offset:2048 sc1\n"
    "v_mfma_f32_32x32x4_2b_bf16 v[120:151], v[20:21], v[20:21], 0\n"
    "buffer_load_dwordx4 a[16:19], v1, s[24:27], s57 offen offset:0 sc1\n"
    "s_waitcnt vmcnt(5) lgkmcnt(5)\n"
    "v_mfma_f32_32x32x16_bf16 v[24:39], a[0:3], v[152:155], 0\n"
    "buffer_load_dwordx4 a[20:23], v1, s[24:27], s57 offen offset:1024 sc1\n"
    "s_waitcnt lgkmcnt(4)\n"
    "v_mfma_f32_32x32x16_bf16 v[40:55], a[0:3], v[156:159], 0\n"
    "s_add_u32 s57, s60, 0x1a00\n"
    "buffer_load_dwordx4 a[28:31], v1, s[24:27], s57 offen offset:0 sc1\n"
    "s_waitcnt vmcnt(6)\n"
    "v_mfma_f32_32x32x16_bf16 v[56:71], a[4:7], v[152:155], 0\n"
    "buffer_load_dwordx4 a[32:35], v1, s[24:27], s57 offen offset:1024 sc1\n"
    "buffer_load_dwordx4 a[36:39], v1, s[24:27], s57 offen offset:2048 sc1\n"
    "s_waitcnt vmcnt(5) lgkmcnt(3)\n"
    "s_nop 2\n"
    "v_mfma_f32_32x32x4_2b_bf16 v[24:55], a[24:25], v[168:169], v[24:55]\n"
    "buffer_load_dwordx4 a[40:43], v1, s[24:27], s57 offen offset:3072 sc1\n"
    "s_add_u32 s57, s60, 0x2a00\n"
    "buffer_load_dwordx4 a[52:55], v13, s[24:27], s57 offen offset:2048 sc1\n"
    "s_nop 15\n"
    "s_nop 0\n"
    "v_exp_f32 v24, v24\n"
    "v_exp_f32 v25, v25\n"
    "buffer_load_dwordx4 a[44:47], v1, s[24:27], s57 offen offset:0 sc1\n"
    "v_exp_f32 v26, v26\n"
    "v_exp_f32 v27, v27\n"
    "v_exp_f32 v28, v28\n"
    "v_mfma_f32_32x32x16_bf16 v[72:87], a[4:7], v[156:159], 0\n"
    "v_exp_f32 v29, v29\n"
    "v_exp_f32 v30, v30\n"
    "v_exp_f32 v31, v31\n"
    "v_exp_f32 v32, v32\n"
    "v_exp_f32 v33, v33\n"
    "v_smfmac_f32_32x32x32_bf16 v[88:103], a[8:11], v[24:31], v2\n"
    "buffer_load_dwordx4 a[48:51], v1, s[24:27], s57 offen offset:1024 sc1\n"
    "v_exp_f32 v34, v34\n"
    "v_exp_f32 v35, v35\n"
    "v_exp_f32 v36, v36\n"
    "v_exp_f32 v37, v37\n"
    "v_mfma_f32_32x32x4_2b_bf16 v[56:87], a[26:27], v[168:169], v[56:87]\n"
    "v_exp_f32 v38, v38\n"
    "v_exp_f32 v39, v39\n"
    "v_exp_f32 v40, v40\n"
    "v_exp_f32 v41, v41\n"
    "v_exp_f32 v42, v42\n"
    "v_exp_f32 v43, v43\n"
    "v_exp_f32 v44, v44\n"
    "v_exp_f32 v45, v45\n"
    "v_smfmac_f32_32x32x32_bf16 v[88:103], a[12:15], v[32:39], v2\n"
    "s_add_u32 s57, s60, 0x3400\n"
    "buffer_load_dwordx4 a[56:59], v1, s[24:27], s57 offen offset:0 sc1\n"
    "v_exp_f32 v46, v46\n"
    "v_exp_f32 v47, v47\n"
    "v_exp_f32 v48, v48\n"
    "v_exp_f32 v49, v49\n"
    "v_smfmac_f32_32x32x32_bf16 v[104:119], a[8:11], v[40:47], v2\n"
    "buffer_load_dwordx4 a[60:63], v1, s[24:27], s57 offen offset:1024 sc1\n"
    "v_exp_f32 v50, v50\n"
    "v_exp_f32 v51, v51\n"
    "v_exp_f32 v52, v52\n"
    "s_waitcnt lgkmcnt(2)\n"
    "v_mfma_f32_32x32x16_bf16 v[24:39], a[0:3], v[160:163], 0\n"
    "v_exp_f32 v53, v53\n"
    "v_exp_f32 v54, v54\n"
    "v_exp_f32 v55, v55\n"
    "v_exp_f32 v56, v56\n"
    "v_exp_f32 v57, v57\n"
    "v_smfmac_f32_32x32x32_bf16 v[104:119], a[12:15], v[48:55], v2\n"
    "buffer_load_dwordx4 a[64:67], v1, s[24:27], s57 offen offset:2048 sc1\n"
    "v_exp_f32 v58, v58\n"
    "v_exp_f32 v59, v59\n"
    "v_exp_f32 v60, v60\n"
    "s_waitcnt lgkmcnt(1)\n"
    "v_mfma_f32_32x32x16_bf16 v[40:55], a[0:3], v[164:167], 0\n"
    "v_exp_f32 v61, v61\n"
    "v_exp_f32 v62, v62\n"
    "v_exp_f32 v63, v63\n"
    "v_exp_f32 v64, v64\n"
    "v_exp_f32 v65, v65\n"
    "s_waitcnt vmcnt(11)\n"
    "v_smfmac_f32_32x32x32_bf16 v[88:103], a[16:19], v[56:63], v2\n"
    "buffer_load_dwordx4 a[68:71], v1, s[24:27], s57 offen offset:3072 sc1\n"
    "v_exp_f32 v66, v66\n"
    "v_exp_f32 v67, v67\n"
    "v_exp_f32 v68, v68\n"
    "v_exp_f32 v69, v69\n"
    "s_waitcnt lgkmcnt(0)\n"
    "v_mfma_f32_32x32x4_2b_bf16 v[24:55], a[24:25], v[170:171], v[24:55]\n"
    "v_exp_f32 v70, v70\n"
    "v_exp_f32 v71, v71\n"
    "v_exp_f32 v72, v72\n"
    "v_exp_f32 v73, v73\n"
    "v_exp_f32 v74, v74\n"
    "v_exp_f32 v75, v75\n"
    "v_exp_f32 v76, v76\n"
    "v_exp_f32 v77, v77\n"
    "s_waitcnt vmcnt(11)\n"
    "v_smfmac_f32_32x32x32_bf16 v[88:103], a[20:23], v[64:71], v2\n"
    "s_add_u32 s57, s60, 0x4400\n"
    "buffer_load_dwordx4 a[80:83], v13, s[24:27], s57 offen offset:2048 sc1\n"
    "v_exp_f32 v78, v78\n"
    "v_exp_f32 v79, v79\n"
    "v_exp_f32 v80, v80\n"
    "v_exp_f32 v81, v81\n"
    "v_smfmac_f32_32x32x32_bf16 v[104:119], a[16:19], v[72:79], v2\n"
    "buffer_load_dwordx4 a[72:75], v1, s[24:27], s57 offen offset:0 sc1\n"
    "v_exp_f32 v82, v82\n"
    "v_exp_f32 v83, v83\n"
    "v_exp_f32 v84, v84\n"
    "v_mfma_f32_32x32x16_bf16 v[56:71], a[4:7], v[160:163], 0\n"
    "v_exp_f32 v85, v85\n"
    "v_exp_f32 v86, v86\n"
    "v_exp_f32 v87, v87\n"
    "v_exp_f32 v24, v24\n"
    "v_exp_f32 v25, v25\n"
    "v_smfmac_f32_32x32x32_bf16 v[104:119], a[20:23], v[80:87], v2\n"
    "buffer_load_dwordx4 a[76:79], v1, s[24:27], s57 offen offset:1024 sc1\n"
    "v_exp_f32 v26, v26\n"
    "v_exp_f32 v27, v27\n"
    "v_exp_f32 v28, v28\n"
    "v_mfma_f32_32x32x16_bf16 v[72:87], a[4:7], v[164:167], 0\n"
    "s_lshl_b32 s53, s41, 10\n"
    "v_exp_f32 v29, v29\n"
    "v_exp_f32 v30, v30\n"
    "v_exp_f32 v31, v31\n"
    "v_exp_f32 v32, v32\n"
    "v_exp_f32 v33, v33\n"
    "v_smfmac_f32_32x32x32_bf16 v[120:135], a[8:11], v[24:31], v2\n"
    "s_add_u32 m0, s53, 0x1f000\n"
    "s_nop 0\n"
    "buffer_load_dwordx4 v232, s[20:23], s92 offen nt lds\n"
    "v_exp_f32 v34, v34\n"
    "v_exp_f32 v35, v35\n"
    "v_exp_f32 v36, v36\n"
    "v_exp_f32 v37, v37\n"
    "v_mfma_f32_32x32x4_2b_bf16 v[56:87], a[26:27], v[170:171], v[56:87]\n"
    "v_exp_f32 v38, v38\n"
    "v_exp_f32 v39, v39\n"
    "v_exp_f32 v40, v40\n"
    "v_exp_f32 v41, v41\n"
    "v_exp_f32 v42, v42\n"
    "v_exp_f32 v43, v43\n"
    "v_exp_f32 v44, v44\n"
    "v_exp_f32 v45, v45\n"
    "v_smfmac_f32_32x32x32_bf16 v[120:135], a[12:15], v[32:39], v2\n"
    "s_add_u32 m0, s53, 0x20000\n"
    "s_add_u32 s92, s92, s93\n"
    "buffer_load_dwordx4 v232, s[20:23], s92 offen nt lds\n"
    "v_exp_f32 v46, v46\n"
    "v_exp_f32 v47, v47\n"
    "v_exp_f32 v48, v48\n"
    "v_exp_f32 v49, v49\n"
    "v_smfmac_f32_32x32x32_bf16 v[136:151], a[8:11], v[40:47], v2\n"
    "s_waitcnt vmcnt(9)\n"
    "v_exp_f32 v50, v50\n"
    "v_exp_f32 v51, v51\n"
    "v_exp_f32 v52, v52\n"
    "v_mfma_f32_32x32x16_bf16 v[24:39], a[28:31], v[152:155], 0\n"
    "s_add_u32 m0, s53, 0x21000\n"
    "v_exp_f32 v53, v53\n"
    "v_exp_f32 v54, v54\n"
    "v_exp_f32 v55, v55\n"
    "v_exp_f32 v56, v56\n"
    "v_exp_f32 v57, v57\n"
    "v_smfmac_f32_32x32x32_bf16 v[136:151], a[12:15], v[48:55], v2\n"
    "s_add_u32 s92, s92, s93\n"
    "buffer_load_dwordx4 v232, s[20:23], s92 offen nt lds\n"
    "v_exp_f32 v58, v58\n"
    "v_exp_f32 v59, v59\n"
    "v_exp_f32 v60, v60\n"
    "v_mfma_f32_32x32x16_bf16 v[40:55], a[28:31], v[156:159], 0\n"
    "s_add_u32 m0, s53, 0x22000\n"
    "v_exp_f32 v61, v61\n"
    "v_exp_f32 v62, v62\n"
    "v_exp_f32 v63, v63\n"
    "v_exp_f32 v64, v64\n"
    "v_exp_f32 v65, v65\n"
    "v_smfmac_f32_32x32x32_bf16 v[120:135], a[16:19], v[56:63], v2\n"
    "s_add_u32 s92, s92, s93\n"
    "buffer_load_dwordx4 v232, s[20:23], s92 offen nt lds\n"
    "v_exp_f32 v66, v66\n"
    "v_exp_f32 v67, v67\n"
    "v_exp_f32 v68, v68\n"
    "v_exp_f32 v69, v69\n"
    "v_mfma_f32_32x32x4_2b_bf16 v[24:55], a[52:53], v[168:169], v[24:55]\n"
    "v_exp_f32 v70, v70\n"
    "v_exp_f32 v71, v71\n"
    "v_exp_f32 v72, v72\n"
    "v_exp_f32 v73, v73\n"
    "v_exp_f32 v74, v74\n"
    "v_exp_f32 v75, v75\n"
    "v_exp_f32 v76, v76\n"
    "v_exp_f32 v77, v77\n"
    "v_smfmac_f32_32x32x32_bf16 v[120:135], a[20:23], v[64:71], v2\n"
    "s_add_u32 m0, s53, 0x23000\n"
    "s_add_u32 s92, s92, s93\n"
    "buffer_load_dwordx4 v232, s[20:23], s92 offen nt lds\n"
    "v_exp_f32 v78, v78\n"
    "v_exp_f32 v79, v79\n"
    "v_exp_f32 v80, v80\n"
    "v_exp_f32 v81, v81\n"
    "v_smfmac_f32_32x32x32_bf16 v[136:151], a[16:19], v[72:79], v2\n"
    "buffer_load_ushort v236, v236, s[32:35], s94 offen\n"
    "v_exp_f32 v82, v82\n"
    "v_exp_f32 v83, v83\n"
    "v_exp_f32 v84, v84\n"
    "v_mfma_f32_32x32x16_bf16 v[56:71], a[32:35], v[152:155], 0\n"
    "v_exp_f32 v85, v85\n"
    "v_exp_f32 v86, v86\n"
    "v_exp_f32 v87, v87\n"
    "v_exp_f32 v24, v24\n"
    "v_exp_f32 v25, v25\n"
    "v_smfmac_f32_32x32x32_bf16 v[136:151], a[20:23], v[80:87], v2\n"
    "v_exp_f32 v26, v26\n"
    "v_exp_f32 v27, v27\n"
    "v_exp_f32 v28, v28\n"
    "v_mfma_f32_32x32x16_bf16 v[72:87], a[32:35], v[156:159], 0\n"
    "v_exp_f32 v29, v29\n"
    "v_exp_f32 v30, v30\n"
    "v_exp_f32 v31, v31\n"
    "v_exp_f32 v32, v32\n"
    "v_exp_f32 v33, v33\n"
    "v_smfmac_f32_32x32x32_bf16 v[88:103], a[36:39], v[24:31], v2\n"
    "v_exp_f32 v34, v34\n"
    "v_exp_f32 v35, v35\n"
    "v_exp_f32 v36, v36\n"
    "v_exp_f32 v37, v37\n"
    "v_mfma_f32_32x32x4_2b_bf16 v[56:87], a[54:55], v[168:169], v[56:87]\n"
    "v_exp_f32 v38, v38\n"
    "v_exp_f32 v39, v39\n"
    "v_exp_f32 v40, v40\n"
    "v_exp_f32 v41, v41\n"
    "v_exp_f32 v42, v42\n"
    "v_exp_f32 v43, v43\n"
    "v_exp_f32 v44, v44\n"
    "v_exp_f32 v45, v45\n"
    "v_smfmac_f32_32x32x32_bf16 v[88:103], a[40:43], v[32:39], v2\n"
    "v_exp_f32 v46, v46\n"
    "v_exp_f32 v47, v47\n"
    "v_exp_f32 v48, v48\n"
    "v_exp_f32 v49, v49\n"
    "v_smfmac_f32_32x32x32_bf16 v[104:119], a[36:39], v[40:47], v2\n"
    "v_exp_f32 v50, v50\n"
    "v_exp_f32 v51, v51\n"
    "v_exp_f32 v52, v52\n"
    "v_mfma_f32_32x32x16_bf16 v[24:39], a[28:31], v[160:163], 0\n"
    "v_exp_f32 v53, v53\n"
    "v_exp_f32 v54, v54\n"
    "v_exp_f32 v55, v55\n"
    "v_exp_f32 v56, v56\n"
    "v_exp_f32 v57, v57\n"
    "v_smfmac_f32_32x32x32_bf16 v[104:119], a[40:43], v[48:55], v2\n"
    "v_exp_f32 v58, v58\n"
    "v_exp_f32 v59, v59\n"
    "v_exp_f32 v60, v60\n"
    "v_mfma_f32_32x32x16_bf16 v[40:55], a[28:31], v[164:167], 0\n"
    "v_exp_f32 v61, v61\n"
    "v_exp_f32 v62, v62\n"
    "v_exp_f32 v63, v63\n"
    "v_exp_f32 v64, v64\n"
    "v_exp_f32 v65, v65\n"
    "v_smfmac_f32_32x32x32_bf16 v[88:103], a[44:47], v[56:63], v2\n"
    "v_exp_f32 v66, v66\n"
    "v_exp_f32 v67, v67\n"
    "v_exp_f32 v68, v68\n"
    "v_exp_f32 v69, v69\n"
    "v_mfma_f32_32x32x4_2b_bf16 v[24:55], a[52:53], v[170:171], v[24:55]\n"
    "v_exp_f32 v70, v70\n"
    "v_exp_f32 v71, v71\n"
    "v_exp_f32 v72, v72\n"
    "v_exp_f32 v73, v73\n"
    "v_exp_f32 v74, v74\n"
    "v_exp_f32 v75, v75\n"
    "v_exp_f32 v76, v76\n"
    "v_exp_f32 v77, v77\n"
    "v_smfmac_f32_32x32x32_bf16 v[88:103], a[48:51], v[64:71], v2\n"
    "v_exp_f32 v78, v78\n"
    "v_exp_f32 v79, v79\n"
    "v_exp_f32 v80, v80\n"
    "v_exp_f32 v81, v81\n"
    "v_smfmac_f32_32x32x32_bf16 v[104:119], a[44:47], v[72:79], v2\n"
    "v_exp_f32 v82, v82\n"
    "v_exp_f32 v83, v83\n"
    "v_exp_f32 v84, v84\n"
    "v_mfma_f32_32x32x16_bf16 v[56:71], a[32:35], v[160:163], 0\n"
    "v_exp_f32 v85, v85\n"
    "v_exp_f32 v86, v86\n"
    "v_exp_f32 v87, v87\n"
    "v_exp_f32 v24, v24\n"
    "v_exp_f32 v25, v25\n"
    "v_smfmac_f32_32x32x32_bf16 v[104:119], a[48:51], v[80:87], v2\n"
    "v_exp_f32 v26, v26\n"
    "v_exp_f32 v27, v27\n"
    "v_exp_f32 v28, v28\n"
    "v_mfma_f32_32x32x16_bf16 v[72:87], a[32:35], v[164:167], 0\n"
    "v_exp_f32 v29, v29\n"
    "v_exp_f32 v30, v30\n"
    "v_exp_f32 v31, v31\n"
    "v_exp_f32 v32, v32\n"
    "v_exp_f32 v33, v33\n"
    "v_smfmac_f32_32x32x32_bf16 v[120:135], a[36:39], v[24:31], v2\n"
    "v_exp_f32 v34, v34\n"
    "v_exp_f32 v35, v35\n"
    "v_exp_f32 v36, v36\n"
    "v_exp_f32 v37, v37\n"
    "v_mfma_f32_32x32x4_2b_bf16 v[56:87], a[54:55], v[170:171], v[56:87]\n"
    "v_exp_f32 v38, v38\n"
    "v_exp_f32 v39, v39\n"
    "v_exp_f32 v40, v40\n"
    "v_exp_f32 v41, v41\n"
    "v_exp_f32 v42, v42\n"
    "v_exp_f32 v43, v43\n"
    "v_exp_f32 v44, v44\n"
    "v_exp_f32 v45, v45\n"
    "v_smfmac_f32_32x32x32_bf16 v[120:135], a[40:43], v[32:39], v2\n"
    "v_exp_f32 v46, v46\n"
    "v_exp_f32 v47, v47\n"
    "v_exp_f32 v48, v48\n"
    "v_exp_f32 v49, v49\n"
    "v_smfmac_f32_32x32x32_bf16 v[136:151], a[36:39], v[40:47], v2\n"
    "s_waitcnt vmcnt(6)\n"
    "v_exp_f32 v50, v50\n"
    "v_exp_f32 v51, v51\n"
    "v_exp_f32 v52, v52\n"
    "v_mfma_f32_32x32x16_bf16 v[24:39], a[56:59], v[152:155], 0\n"
    "v_exp_f32 v53, v53\n"
    "v_exp_f32 v54, v54\n"
    "v_exp_f32 v55, v55\n"
    "v_exp_f32 v56, v56\n"
    "v_exp_f32 v57, v57\n"
    "v_smfmac_f32_32x32x32_bf16 v[136:151], a[40:43], v[48:55], v2\n"
    "s_add_u32 s57, s60, 0x4e00\n"
    "buffer_load_dwordx4 a[84:87], v1, s[24:27], s57 offen offset:0 sc1\n"
    "v_exp_f32 v58, v58\n"
    "v_exp_f32 v59, v59\n"
    "v_exp_f32 v60, v60\n"
    "v_mfma_f32_32x32x16_bf16 v[40:55], a[56:59], v[156:159], 0\n"
    "v_exp_f32 v61, v61\n"
    "v_exp_f32 v62, v62\n"
    "v_exp_f32 v63, v63\n"
    "v_exp_f32 v64, v64\n"
    "v_exp_f32 v65, v65\n"
    "v_smfmac_f32_32x32x32_bf16 v[120:135], a[44:47], v[56:63], v2\n"
    "buffer_load_dwordx4 a[88:91], v1, s[24:27], s57 offen offset:1024 sc1\n"
    "v_exp_f32 v66, v66\n"
    "v_exp_f32 v67, v67\n"
    "v_exp_f32 v68, v68\n"
    "v_exp_f32 v69, v69\n"
    "v_mfma_f32_32x32x4_2b_bf16 v[24:55], a[80:81], v[168:169], v[24:55]\n"
    "v_exp_f32 v70, v70\n"
    "v_exp_f32 v71, v71\n"
    "v_exp_f32 v72, v72\n"
    "v_exp_f32 v73, v73\n"
    "v_exp_f32 v74, v74\n"
    "v_exp_f32 v75, v75\n"
    "v_exp_f32 v76, v76\n"
    "v_exp_f32 v77, v77\n"
    "v_smfmac_f32_32x32x32_bf16 v[120:135], a[48:51], v[64:71], v2\n"
    "buffer_load_dwordx4 a[92:95], v1, s[24:27], s57 offen offset:2048 sc1\n"
    "v_exp_f32 v78, v78\n"
    "v_exp_f32 v79, v79\n"
    "v_exp_f32 v80, v80\n"
    "v_exp_f32 v81, v81\n"
    "v_smfmac_f32_32x32x32_bf16 v[136:151], a[44:47], v[72:79], v2\n"
    "buffer_load_dwordx4 a[96:99], v1, s[24:27], s57 offen offset:3072 sc1\n"
    "v_exp_f32 v82, v82\n"
    "v_exp_f32 v83, v83\n"
    "v_exp_f32 v84, v84\n"
    "v_mfma_f32_32x32x16_bf16 v[56:71], a[60:63], v[152:155], 0\n"
    "s_add_u32 s57, s60, 0x5e00\n"
    "v_exp_f32 v85, v85\n"
    "v_exp_f32 v86, v86\n"
    "v_exp_f32 v87, v87\n"
    "v_exp_f32 v24, v24\n"
    "v_exp_f32 v25, v25\n"
    "v_smfmac_f32_32x32x32_bf16 v[136:151], a[48:51], v[80:87], v2\n"
    "buffer_load_dwordx4 a[108:111], v13, s[24:27], s57 offen offset:2048 sc1\n"
    "v_exp_f32 v26, v26\n"
    "v_exp_f32 v27, v27\n"
    "v_exp_f32 v28, v28\n"
    "v_mfma_f32_32x32x16_bf16 v[72:87], a[60:63], v[156:159], 0\n"
    "v_exp_f32 v29, v29\n"
    "v_exp_f32 v30, v30\n"
    "v_exp_f32 v31, v31\n"
    "v_exp_f32 v32, v32\n"
    "v_exp_f32 v33, v33\n"
    "v_smfmac_f32_32x32x32_bf16 v[88:103], a[64:67], v[24:31], v2\n"
    "buffer_load_dwordx4 a[100:103], v1, s[24:27], s57 offen offset:0 sc1\n"
    "v_exp_f32 v34, v34\n"
    "v_exp_f32 v35, v35\n"
    "v_exp_f32 v36, v36\n"
    "v_exp_f32 v37, v37\n"
    "v_mfma_f32_32x32x4_2b_bf16 v[56:87], a[82:83], v[168:169], v[56:87]\n"
    "v_exp_f32 v38, v38\n"
    "v_exp_f32 v39, v39\n"
    "v_exp_f32 v40, v40\n"
    "v_exp_f32 v41, v41\n"
    "v_exp_f32 v42, v42\n"
    "v_exp_f32 v43, v43\n"
    "v_exp_f32 v44, v44\n"
    "v_exp_f32 v45, v45\n"
    "v_smfmac_f32_32x32x32_bf16 v[88:103], a[68:71], v[32:39], v2\n"
    "buffer_load_dwordx4 a[104:107], v1, s[24:27], s57 offen offset:1024 sc1\n"
    "v_exp_f32 v46, v46\n"
    "v_exp_f32 v47, v47\n"
    "v_exp_f32 v48, v48\n"
    "v_exp_f32 v49, v49\n"
    "v_smfmac_f32_32x32x32_bf16 v[104:119], a[64:67], v[40:47], v2\n"
    "s_add_u32 s57, s60, 0x6800\n"
    "buffer_load_dwordx4 a[112:115], v1, s[24:27], s57 offen offset:0 sc1\n"
    "v_exp_f32 v50, v50\n"
    "v_exp_f32 v51, v51\n"
    "v_exp_f32 v52, v52\n"
    "v_mfma_f32_32x32x16_bf16 v[24:39], a[56:59], v[160:163], 0\n"
    "v_exp_f32 v53, v53\n"
    "v_exp_f32 v54, v54\n"
    "v_exp_f32 v55, v55\n"
    "v_exp_f32 v56, v56\n"
    "v_exp_f32 v57, v57\n"
    "v_smfmac_f32_32x32x32_bf16 v[104:119], a[68:71], v[48:55], v2\n"
    "s_waitcnt vmcnt(9)\n"
    "s_barrier\n"
    "buffer_load_dwordx4 a[116:119], v1, s[24:27], s57 offen offset:1024 sc1\n"
    "ds_read_b64_tr_b16 v[172:173], v233 offset:0\n"
    "ds_read_b64_tr_b16 v[174:175], v233 offset:512\n"
    "v_exp_f32 v58, v58\n"
    "v_exp_f32 v59, v59\n"
    "v_exp_f32 v60, v60\n"
    "v_mfma_f32_32x32x16_bf16 v[40:55], a[56:59], v[164:167], 0\n"
    "ds_read_b64_tr_b16 v[176:177], v234 offset:4096\n"
    "ds_read_b64_tr_b16 v[178:179], v234 offset:4608\n"
    "v_exp_f32 v61, v61\n"
    "v_exp_f32 v62, v62\n"
    "v_exp_f32 v63, v63\n"
    "v_exp_f32 v64, v64\n"
    "v_exp_f32 v65, v65\n"
    "v_smfmac_f32_32x32x32_bf16 v[88:103], a[72:75], v[56:63], v2\n"
    "buffer_load_dwordx4 a[120:123], v1, s[24:27], s57 offen offset:2048 sc1\n"
    "ds_read_b64_tr_b16 v[180:181], v233 offset:8192\n"
    "ds_read_b64_tr_b16 v[182:183], v233 offset:8704\n"
    "v_exp_f32 v66, v66\n"
    "v_exp_f32 v67, v67\n"
    "v_exp_f32 v68, v68\n"
    "v_exp_f32 v69, v69\n"
    "v_mfma_f32_32x32x4_2b_bf16 v[24:55], a[80:81], v[170:171], v[24:55]\n"
    "v_exp_f32 v70, v70\n"
    "v_exp_f32 v71, v71\n"
    "v_exp_f32 v72, v72\n"
    "v_exp_f32 v73, v73\n"
    "v_exp_f32 v74, v74\n"
    "v_exp_f32 v75, v75\n"
    "v_exp_f32 v76, v76\n"
    "v_exp_f32 v77, v77\n"
    "v_smfmac_f32_32x32x32_bf16 v[88:103], a[76:79], v[64:71], v2\n"
    "buffer_load_dwordx4 a[124:127], v1, s[24:27], s57 offen offset:3072 sc1\n"
    "ds_read_b64_tr_b16 v[184:185], v235 offset:12288\n"
    "ds_read_b64_tr_b16 v[186:187], v235 offset:12800\n"
    "v_exp_f32 v78, v78\n"
    "v_exp_f32 v79, v79\n"
    "v_exp_f32 v80, v80\n"
    "v_exp_f32 v81, v81\n"
    "v_smfmac_f32_32x32x32_bf16 v[104:119], a[72:75], v[72:79], v2\n"
    "s_add_u32 s57, s60, 0x7800\n"
    "buffer_load_dwordx4 a[136:139], v13, s[24:27], s57 offen offset:2048 sc1\n"
    "ds_read_b64_tr_b16 v[188:189], v233 offset:16384\n"
    "ds_read_b64_tr_b16 v[190:191], v233 offset:16896\n"
    "v_exp_f32 v82, v82\n"
    "v_exp_f32 v83, v83\n"
    "v_exp_f32 v84, v84\n"
    "v_mfma_f32_32x32x16_bf16 v[56:71], a[60:63], v[160:163], 0\n"
    "s_waitcnt lgkmcnt(0)\n"
    "v_exp_f32 v85, v85\n"
    "v_exp_f32 v86, v86\n"
    "v_exp_f32 v87, v87\n"
    "v_mfma_f32_16x16x32_bf16 v[192:195], v[252:255], v[172:175], 0\n"
    "v_exp_f32 v24, v24\n"
    "v_exp_f32 v25, v25\n"
    "v_smfmac_f32_32x32x32_bf16 v[104:119], a[76:79], v[80:87], v2\n"
    "buffer_load_dwordx4 a[128:131], v1, s[24:27], s57 offen offset:0 sc1\n"
    "v_exp_f32 v26, v26\n"
    "v_exp_f32 v27, v27\n"
    "v_exp_f32 v28, v28\n"
    "v_mfma_f32_32x32x16_bf16 v[72:87], a[60:63], v[164:167], 0\n"
    "v_exp_f32 v29, v29\n"
    "v_exp_f32 v30, v30\n"
    "v_exp_f32 v31, v31\n"
    "v_mfma_f32_16x16x32_bf16 v[192:195], v[252:255], v[176:179], v[192:195]\n"
    "v_exp_f32 v32, v32\n"
    "v_exp_f32 v33, v33\n"
    "v_smfmac_f32_32x32x32_bf16 v[120:135], a[64:67], v[24:31], v2\n"
    "buffer_load_dwordx4 a[132:135], v1, s[24:27], s57 offen offset:1024 sc1\n"
    "v_exp_f32 v34, v34\n"
    "v_exp_f32 v35, v35\n"
    "v_exp_f32 v36, v36\n"
    "v_exp_f32 v37, v37\n"
    "v_mfma_f32_32x32x4_2b_bf16 v[56:87], a[82:83], v[170:171], v[56:87]\n"
    "v_exp_f32 v38, v38\n"
    "v_exp_f32 v39, v39\n"
    "v_exp_f32 v40, v40\n"
    "v_exp_f32 v41, v41\n"
    "v_exp_f32 v42, v42\n"
    "v_exp_f32 v43, v43\n"
    "v_exp_f32 v44, v44\n"
    "v_exp_f32 v45, v45\n"
    "v_smfmac_f32_32x32x32_bf16 v[120:135], a[68:71], v[32:39], v2\n"
    "s_add_u32 s57, s60, 0x8200\n"
    "buffer_load_dwordx4 a[140:143], v1, s[24:27], s57 offen offset:0 sc1\n"
    "v_exp_f32 v46, v46\n"
    "v_exp_f32 v47, v47\n"
    "v_exp_f32 v48, v48\n"
    "v_exp_f32 v49, v49\n"
    "v_smfmac_f32_32x32x32_bf16 v[136:151], a[64:67], v[40:47], v2\n"
    "s_waitcnt vmcnt(8)\n"
    "v_exp_f32 v50, v50\n"
    "v_exp_f32 v51, v51\n"
    "v_exp_f32 v52, v52\n"
    "v_mfma_f32_32x32x16_bf16 v[24:39], a[84:87], v[152:155], 0\n"
    "v_exp_f32 v53, v53\n"
    "v_exp_f32 v54, v54\n"
    "v_exp_f32 v55, v55\n"
    "v_mfma_f32_16x16x32_bf16 v[192:195], v[252:255], v[180:183], v[192:195]\n"
    "v_exp_f32 v56, v56\n"
    "v_exp_f32 v57, v57\n"
    "v_smfmac_f32_32x32x32_bf16 v[136:151], a[68:71], v[48:55], v2\n"
    "buffer_load_dwordx4 a[144:147], v1, s[24:27], s57 offen offset:1024 sc1\n"
    "v_exp_f32 v58, v58\n"
    "v_exp_f32 v59, v59\n"
    "v_exp_f32 v60, v60\n"
    "v_mfma_f32_32x32x16_bf16 v[40:55], a[84:87], v[156:159], 0\n"
    "v_exp_f32 v61, v61\n"
    "v_exp_f32 v62, v62\n"
    "v_exp_f32 v63, v63\n"
    "v_mfma_f32_16x16x32_bf16 v[192:195], v[252:255], v[184:187], v[192:195]\n"
    "v_exp_f32 v64, v64\n"
    "v_exp_f32 v65, v65\n"
    "v_smfmac_f32_32x32x32_bf16 v[120:135], a[72:75], v[56:63], v2\n"
    "buffer_load_dwordx4 a[148:151], v1, s[24:27], s57 offen offset:2048 sc1\n"
    "v_exp_f32 v66, v66\n"
    "v_exp_f32 v67, v67\n"
    "v_exp_f32 v68, v68\n"
    "v_exp_f32 v69, v69\n"
    "v_mfma_f32_32x32x4_2b_bf16 v[24:55], a[108:109], v[168:169], v[24:55]\n"
    "v_exp_f32 v70, v70\n"
    "v_exp_f32 v71, v71\n"
    "v_exp_f32 v72, v72\n"
    "v_exp_f32 v73, v73\n"
    "v_exp_f32 v74, v74\n"
    "v_exp_f32 v75, v75\n"
    "v_exp_f32 v76, v76\n"
    "v_exp_f32 v77, v77\n"
    "v_smfmac_f32_32x32x32_bf16 v[120:135], a[76:79], v[64:71], v2\n"
    "buffer_load_dwordx4 a[152:155], v1, s[24:27], s57 offen offset:3072 sc1\n"
    "v_exp_f32 v78, v78\n"
    "v_exp_f32 v79, v79\n"
    "v_exp_f32 v80, v80\n"
    "v_exp_f32 v81, v81\n"
    "v_smfmac_f32_32x32x32_bf16 v[136:151], a[72:75], v[72:79], v2\n"
    "s_add_u32 s57, s60, 0x9200\n"
    "buffer_load_dwordx4 a[164:167], v13, s[24:27], s57 offen offset:2048 sc1\n"
    "v_exp_f32 v82, v82\n"
    "v_exp_f32 v83, v83\n"
    "v_exp_f32 v84, v84\n"
    "v_mfma_f32_32x32x16_bf16 v[56:71], a[88:91], v[152:155], 0\n"
    "v_exp_f32 v85, v85\n"
    "v_exp_f32 v86, v86\n"
    "v_exp_f32 v87, v87\n"
    "v_mfma_f32_16x16x32_bf16 v[192:195], v[252:255], v[188:191], v[192:195]\n"
    "v_exp_f32 v24, v24\n"
    "v_exp_f32 v25, v25\n"
    "v_smfmac_f32_32x32x32_bf16 v[136:151], a[76:79], v[80:87], v2\n"
    "buffer_store_dwordx4 v[172:175], v1, s[24:27], s86 offen offset:0 sc1\n"
    "v_exp_f32 v26, v26\n"
    "v_exp_f32 v27, v27\n"
    "v_exp_f32 v28, v28\n"
    "v_mfma_f32_32x32x16_bf16 v[72:87], a[88:91], v[156:159], 0\n"
    "v_cvt_pk_bf16_f32 v195, v192, 0\n"
    "v_lshlrev_b32 v193, 16, v195\n"
    "v_sub_f32 v193, v192, v193\n"
    "v_cvt_pk_bf16_f32 v196, v192, v193\n"
    "v_lshl_or_b32 v197, v236, 16, v195\n"
    "v_lshl_or_b32 v198, v236, 16, v236\n"
    "v_exp_f32 v29, v29\n"
    "v_exp_f32 v30, v30\n"
    "v_exp_f32 v31, v31\n"
    "v_exp_f32 v32, v32\n"
    "v_exp_f32 v33, v33\n"
    "v_smfmac_f32_32x32x32_bf16 v[88:103], a[92:95], v[24:31], v2\n"
    "buffer_load_dwordx4 a[156:159], v1, s[24:27], s57 offen offset:0 sc1\n"
    "v_mov_b32 v199, 0\n"
    "v_exp_f32 v34, v34\n"
    "v_exp_f32 v35, v35\n"
    "v_exp_f32 v36, v36\n"
    "v_exp_f32 v37, v37\n"
    "v_mfma_f32_32x32x4_2b_bf16 v[56:87], a[110:111], v[168:169], v[56:87]\n"
    "v_exp_f32 v38, v38\n"
    "v_exp_f32 v39, v39\n"
    "v_exp_f32 v40, v40\n"
    "v_exp_f32 v41, v41\n"
    "v_exp_f32 v42, v42\n"
    "v_exp_f32 v43, v43\n"
    "v_exp_f32 v44, v44\n"
    "v_exp_f32 v45, v45\n"
    "v_smfmac_f32_32x32x32_bf16 v[88:103], a[96:99], v[32:39], v2\n"
    "buffer_load_dwordx4 a[160:163], v1, s[24:27], s57 offen offset:1024 sc1\n"
    "v_exp_f32 v46, v46\n"
    "v_exp_f32 v47, v47\n"
    "v_exp_f32 v48, v48\n"
    "v_exp_f32 v49, v49\n"
    "v_smfmac_f32_32x32x32_bf16 v[104:119], a[92:95], v[40:47], v2\n"
    "s_add_u32 s57, s60, 0x9c00\n"
    "buffer_load_dwordx4 a[168:171], v1, s[24:27], s57 offen offset:0 sc1\n"
    "v_exp_f32 v50, v50\n"
    "v_exp_f32 v51, v51\n"
    "v_exp_f32 v52, v52\n"
    "v_mfma_f32_32x32x16_bf16 v[24:39], a[84:87], v[160:163], 0\n"
    "v_exp_f32 v53, v53\n"
    "v_exp_f32 v54, v54\n"
    "v_exp_f32 v55, v55\n"
    "v_exp_f32 v56, v56\n"
    "v_exp_f32 v57, v57\n"
    "v_smfmac_f32_32x32x32_bf16 v[104:119], a[96:99], v[48:55], v2\n"
    "buffer_store_dwordx4 v[176:179], v1, s[24:27], s87 offen offset:0 sc1\n"
    "v_exp_f32 v58, v58\n"
    "v_exp_f32 v59, v59\n"
    "v_exp_f32 v60, v60\n"
    "v_mfma_f32_32x32x16_bf16 v[40:55], a[84:87], v[164:167], 0\n"
    "v_exp_f32 v61, v61\n"
    "v_exp_f32 v62, v62\n"
    "v_exp_f32 v63, v63\n"
    "v_exp_f32 v64, v64\n"
    "v_exp_f32 v65, v65\n"
    "v_smfmac_f32_32x32x32_bf16 v[88:103], a[100:103], v[56:63], v2\n"
    "buffer_load_dwordx4 a[172:175], v1, s[24:27], s57 offen offset:1024 sc1\n"
    "v_exp_f32 v66, v66\n"
    "v_exp_f32 v67, v67\n"
    "v_exp_f32 v68, v68\n"
    "v_exp_f32 v69, v69\n"
    "v_mfma_f32_32x32x4_2b_bf16 v[24:55], a[108:109], v[170:171], v[24:55]\n"
    "v_exp_f32 v70, v70\n"
    "v_exp_f32 v71, v71\n"
    "v_exp_f32 v72, v72\n"
    "v_exp_f32 v73, v73\n"
    "v_exp_f32 v74, v74\n"
    "v_exp_f32 v75, v75\n"
    "v_exp_f32 v76, v76\n"
    "v_exp_f32 v77, v77\n"
    "v_smfmac_f32_32x32x32_bf16 v[88:103], a[104:107], v[64:71], v2\n"
    "buffer_load_dwordx4 a[176:179], v1, s[24:27], s57 offen offset:2048 sc1\n"
    "v_exp_f32 v78, v78\n"
    "v_exp_f32 v79, v79\n"
    "v_exp_f32 v80, v80\n"
    "v_exp_f32 v81, v81\n"
    "v_smfmac_f32_32x32x32_bf16 v[104:119], a[100:103], v[72:79], v2\n"
    "buffer_load_dwordx4 a[180:183], v1, s[24:27], s57 offen offset:3072 sc1\n"
    "v_exp_f32 v82, v82\n"
    "v_exp_f32 v83, v83\n"
    "v_exp_f32 v84, v84\n"
    "v_mfma_f32_32x32x16_bf16 v[56:71], a[88:91], v[160:163], 0\n"
    "s_add_u32 s57, s60, 0xac00\n"
    "v_exp_f32 v85, v85\n"
    "v_exp_f32 v86, v86\n"
    "v_exp_f32 v87, v87\n"
    "v_exp_f32 v24, v24\n"
    "v_exp_f32 v25, v25\n"
    "v_smfmac_f32_32x32x32_bf16 v[104:119], a[104:107], v[80:87], v2\n"
    "buffer_store_dwordx4 v[180:183], v1, s[24:27], s88 offen offset:0 sc1\n"
    "v_exp_f32 v26, v26\n"
    "v_exp_f32 v27, v27\n"
    "v_exp_f32 v28, v28\n"
    "v_mfma_f32_32x32x16_bf16 v[72:87], a[88:91], v[164:167], 0\n"
    "v_exp_f32 v29, v29\n"
    "v_exp_f32 v30, v30\n"
    "v_exp_f32 v31, v31\n"
    "v_exp_f32 v32, v32\n"
    "v_exp_f32 v33, v33\n"
    "v_smfmac_f32_32x32x32_bf16 v[120:135], a[92:95], v[24:31], v2\n"
    "buffer_load_dwordx4 a[192:195], v13, s[24:27], s57 offen offset:2048 sc1\n"
    "v_exp_f32 v34, v34\n"
    "v_exp_f32 v35, v35\n"
    "v_exp_f32 v36, v36\n"
    "v_exp_f32 v37, v37\n"
    "v_mfma_f32_32x32x4_2b_bf16 v[56:87], a[110:111], v[170:171], v[56:87]\n"
    "v_exp_f32 v38, v38\n"
    "v_exp_f32 v39, v39\n"
    "v_exp_f32 v40, v40\n"
    "v_exp_f32 v41, v41\n"
    "v_exp_f32 v42, v42\n"
    "v_exp_f32 v43, v43\n"
    "v_exp_f32 v44, v44\n"
    "v_exp_f32 v45, v45\n"
    "v_smfmac_f32_32x32x32_bf16 v[120:135], a[96:99], v[32:39], v2\n"
    "buffer_load_dwordx4 a[184:187], v1, s[24:27], s57 offen offset:0 sc1\n"
    "v_exp_f32 v46, v46\n"
    "v_exp_f32 v47, v47\n"
    "v_exp_f32 v48, v48\n"
    "v_exp_f32 v49, v49\n"
    "v_smfmac_f32_32x32x32_bf16 v[136:151], a[92:95], v[40:47], v2\n"
    "s_waitcnt vmcnt(16)\n"
    "v_exp_f32 v50, v50\n"
    "v_exp_f32 v51, v51\n"
    "v_exp_f32 v52, v52\n"
    "v_mfma_f32_32x32x16_bf16 v[24:39], a[112:115], v[152:155], 0\n"
    "buffer_load_dwordx4 a[188:191], v1, s[24:27], s57 offen offset:1024 sc1\n"
    "v_exp_f32 v53, v53\n"
    "v_exp_f32 v54, v54\n"
    "v_exp_f32 v55, v55\n"
    "v_exp_f32 v56, v56\n"
    "v_exp_f32 v57, v57\n"
    "v_smfmac_f32_32x32x32_bf16 v[136:151], a[96:99], v[48:55], v2\n"
    "Lhop3:\n"
    "s_cbranch_execz LhopA\n"
    "buffer_store_dwordx4 v[184:187], v1, s[24:27], s89 offen offset:0 sc1\n"
    "v_exp_f32 v58, v58\n"
    "v_exp_f32 v59, v59\n"
    "v_exp_f32 v60, v60\n"
    "v_mfma_f32_32x32x16_bf16 v[40:55], a[112:115], v[156:159], 0\n"
    "s_add_u32 s57, s60, 0xb600\n"
    "v_exp_f32 v61, v61\n"
    "v_exp_f32 v62, v62\n"
    "v_exp_f32 v63, v63\n"
    "v_exp_f32 v64, v64\n"
    "v_exp_f32 v65, v65\n"
    "v_smfmac_f32_32x32x32_bf16 v[120:135], a[100:103], v[56:63], v2\n"
    "buffer_load_dwordx4 a[196:199], v1, s[24:27], s57 offen offset:0 sc1\n"
    "v_exp_f32 v66, v66\n"
    "v_exp_f32 v67, v67\n"
    "v_exp_f32 v68, v68\n"
    "v_exp_f32 v69, v69\n"
    "v_mfma_f32_32x32x4_2b_bf16 v[24:55], a[136:137], v[168:169], v[24:55]\n"
    "v_exp_f32 v70, v70\n"
    "v_exp_f32 v71, v71\n"
    "v_exp_f32 v72, v72\n"
    "v_exp_f32 v73, v73\n"
    "v_exp_f32 v74, v74\n"
    "v_exp_f32 v75, v75\n"
    "v_exp_f32 v76, v76\n"
    "v_exp_f32 v77, v77\n"
    "v_smfmac_f32_32x32x32_bf16 v[120:135], a[104:107], v[64:71], v2\n"
    "buffer_load_dwordx4 a[200:203], v1, s[24:27], s57 offen offset:1024 sc1\n"
    "v_exp_f32 v78, v78\n"
    "v_exp_f32 v79, v79\n"
    "v_exp_f32 v80, v80\n"
    "v_exp_f32 v81, v81\n"
    "v_smfmac_f32_32x32x32_bf16 v[136:151], a[100:103], v[72:79], v2\n"
    "buffer_load_dwordx4 a[204:207], v1, s[24:27], s57 offen offset:2048 sc1\n"
    "v_exp_f32 v82, v82\n"
    "v_exp_f32 v83, v83\n"
    "v_exp_f32 v84, v84\n"
    "v_mfma_f32_32x32x16_bf16 v[56:71], a[116:119], v[152:155], 0\n"
    "v_exp_f32 v85, v85\n"
    "v_exp_f32 v86, v86\n"
    "v_exp_f32 v87, v87\n"
    "v_exp_f32 v24, v24\n"
    "v_exp_f32 v25, v25\n"
    "v_smfmac_f32_32x32x32_bf16 v[136:151], a[104:107], v[80:87], v2\n"
    "buffer_store_dwordx4 v[188:191], v1, s[24:27], s90 offen offset:0 sc1\n"
    "v_exp_f32 v26, v26\n"
    "v_exp_f32 v27, v27\n"
    "v_exp_f32 v28, v28\n"
    "v_mfma_f32_32x32x16_bf16 v[72:87], a[116:119], v[156:159], 0\n"
    "v_exp_f32 v29, v29\n"
    "v_exp_f32 v30, v30\n"
    "v_exp_f32 v31, v31\n"
    "v_exp_f32 v32, v32\n"
    "v_exp_f32 v33, v33\n"
    "v_smfmac_f32_32x32x32_bf16 v[88:103], a[120:123], v[24:31], v2\n"
    "buffer_load_dwordx4 a[208:211], v1, s[24:27], s57 offen offset:3072 sc1\n"
    "v_exp_f32 v34, v34\n"
    "v_exp_f32 v35, v35\n"
    "v_exp_f32 v36, v36\n"
    "v_exp_f32 v37, v37\n"
    "v_mfma_f32_32x32x4_2b_bf16 v[56:87], a[138:139], v[168:169], v[56:87]\n"
    "v_exp_f32 v38, v38\n"
    "v_exp_f32 v39, v39\n"
    "v_exp_f32 v40, v40\n"
    "v_exp_f32 v41, v41\n"
    "v_exp_f32 v42, v42\n"
    "v_exp_f32 v43, v43\n"
    "v_exp_f32 v44, v44\n"
    "v_exp_f32 v45, v45\n"
    "v_smfmac_f32_32x32x32_bf16 v[88:103], a[124:127], v[32:39], v2\n"
    "s_add_u32 s57, s60, 0xc600\n"
    "buffer_load_dwordx4 a[220:223], v13, s[24:27], s57 offen offset:2048 sc1\n"
    "v_exp_f32 v46, v46\n"
    "v_exp_f32 v47, v47\n"
    "v_exp_f32 v48, v48\n"
    "v_exp_f32 v49, v49\n"
    "v_smfmac_f32_32x32x32_bf16 v[104:119], a[120:123], v[40:47], v2\n"
    "buffer_load_dwordx4 a[212:215], v1, s[24:27], s57 offen offset:0 sc1\n"
    "v_exp_f32 v50, v50\n"
    "v_exp_f32 v51, v51\n"
    "v_exp_f32 v52, v52\n"
    "v_mfma_f32_32x32x16_bf16 v[24:39], a[112:115], v[160:163], 0\n"
    "v_exp_f32 v53, v53\n"
    "v_exp_f32 v54, v54\n"
    "v_exp_f32 v55, v55\n"
    "v_exp_f32 v56, v56\n"
    "v_exp_f32 v57, v57\n"
    "v_smfmac_f32_32x32x32_bf16 v[104:119], a[124:127], v[48:55], v2\n"
    "buffer_store_dwordx4 v[196:199], v15, s[24:27], s91 offen offset:0 sc1\n"
    "v_mov_b32 v240, -1\n"
    "v_mov_b32 v241, -1\n"
    "v_mov_b32 v242, -1\n"
    "v_mov_b32 v243, -1\n"
    "v_exp_f32 v58, v58\n"
    "v_exp_f32 v59, v59\n"
    "v_exp_f32 v60, v60\n"
    "v_mfma_f32_32x32x16_bf16 v[40:55], a[112:115], v[164:167], 0\n"
    "v_exp_f32 v61, v61\n"
    "v_exp_f32 v62, v62\n"
    "v_exp_f32 v63, v63\n"
    "v_exp_f32 v64, v64\n"
    "v_exp_f32 v65, v65\n"
    "v_smfmac_f32_32x32x32_bf16 v[88:103], a[128:131], v[56:63], v2\n"
    "buffer_load_dwordx4 a[216:219], v1, s[24:27], s57 offen offset:1024 sc1\n"
    "v_exp_f32 v66, v66\n"
    "v_exp_f32 v67, v67\n"
    "v_exp_f32 v68, v68\n"
    "v_exp_f32 v69, v69\n"
    "v_mfma_f32_32x32x4_2b_bf16 v[24:55], a[136:137], v[170:171], v[24:55]\n"
    "v_exp_f32 v70, v70\n"
    "v_exp_f32 v71, v71\n"
    "v_exp_f32 v72, v72\n"
    "v_exp_f32 v73, v73\n"
    "v_exp_f32 v74, v74\n"
    "v_exp_f32 v75, v75\n"
    "v_exp_f32 v76, v76\n"
    "v_exp_f32 v77, v77\n"
    "v_smfmac_f32_32x32x32_bf16 v[88:103], a[132:135], v[64:71], v2\n"
    "v_exp_f32 v78, v78\n"
    "v_exp_f32 v79, v79\n"
    "v_exp_f32 v80, v80\n"
    "v_exp_f32 v81, v81\n"
    "v_smfmac_f32_32x32x32_bf16 v[104:119], a[128:131], v[72:79], v2\n"
    "v_exp_f32 v82, v82\n"
    "v_exp_f32 v83, v83\n"
    "v_exp_f32 v84, v84\n"
    "v_mfma_f32_32x32x16_bf16 v[56:71], a[116:119], v[160:163], 0\n"
    "v_exp_f32 v85, v85\n"
    "v_exp_f32 v86, v86\n"
    "v_exp_f32 v87, v87\n"
    "v_exp_f32 v24, v24\n"
    "v_exp_f32 v25, v25\n"
    "v_smfmac_f32_32x32x32_bf16 v[104:119], a[132:135], v[80:87], v2\n"
    "buffer_store_dwordx4 v[240:243], v5, s[24:27], s61 offen offset:0 sc1\n"
    "v_exp_f32 v26, v26\n"
    "v_exp_f32 v27, v27\n"
    "v_exp_f32 v28, v28\n"
    "v_mfma_f32_32x32x16_bf16 v[72:87], a[116:119], v[164:167], 0\n"
    "v_exp_f32 v29, v29\n"
    "v_exp_f32 v30, v30\n"
    "v_exp_f32 v31, v31\n"
    "v_exp_f32 v32, v32\n"
    "v_exp_f32 v33, v33\n"
    "v_smfmac_f32_32x32x32_bf16 v[120:135], a[120:123], v[24:31], v2\n"
    "v_exp_f32 v34, v34\n"
    "v_exp_f32 v35, v35\n"
    "v_exp_f32 v36, v36\n"
    "v_exp_f32 v37, v37\n"
    "v_mfma_f32_32x32x4_2b_bf16 v[56:87], a[138:139], v[170:171], v[56:87]\n"
    "v_exp_f32 v38, v38\n"
    "v_exp_f32 v39, v39\n"
    "v_exp_f32 v40, v40\n"
    "v_exp_f32 v41, v41\n"
    "v_exp_f32 v42, v42\n"
    "v_exp_f32 v43, v43\n"
    "v_exp_f32 v44, v44\n"
    "v_exp_f32 v45, v45\n"
    "v_smfmac_f32_32x32x32_bf16 v[120:135], a[124:127], v[32:39], v2\n"
    "v_exp_f32 v46, v46\n"
    "v_exp_f32 v47, v47\n"
    "v_exp_f32 v48, v48\n"
    "v_exp_f32 v49, v49\n"
    "v_smfmac_f32_32x32x32_bf16 v[136:151], a[120:123], v[40:47], v2\n"
    "s_waitcnt vmcnt(20)\n"
    "v_exp_f32 v50, v50\n"
    "v_exp_f32 v51, v51\n"
    "v_exp_f32 v52, v52\n"
    "v_mfma_f32_32x32x16_bf16 v[24:39], a[140:143], v[152:155], 0\n"
    "v_exp_f32 v53, v53\n"
    "v_exp_f32 v54, v54\n"
    "v_exp_f32 v55, v55\n"
    "v_exp_f32 v56, v56\n"
    "v_exp_f32 v57, v57\n"
    "v_smfmac_f32_32x32x32_bf16 v[136:151], a[124:127], v[48:55], v2\n"
    "v_exp_f32 v58, v58\n"
    "v_exp_f32 v59, v59\n"
    "v_exp_f32 v60, v60\n"
    "v_mfma_f32_32x32x16_bf16 v[40:55], a[140:143], v[156:159], 0\n"
    "v_exp_f32 v61, v61\n"
    "v_exp_f32 v62, v62\n"
    "v_exp_f32 v63, v63\n"
    "v_exp_f32 v64, v64\n"
    "v_exp_f32 v65, v65\n"
    "v_smfmac_f32_32x32x32_bf16 v[120:135], a[128:131], v[56:63], v2\n"
    "v_exp_f32 v66, v66\n"
    "v_exp_f32 v67, v67\n"
    "v_exp_f32 v68, v68\n"
    "v_exp_f32 v69, v69\n"
    "v_mfma_f32_32x32x4_2b_bf16 v[24:55], a[164:165], v[168:169], v[24:55]\n"
    "v_exp_f32 v70, v70\n"
    "v_exp_f32 v71, v71\n"
    "v_exp_f32 v72, v72\n"
    "v_exp_f32 v73, v73\n"
    "v_exp_f32 v74, v74\n"
    "v_exp_f32 v75, v75\n"
    "v_exp_f32 v76, v76\n"
    "v_exp_f32 v77, v77\n"
    "v_smfmac_f32_32x32x32_bf16 v[120:135], a[132:135], v[64:71], v2\n"
    "buffer_store_dwordx2 v[240:241], v6, s[24:27], s62 offen offset:0 sc1\n"
    "v_exp_f32 v78, v78\n"
    "v_exp_f32 v79, v79\n"
    "v_exp_f32 v80, v80\n"
    "v_exp_f32 v81, v81\n"
    "v_smfmac_f32_32x32x32_bf16 v[136:151], a[128:131], v[72:79], v2\n"
    "s_add_u32 s58, s61, 0x2800\n"
    "v_exp_f32 v82, v82\n"
    "v_exp_f32 v83, v83\n"
    "v_exp_f32 v84, v84\n"
    "v_mfma_f32_32x32x16_bf16 v[56:71], a[144:147], v[152:155], 0\n"
    "v_exp_f32 v85, v85\n"
    "v_exp_f32 v86, v86\n"
    "v_exp_f32 v87, v87\n"
    "v_exp_f32 v24, v24\n"
    "v_exp_f32 v25, v25\n"
    "v_smfmac_f32_32x32x32_bf16 v[136:151], a[132:135], v[80:87], v2\n"
    "v_exp_f32 v26, v26\n"
    "v_exp_f32 v27, v27\n"
    "v_exp_f32 v28, v28\n"
    "v_mfma_f32_32x32x16_bf16 v[72:87], a[144:147], v[156:159], 0\n"
    "v_exp_f32 v29, v29\n"
    "v_exp_f32 v30, v30\n"
    "v_exp_f32 v31, v31\n"
    "v_exp_f32 v32, v32\n"
    "v_exp_f32 v33, v33\n"
    "v_smfmac_f32_32x32x32_bf16 v[88:103], a[148:151], v[24:31], v2\n"
    "v_exp_f32 v34, v34\n"
    "v_exp_f32 v35, v35\n"
    "v_exp_f32 v36, v36\n"
    "v_exp_f32 v37, v37\n"
    "v_mfma_f32_32x32x4_2b_bf16 v[56:87], a[166:167], v[168:169], v[56:87]\n"
    "v_exp_f32 v38, v38\n"
    "v_exp_f32 v39, v39\n"
    "v_exp_f32 v40, v40\n"
    "v_exp_f32 v41, v41\n"
    "v_exp_f32 v42, v42\n"
    "v_exp_f32 v43, v43\n"
    "v_exp_f32 v44, v44\n"
    "v_exp_f32 v45, v45\n"
    "v_smfmac_f32_32x32x32_bf16 v[88:103], a[152:155], v[32:39], v2\n"
    "v_exp_f32 v46, v46\n"
    "v_exp_f32 v47, v47\n"
    "v_exp_f32 v48, v48\n"
    "v_exp_f32 v49, v49\n"
    "v_smfmac_f32_32x32x32_bf16 v[104:119], a[148:151], v[40:47], v2\n"
    "v_exp_f32 v50, v50\n"
    "v_exp_f32 v51, v51\n"
    "v_exp_f32 v52, v52\n"
    "v_mfma_f32_32x32x16_bf16 v[24:39], a[140:143], v[160:163], 0\n"
    "v_exp_f32 v53, v53\n"
    "v_exp_f32 v54, v54\n"
    "v_exp_f32 v55, v55\n"
    "v_exp_f32 v56, v56\n"
    "v_exp_f32 v57, v57\n"
    "v_smfmac_f32_32x32x32_bf16 v[104:119], a[152:155], v[48:55], v2\n"
    "s_lshl_b32 s54, s40, 3\n"
    "s_add_u32 s54, s54, 0x10000\n"
    "v_exp_f32 v58, v58\n"
    "v_exp_f32 v59, v59\n"
    "v_exp_f32 v60, v60\n"
    "v_mfma_f32_32x32x16_bf16 v[40:55], a[140:143], v[164:167], 0\n"
    "s_waitcnt vmcnt(3)\n"
    "v_exp_f32 v61, v61\n"
    "v_exp_f32 v62, v62\n"
    "v_exp_f32 v63, v63\n"
    "v_exp_f32 v64, v64\n"
    "v_exp_f32 v65, v65\n"
    "v_smfmac_f32_32x32x32_bf16 v[88:103], a[156:159], v[56:63], v2\n"
    "s_barrier\n"
    "buffer_store_dwordx4 v[240:243], v5, s[24:27], s58 offen offset:0 sc1\n"
    "v_mov_b32 v16, s6\n"
    "v_mov_b32 v17, s7\n"
    "v_exp_f32 v66, v66\n"
    "v_exp_f32 v67, v67\n"
    "v_exp_f32 v68, v68\n"
    "v_exp_f32 v69, v69\n"
    "v_mfma_f32_32x32x4_2b_bf16 v[24:55], a[164:165], v[170:171], v[24:55]\n"
    "v_exp_f32 v70, v70\n"
    "v_exp_f32 v71, v71\n"
    "v_exp_f32 v72, v72\n"
    "v_exp_f32 v73, v73\n"
    "v_exp_f32 v74, v74\n"
    "v_exp_f32 v75, v75\n"
    "v_exp_f32 v76, v76\n"
    "v_exp_f32 v77, v77\n"
    "v_smfmac_f32_32x32x32_bf16 v[88:103], a[160:163], v[64:71], v2\n"
    "buffer_store_dwordx2 v[16:17], v14, s[24:27], s54 offen offset:0 sc1\n"
    "s_add_u32 s58, s62, 0x2800\n"
    "v_lshrrev_b32 v19, 8, v1\n"
    "v_lshlrev_b32 v18, 4, v19\n"
    "v_lshlrev_b32 v19, 3, v19\n"
    "v_exp_f32 v78, v78\n"
    "v_exp_f32 v79, v79\n"
    "v_exp_f32 v80, v80\n"
    "v_exp_f32 v81, v81\n"
    "v_smfmac_f32_32x32x32_bf16 v[104:119], a[156:159], v[72:79], v2\n"
    "buffer_load_dwordx4 v[222:225], v18, s[36:39], s82 offen\n"
    "v_exp_f32 v82, v82\n"
    "v_exp_f32 v83, v83\n"
    "v_exp_f32 v84, v84\n"
    "v_mfma_f32_32x32x16_bf16 v[56:71], a[144:147], v[160:163], 0\n"
    "buffer_load_dwordx4 v[226:229], v18, s[36:39], s82 offen offset:64\n"
    "v_exp_f32 v85, v85\n"
    "v_exp_f32 v86, v86\n"
    "v_exp_f32 v87, v87\n"
    "v_exp_f32 v24, v24\n"
    "v_exp_f32 v25, v25\n"
    "v_smfmac_f32_32x32x32_bf16 v[104:119], a[160:163], v[80:87], v2\n"
    "buffer_load_dwordx2 v[230:231], v19, s[36:39], s83 offen\n"
    "v_exp_f32 v26, v26\n"
    "v_exp_f32 v27, v27\n"
    "v_exp_f32 v28, v28\n"
    "v_mfma_f32_32x32x16_bf16 v[72:87], a[144:147], v[164:167], 0\n"
    "s_min_u32 s54, s40, 1\n"
    "v_exp_f32 v29, v29\n"
    "v_exp_f32 v30, v30\n"
    "v_exp_f32 v31, v31\n"
    "v_exp_f32 v32, v32\n"
    "v_exp_f32 v33, v33\n"
    "v_smfmac_f32_32x32x32_bf16 v[120:135], a[148:151], v[24:31], v2\n"
    "s_lshl_b32 s54, s54, 30\n"
    "v_or_b32 v18, s54, v14\n"
    "v_mov_b32 v16, s6\n"
    "v_mov_b32 v17, s7\n"
    "v_exp_f32 v34, v34\n"
    "v_exp_f32 v35, v35\n"
    "v_exp_f32 v36, v36\n"
    "v_exp_f32 v37, v37\n"
    "v_mfma_f32_32x32x4_2b_bf16 v[56:87], a[166:167], v[170:171], v[56:87]\n"
    "v_exp_f32 v38, v38\n"
    "v_exp_f32 v39, v39\n"
    "v_exp_f32 v40, v40\n"
    "v_exp_f32 v41, v41\n"
    "v_exp_f32 v42, v42\n"
    "v_exp_f32 v43, v43\n"
    "v_exp_f32 v44, v44\n"
    "v_exp_f32 v45, v45\n"
    "v_smfmac_f32_32x32x32_bf16 v[120:135], a[152:155], v[32:39], v2\n"
    "buffer_store_dwordx2 v[16:17], v18, s[24:27], 0 offen offset:0 sc1\n"
    "v_exp_f32 v46, v46\n"
    "v_exp_f32 v47, v47\n"
    "v_exp_f32 v48, v48\n"
    "v_exp_f32 v49, v49\n"
    "v_smfmac_f32_32x32x32_bf16 v[136:151], a[148:151], v[40:47], v2\n"
    "v_exp_f32 v50, v50\n"
    "v_exp_f32 v51, v51\n"
    "v_exp_f32 v52, v52\n"
    "v_mfma_f32_32x32x16_bf16 v[24:39], a[168:171], v[152:155], 0\n"
    "buffer_store_dwordx2 v[240:241], v6, s[24:27], s58 offen offset:0 sc1\n"
    "v_exp_f32 v53, v53\n"
    "v_exp_f32 v54, v54\n"
    "v_exp_f32 v55, v55\n"
    "v_exp_f32 v56, v56\n"
    "v_exp_f32 v57, v57\n"
    "v_smfmac_f32_32x32x32_bf16 v[136:151], a[152:155], v[48:55], v2\n"
    "v_exp_f32 v58, v58\n"
    "v_exp_f32 v59, v59\n"
    "v_exp_f32 v60, v60\n"
    "v_mfma_f32_32x32x16_bf16 v[40:55], a[168:171], v[156:159], 0\n"
    "v_exp_f32 v61, v61\n"
    "v_exp_f32 v62, v62\n"
    "v_exp_f32 v63, v63\n"
    "v_exp_f32 v64, v64\n"
    "v_exp_f32 v65, v65\n"
    "v_smfmac_f32_32x32x32_bf16 v[120:135], a[156:159], v[56:63], v2\n"
    "v_exp_f32 v66, v66\n"
    "v_exp_f32 v67, v67\n"
    "v_exp_f32 v68, v68\n"
    "v_exp_f32 v69, v69\n"
    "v_mfma_f32_32x32x4_2b_bf16 v[24:55], a[192:193], v[168:169], v[24:55]\n"
    "v_exp_f32 v70, v70\n"
    "v_exp_f32 v71, v71\n"
    "v_exp_f32 v72, v72\n"
    "v_exp_f32 v73, v73\n"
    "v_exp_f32 v74, v74\n"
    "v_exp_f32 v75, v75\n"
    "v_exp_f32 v76, v76\n"
    "v_exp_f32 v77, v77\n"
    "v_smfmac_f32_32x32x32_bf16 v[120:135], a[160:163], v[64:71], v2\n"
    "v_exp_f32 v78, v78\n"
    "v_exp_f32 v79, v79\n"
    "v_exp_f32 v80, v80\n"
    "v_exp_f32 v81, v81\n"
    "v_smfmac_f32_32x32x32_bf16 v[136:151], a[156:159], v[72:79], v2\n"
    "v_exp_f32 v82, v82\n"
    "v_exp_f32 v83, v83\n"
    "v_exp_f32 v84, v84\n"
    "v_mfma_f32_32x32x16_bf16 v[56:71], a[172:175], v[152:155], 0\n"
    "v_exp_f32 v85, v85\n"
    "v_exp_f32 v86, v86\n"
    "v_exp_f32 v87, v87\n"
    "v_exp_f32 v24, v24\n"
    "v_exp_f32 v25, v25\n"
    "v_smfmac_f32_32x32x32_bf16 v[136:151], a[160:163], v[80:87], v2\n"
    "v_exp_f32 v26, v26\n"
    "v_exp_f32 v27, v27\n"
    "v_exp_f32 v28, v28\n"
    "v_mfma_f32_32x32x16_bf16 v[72:87], a[172:175], v[156:159], 0\n"
    "v_exp_f32 v29, v29\n"
    "v_exp_f32 v30, v30\n"
    "v_exp_f32 v31, v31\n"
    "v_exp_f32 v32, v32\n"
    "v_exp_f32 v33, v33\n"
    "v_smfmac_f32_32x32x32_bf16 v[88:103], a[176:179], v[24:31], v2\n"
    "v_exp_f32 v34, v34\n"
    "v_exp_f32 v35, v35\n"
    "v_exp_f32 v36, v36\n"
    "v_exp_f32 v37, v37\n"
    "v_mfma_f32_32x32x4_2b_bf16 v[56:87], a[194:195], v[168:169], v[56:87]\n"
    "v_exp_f32 v38, v38\n"
    "v_exp_f32 v39, v39\n"
    "v_exp_f32 v40, v40\n"
    "v_exp_f32 v41, v41\n"
    "v_exp_f32 v42, v42\n"
    "v_exp_f32 v43, v43\n"
    "v_exp_f32 v44, v44\n"
    "v_exp_f32 v45, v45\n"
    "v_smfmac_f32_32x32x32_bf16 v[88:103], a[180:183], v[32:39], v2\n"
    "buffer_store_dwordx4 v[240:243], v1, s[24:27], s65 offen offset:0 sc1\n"
    "v_exp_f32 v46, v46\n"
    "v_exp_f32 v47, v47\n"
    "v_exp_f32 v48, v48\n"
    "v_exp_f32 v49, v49\n"
    "v_smfmac_f32_32x32x32_bf16 v[104:119], a[176:179], v[40:47], v2\n"
    "s_waitcnt vmcnt(10)\n"
    "v_exp_f32 v50, v50\n"
    "v_exp_f32 v51, v51\n"
    "v_exp_f32 v52, v52\n"
    "v_mfma_f32_32x32x16_bf16 v[24:39], a[196:199], v[152:155], 0\n"
    "s_add_u32 s58, s65, 0x2000\n"
    "v_exp_f32 v53, v53\n"
    "v_exp_f32 v54, v54\n"
    "v_exp_f32 v55, v55\n"
    "v_exp_f32 v56, v56\n"
    "v_exp_f32 v57, v57\n"
    "v_smfmac_f32_32x32x32_bf16 v[104:119], a[180:183], v[48:55], v2\n"
    "v_exp_f32 v58, v58\n"
    "v_exp_f32 v59, v59\n"
    "v_exp_f32 v60, v60\n"
    "v_mfma_f32_32x32x16_bf16 v[40:55], a[196:199], v[156:159], 0\n"
    "v_exp_f32 v61, v61\n"
    "v_exp_f32 v62, v62\n"
    "v_exp_f32 v63, v63\n"
    "v_exp_f32 v64, v64\n"
    "v_exp_f32 v65, v65\n"
    "v_smfmac_f32_32x32x32_bf16 v[88:103], a[184:187], v[56:63], v2\n"
    "v_exp_f32 v66, v66\n"
    "v_exp_f32 v67, v67\n"
    "v_exp_f32 v68, v68\n"
    "v_exp_f32 v69, v69\n"
    "v_mfma_f32_32x32x4_2b_bf16 v[24:55], a[220:221], v[168:169], v[24:55]\n"
    "v_exp_f32 v70, v70\n"
    "v_exp_f32 v71, v71\n"
    "v_exp_f32 v72, v72\n"
    "v_exp_f32 v73, v73\n"
    "v_exp_f32 v74, v74\n"
    "v_exp_f32 v75, v75\n"
    "v_exp_f32 v76, v76\n"
    "v_exp_f32 v77, v77\n"
    "v_smfmac_f32_32x32x32_bf16 v[88:103], a[188:191], v[64:71], v2\n"
    "v_exp_f32 v78, v78\n"
    "v_exp_f32 v79, v79\n"
    "v_exp_f32 v80, v80\n"
    "v_exp_f32 v81, v81\n"
    "v_smfmac_f32_32x32x32_bf16 v[104:119], a[184:187], v[72:79], v2\n"
    "v_exp_f32 v82, v82\n"
    "v_exp_f32 v83, v83\n"
    "v_exp_f32 v84, v84\n"
    "v_mfma_f32_32x32x16_bf16 v[56:71], a[200:203], v[152:155], 0\n"
    "v_exp_f32 v85, v85\n"
    "v_exp_f32 v86, v86\n"
    "v_exp_f32 v87, v87\n"
    "v_exp_f32 v24, v24\n"
    "v_exp_f32 v25, v25\n"
    "v_smfmac_f32_32x32x32_bf16 v[104:119], a[188:191], v[80:87], v2\n"
    "buffer_store_dwordx4 v[240:243], v1, s[24:27], s58 offen offset:0 sc1\n"
    "v_exp_f32 v26, v26\n"
    "v_exp_f32 v27, v27\n"
    "v_exp_f32 v28, v28\n"
    "v_mfma_f32_32x32x16_bf16 v[72:87], a[200:203], v[156:159], 0\n"
    "v_exp_f32 v29, v29\n"
    "v_exp_f32 v30, v30\n"
    "v_exp_f32 v31, v31\n"
    "v_exp_f32 v32, v32\n"
    "v_exp_f32 v33, v33\n"
    "v_smfmac_f32_32x32x32_bf16 v[88:103], a[204:207], v[24:31], v2\n"
    "v_exp_f32 v34, v34\n"
    "v_exp_f32 v35, v35\n"
    "v_exp_f32 v36, v36\n"
    "v_exp_f32 v37, v37\n"
    "v_mfma_f32_32x32x4_2b_bf16 v[56:87], a[222:223], v[168:169], v[56:87]\n"
    "v_exp_f32 v38, v38\n"
    "v_exp_f32 v39, v39\n"
    "v_exp_f32 v40, v40\n"
    "v_exp_f32 v41, v41\n"
    "v_exp_f32 v42, v42\n"
    "v_exp_f32 v43, v43\n"
    "v_exp_f32 v44, v44\n"
    "v_exp_f32 v45, v45\n"
    "v_smfmac_f32_32x32x32_bf16 v[88:103], a[208:211], v[32:39], v2\n"
    "v_exp_f32 v46, v46\n"
    "v_exp_f32 v47, v47\n"
    "v_exp_f32 v48, v48\n"
    "v_exp_f32 v49, v49\n"
    "v_smfmac_f32_32x32x32_bf16 v[104:119], a[204:207], v[40:47], v2\n"
    "v_exp_f32 v50, v50\n"
    "v_exp_f32 v51, v51\n"
    "v_exp_f32 v52, v52\n"
    "v_mfma_f32_32x32x16_bf16 v[24:39], a[168:171], v[160:163], 0\n"
    "v_exp_f32 v53, v53\n"
    "v_exp_f32 v54, v54\n"
    "v_exp_f32 v55, v55\n"
    "v_exp_f32 v56, v56\n"
    "v_exp_f32 v57, v57\n"
    "v_smfmac_f32_32x32x32_bf16 v[104:119], a[208:211], v[48:55], v2\n"
    "v_xor_b32 v17, 0x400, v237\n"
    "v_lshrrev_b32 v23, 1, v1\n"
    "v_exp_f32 v58, v58\n"
    "v_exp_f32 v59, v59\n"
    "v_exp_f32 v60, v60\n"
    "v_mfma_f32_32x32x16_bf16 v[40:55], a[168:171], v[164:167], 0\n"
    "s_lshl_b32 s59, s41, 9\n"
    "ds_read_b128 v[152:155], v237 offset:4096\n"
    "ds_read_b128 v[156:159], v17 offset:4096\n"
    "v_exp_f32 v61, v61\n"
    "v_exp_f32 v62, v62\n"
    "v_exp_f32 v63, v63\n"
    "v_exp_f32 v64, v64\n"
    "v_exp_f32 v65, v65\n"
    "v_smfmac_f32_32x32x32_bf16 v[88:103], a[212:215], v[56:63], v2\n"
    "s_add_u32 s59, s59, 0x10000\n"
    "buffer_load_dwordx2 v[244:245], v23, s[24:27], s59 offen sc1\n"
    "ds_read_b64 v[168:169], v238 offset:1024\n"
    "v_exp_f32 v66, v66\n"
    "v_exp_f32 v67, v67\n"
    "v_exp_f32 v68, v68\n"
    "v_exp_f32 v69, v69\n"
    "v_mfma_f32_32x32x4_2b_bf16 v[24:55], a[192:193], v[170:171], v[24:55]\n"
    "v_exp_f32 v70, v70\n"
    "v_exp_f32 v71, v71\n"
    "v_exp_f32 v72, v72\n"
    "v_exp_f32 v73, v73\n"
    "v_exp_f32 v74, v74\n"
    "v_exp_f32 v75, v75\n"
    "v_exp_f32 v76, v76\n"
    "v_exp_f32 v77, v77\n"
    "v_smfmac_f32_32x32x32_bf16 v[88:103], a[216:219], v[64:71], v2\n"
    "v_exp_f32 v78, v78\n"
    "v_exp_f32 v79, v79\n"
    "v_exp_f32 v80, v80\n"
    "v_exp_f32 v81, v81\n"
    "v_smfmac_f32_32x32x32_bf16 v[104:119], a[212:215], v[72:79], v2\n"
    "v_exp_f32 v82, v82\n"
    "v_exp_f32 v83, v83\n"
    "v_exp_f32 v84, v84\n"
    "v_mfma_f32_32x32x16_bf16 v[56:71], a[172:175], v[160:163], 0\n"
    "buffer_store_dwordx2 v[240:241], v7, s[24:27], s66 offen offset:0 sc1\n"
    "v_exp_f32 v85, v85\n"
    "v_exp_f32 v86, v86\n"
    "v_exp_f32 v87, v87\n"
    "v_exp_f32 v24, v24\n"
    "v_exp_f32 v25, v25\n"
    "v_smfmac_f32_32x32x32_bf16 v[104:119], a[216:219], v[80:87], v2\n"
    "s_xor_b32 s50, s75, 0\n"
    "s_mul_i32 s50, s50, 0x2400\n"
    "v_exp_f32 v26, v26\n"
    "v_exp_f32 v27, v27\n"
    "v_exp_f32 v28, v28\n"
    "v_mfma_f32_32x32x16_bf16 v[72:87], a[172:175], v[164:167], 0\n"
    "s_add_u32 s50, s50, 0x1f000\n"
    "v_add_u32 v22, s50, v1\n"
    "v_exp_f32 v29, v29\n"
    "v_exp_f32 v30, v30\n"
    "v_exp_f32 v31, v31\n"
    "v_exp_f32 v32, v32\n"
    "v_exp_f32 v33, v33\n"
    "v_smfmac_f32_32x32x32_bf16 v[120:135], a[176:179], v[24:31], v2\n"
    "ds_write_b128 v22, v[88:91] offset:0\n"
    "v_exp_f32 v34, v34\n"
    "v_exp_f32 v35, v35\n"
    "v_exp_f32 v36, v36\n"
    "v_exp_f32 v37, v37\n"
    "v_mfma_f32_32x32x4_2b_bf16 v[56:87], a[194:195], v[170:171], v[56:87]\n"
    "v_exp_f32 v38, v38\n"
    "v_exp_f32 v39, v39\n"
    "v_exp_f32 v40, v40\n"
    "v_exp_f32 v41, v41\n"
    "v_exp_f32 v42, v42\n"
    "v_exp_f32 v43, v43\n"
    "v_exp_f32 v44, v44\n"
    "v_exp_f32 v45, v45\n"
    "v_smfmac_f32_32x32x32_bf16 v[120:135], a[180:183], v[32:39], v2\n"
    "ds_write_b128 v22, v[92:95] offset:1024\n"
    "v_exp_f32 v46, v46\n"
    "v_exp_f32 v47, v47\n"
    "v_exp_f32 v48, v48\n"
    "v_exp_f32 v49, v49\n"
    "v_smfmac_f32_32x32x32_bf16 v[136:151], a[176:179], v[40:47], v2\n"
    "ds_write_b128 v22, v[96:99] offset:2048\n"
    "v_exp_f32 v50, v50\n"
    "v_exp_f32 v51, v51\n"
    "v_exp_f32 v52, v52\n"
    "v_mfma_f32_32x32x16_bf16 v[24:39], a[196:199], v[160:163], 0\n"
    "v_exp_f32 v53, v53\n"
    "v_exp_f32 v54, v54\n"
    "v_exp_f32 v55, v55\n"
    "v_exp_f32 v56, v56\n"
    "v_exp_f32 v57, v57\n"
    "v_smfmac_f32_32x32x32_bf16 v[136:151], a[180:183], v[48:55], v2\n"
    "s_xor_b32 s50, s75, 1\n"
    "s_mul_i32 s50, s50, 0x2400\n"
    "v_exp_f32 v58, v58\n"
    "v_exp_f32 v59, v59\n"
    "v_exp_f32 v60, v60\n"
    "v_mfma_f32_32x32x16_bf16 v[40:55], a[196:199], v[164:167], 0\n"
    "s_add_u32 s50, s50, 0x1fc00\n"
    "v_add_u32 v22, s50, v1\n"
    "v_exp_f32 v61, v61\n"
    "v_exp_f32 v62, v62\n"
    "v_exp_f32 v63, v63\n"
    "v_exp_f32 v64, v64\n"
    "v_exp_f32 v65, v65\n"
    "v_smfmac_f32_32x32x32_bf16 v[120:135], a[184:187], v[56:63], v2\n"
    "ds_write_b128 v22, v[104:107] offset:0\n"
    "v_exp_f32 v66, v66\n"
    "v_exp_f32 v67, v67\n"
    "v_exp_f32 v68, v68\n"
    "v_exp_f32 v69, v69\n"
    "v_mfma_f32_32x32x4_2b_bf16 v[24:55], a[220:221], v[170:171], v[24:55]\n"
    "v_exp_f32 v70, v70\n"
    "v_exp_f32 v71, v71\n"
    "v_exp_f32 v72, v72\n"
    "v_exp_f32 v73, v73\n"
    "v_exp_f32 v74, v74\n"
    "v_exp_f32 v75, v75\n"
    "v_exp_f32 v76, v76\n"
    "v_exp_f32 v77, v77\n"
    "v_smfmac_f32_32x32x32_bf16 v[120:135], a[188:191], v[64:71], v2\n"
    "ds_write_b128 v22, v[108:111] offset:1024\n"
    "v_exp_f32 v78, v78\n"
    "v_exp_f32 v79, v79\n"
    "v_exp_f32 v80, v80\n"
    "v_exp_f32 v81, v81\n"
    "v_smfmac_f32_32x32x32_bf16 v[136:151], a[184:187], v[72:79], v2\n"
    "ds_write_b128 v22, v[112:115] offset:2048\n"
    "v_exp_f32 v82, v82\n"
    "v_exp_f32 v83, v83\n"
    "v_exp_f32 v84, v84\n"
    "v_mfma_f32_32x32x16_bf16 v[56:71], a[200:203], v[160:163], 0\n"
    "buffer_store_dwordx2 v[240:241], v8, s[24:27], s72 offen offset:0 sc1\n"
    "v_exp_f32 v85, v85\n"
    "v_exp_f32 v86, v86\n"
    "v_exp_f32 v87, v87\n"
    "v_exp_f32 v24, v24\n"
    "v_exp_f32 v25, v25\n"
    "v_smfmac_f32_32x32x32_bf16 v[136:151], a[188:191], v[80:87], v2\n"
    "v_mov_b32 v20, 0\n"
    "v_mov_b32 v21, 0\n"
    "v_exp_f32 v26, v26\n"
    "v_exp_f32 v27, v27\n"
    "v_exp_f32 v28, v28\n"
    "v_mfma_f32_32x32x16_bf16 v[72:87], a[200:203], v[164:167], 0\n"
    "s_waitcnt lgkmcnt(0)\n"
    "v_exp_f32 v29, v29\n"
    "v_exp_f32 v30, v30\n"
    "v_exp_f32 v31, v31\n"
    "v_mfma_f32_32x32x8_bf16 v[88:103], v[20:21], v[20:21], 0\n"
    "v_exp_f32 v32, v32\n"
    "v_exp_f32 v33, v33\n"
    "v_smfmac_f32_32x32x32_bf16 v[120:135], a[204:207], v[24:31], v2\n"
    "v_exp_f32 v34, v34\n"
    "v_exp_f32 v35, v35\n"
    "v_exp_f32 v36, v36\n"
    "v_exp_f32 v37, v37\n"
    "v_mfma_f32_32x32x4_2b_bf16 v[56:87], a[222:223], v[170:171], v[56:87]\n"
    "v_exp_f32 v38, v38\n"
    "v_exp_f32 v39, v39\n"
    "v_exp_f32 v40, v40\n"
    "v_exp_f32 v41, v41\n"
    "v_exp_f32 v42, v42\n"
    "v_exp_f32 v43, v43\n"
    "v_exp_f32 v44, v44\n"
    "v_exp_f32 v45, v45\n"
    "v_smfmac_f32_32x32x32_bf16 v[120:135], a[208:211], v[32:39], v2\n"
    "v_exp_f32 v46, v46\n"
    "v_exp_f32 v47, v47\n"
    "v_exp_f32 v48, v48\n"
    "v_exp_f32 v49, v49\n"
    "v_smfmac_f32_32x32x32_bf16 v[136:151], a[204:207], v[40:47], v2\n"
    "v_exp_f32 v50, v50\n"
    "v_exp_f32 v51, v51\n"
    "v_exp_f32 v52, v52\n"
    "v_mfma_f32_32x32x16_bf16 v[24:39], a[0:3], v[152:155], 0\n"
    "buffer_store_dwordx4 v[240:243], v1, s[24:27], s73 offen offset:0 sc1\n"
    "v_exp_f32 v53, v53\n"
    "v_exp_f32 v54, v54\n"
    "v_exp_f32 v55, v55\n"
    "v_mfma_f32_32x32x8_bf16 v[104:119], v[20:21], v[20:21], 0\n"
    "v_exp_f32 v56, v56\n"
    "v_exp_f32 v57, v57\n"
    "v_smfmac_f32_32x32x32_bf16 v[136:151], a[208:211], v[48:55], v2\n"
    "v_xor_b32 v16, 0x800, v237\n"
    "v_xor_b32 v18, 0x200, v238\n"
    "v_xor_b32 v17, 0xc00, v237\n"
    "v_exp_f32 v58, v58\n"
    "v_exp_f32 v59, v59\n"
    "v_exp_f32 v60, v60\n"
    "v_mfma_f32_32x32x16_bf16 v[40:55], a[0:3], v[156:159], 0\n"
    "ds_read_b128 v[160:163], v16 offset:4096\n"
    "ds_read_b128 v[164:167], v17 offset:4096\n"
    "v_exp_f32 v61, v61\n"
    "v_exp_f32 v62, v62\n"
    "v_exp_f32 v63, v63\n"
    "v_exp_f32 v64, v64\n"
    "v_exp_f32 v65, v65\n"
    "v_smfmac_f32_32x32x32_bf16 v[120:135], a[212:215], v[56:63], v2\n"
    "ds_read_b64 v[170:171], v18 offset:1024\n"
    "v_exp_f32 v66, v66\n"
    "v_exp_f32 v67, v67\n"
    "v_exp_f32 v68, v68\n"
    "v_exp_f32 v69, v69\n"
    "v_mfma_f32_32x32x4_2b_bf16 v[24:55], a[24:25], v[168:169], v[24:55]\n"
    "v_exp_f32 v70, v70\n"
    "v_exp_f32 v71, v71\n"
    "v_exp_f32 v72, v72\n"
    "v_exp_f32 v73, v73\n"
    "v_exp_f32 v74, v74\n"
    "v_exp_f32 v75, v75\n"
    "v_exp_f32 v76, v76\n"
    "v_exp_f32 v77, v77\n"
    "v_smfmac_f32_32x32x32_bf16 v[120:135], a[216:219], v[64:71], v2\n"
    "s_waitcnt vmcnt(3)\n"
    "v_cmp_ne_u64 vcc, s[6:7], v[244:245]\n"
    "s_nop 0\n"
    "s_cbranch_vccnz Lspin9\n"
    "Lseen10:\n"
    "s_barrier\n"
    "v_exp_f32 v78, v78\n"
    "v_exp_f32 v79, v79\n"
    "v_exp_f32 v80, v80\n"
    "v_exp_f32 v81, v81\n"
    "v_smfmac_f32_32x32x32_bf16 v[136:151], a[212:215], v[72:79], v2\n"
    "s_add_u32 m0, s79, 0\n"
    "s_add_u32 s56, s77, 0\n"
    "buffer_load_dwordx4 v1, s[24:27], s56 offen offset:0 sc1 lds\n"
    "v_exp_f32 v82, v82\n"
    "v_exp_f32 v83, v83\n"
    "v_exp_f32 v84, v84\n"
    "v_mfma_f32_32x32x16_bf16 v[56:71], a[4:7], v[152:155], 0\n"
    "v_exp_f32 v85, v85\n"
    "v_exp_f32 v86, v86\n"
    "v_exp_f32 v87, v87\n"
    "v_exp_f32 v24, v24\n"
    "v_exp_f32 v25, v25\n"
    "v_smfmac_f32_32x32x32_bf16 v[136:151], a[216:219], v[80:87], v2\n"
    "s_xor_b32 s50, s75, 2\n"
    "s_mul_i32 s50, s50, 0x2400\n"
    "buffer_load_dwordx4 v1, s[24:27], s56 offen offset:1024 sc1 lds\n"
    "v_exp_f32 v26, v26\n"
    "v_exp_f32 v27, v27\n"
    "v_exp_f32 v28, v28\n"
    "v_mfma_f32_32x32x16_bf16 v[72:87], a[4:7], v[156:159], 0\n"
    "s_add_u32 s50, s50, 0x20800\n"
    "buffer_load_dwordx4 v1, s[24:27], s56 offen offset:2048 sc1 lds\n"
    "v_add_u32 v22, s50, v1\n"
    "v_exp_f32 v29, v29\n"
    "v_exp_f32 v30, v30\n"
    "v_exp_f32 v31, v31\n"
    "v_exp_f32 v32, v32\n"
    "v_exp_f32 v33, v33\n"
    "v_smfmac_f32_32x32x32_bf16 v[88:103], a[8:11], v[24:31], v2\n"
    "ds_write_b128 v22, v[120:123] offset:0\n"
    "v_exp_f32 v34, v34\n"
    "v_exp_f32 v35, v35\n"
    "v_exp_f32 v36, v36\n"
    "v_exp_f32 v37, v37\n"
    "v_mfma_f32_32x32x4_2b_bf16 v[56:87], a[26:27], v[168:169], v[56:87]\n"
    "v_exp_f32 v38, v38\n"
    "v_exp_f32 v39, v39\n"
    "v_exp_f32 v40, v40\n"
    "v_exp_f32 v41, v41\n"
    "v_exp_f32 v42, v42\n"
    "v_exp_f32 v43, v43\n"
    "v_exp_f32 v44, v44\n"
    "v_exp_f32 v45, v45\n"
    "v_smfmac_f32_32x32x32_bf16 v[88:103], a[12:15], v[32:39], v2\n"
    "buffer_load_dwordx4 v1, s[24:27], s56 offen offset:3072 sc1 lds\n"
    "ds_write_b128 v22, v[124:127] offset:1024\n"
    "v_exp_f32 v46, v46\n"
    "v_exp_f32 v47, v47\n"
    "v_exp_f32 v48, v48\n"
    "v_exp_f32 v49, v49\n"
    "v_smfmac_f32_32x32x32_bf16 v[104:119], a[8:11], v[40:47], v2\n"
    "s_add_u32 m0, s79, 0x4000\n"
    "s_add_u32 s56, s77, 0x4000\n"
    "buffer_load_dwordx4 v1, s[24:27], s56 offen offset:0 sc1 lds\n"
    "ds_write_b128 v22, v[128:131] offset:2048\n"
    "v_exp_f32 v50, v50\n"
    "v_exp_f32 v51, v51\n"
    "v_exp_f32 v52, v52\n"
    "v_mfma_f32_32x32x16_bf16 v[24:39], a[28:31], v[152:155], 0\n"
    "buffer_store_dwordx4 v[240:243], v1, s[24:27], s73 offen offset:2048 sc1\n"
    "v_exp_f32 v53, v53\n"
    "v_exp_f32 v54, v54\n"
    "v_exp_f32 v55, v55\n"
    "v_exp_f32 v56, v56\n"
    "v_exp_f32 v57, v57\n"
    "v_smfmac_f32_32x32x32_bf16 v[104:119], a[12:15], v[48:55], v2\n"
    "s_mul_i32 s50, s41, 0x2400\n"
    "s_add_u32 s50, s50, 0x1f000\n"
    "v_add_u32 v22, s50, v1\n"
    "v_exp_f32 v58, v58\n"
    "v_exp_f32 v59, v59\n"
    "v_exp_f32 v60, v60\n"
    "v_mfma_f32_32x32x16_bf16 v[40:55], a[28:31], v[156:159], 0\n"
    "s_waitcnt lgkmcnt(0)\n"
    "buffer_load_dwordx4 v1, s[24:27], s56 offen offset:1024 sc1 lds\n"
    "v_exp_f32 v61, v61\n"
    "v_exp_f32 v62, v62\n"
    "v_exp_f32 v63, v63\n"
    "v_exp_f32 v64, v64\n"
    "v_exp_f32 v65, v65\n"
    "v_smfmac_f32_32x32x32_bf16 v[88:103], a[16:19], v[56:63], v2\n"
    "s_barrier\n"
    "s_add_u32 s58, s73, 0x1000\n"
    "buffer_load_dwordx4 v1, s[24:27], s56 offen offset:2048 sc1 lds\n"
    "ds_read_b128 v[172:175], v22 offset:0\n"
    "ds_read_b128 v[176:179], v22 offset:1024\n"
    "v_exp_f32 v66, v66\n"
    "v_exp_f32 v67, v67\n"
    "Lhop4:\n"
    "s_cbranch_execz LhopA\n"
    "v_exp_f32 v68, v68\n"
    "v_exp_f32 v69, v69\n"
    "v_mfma_f32_32x32x4_2b_bf16 v[24:55], a[52:53], v[168:169], v[24:55]\n"
    "v_exp_f32 v70, v70\n"
    "v_exp_f32 v71, v71\n"
    "v_exp_f32 v72, v72\n"
    "v_exp_f32 v73, v73\n"
    "v_exp_f32 v74, v74\n"
    "v_exp_f32 v75, v75\n"
    "v_exp_f32 v76, v76\n"
    "v_exp_f32 v77, v77\n"
    "v_smfmac_f32_32x32x32_bf16 v[88:103], a[20:23], v[64:71], v2\n"
    "buffer_load_dwordx4 v1, s[24:27], s56 offen offset:3072 sc1 lds\n"
    "ds_read_b128 v[180:183], v22 offset:2048\n"
    "ds_read_b128 v[184:187], v22 offset:3072\n"
    "v_exp_f32 v78, v78\n"
    "v_exp_f32 v79, v79\n"
    "v_exp_f32 v80, v80\n"
    "v_exp_f32 v81, v81\n"
    "v_smfmac_f32_32x32x32_bf16 v[104:119], a[16:19], v[72:79], v2\n"
    "s_add_u32 m0, s79, 0x8000\n"
    "s_add_u32 s56, s77, 0x8000\n"
    "buffer_load_dwordx4 v1, s[24:27], s56 offen offset:0 sc1 lds\n"
    "ds_read_b128 v[188:191], v22 offset:4096\n"
    "ds_read_b128 v[192:195], v22 offset:5120\n"
    "v_exp_f32 v82, v82\n"
    "v_exp_f32 v83, v83\n"
    "v_exp_f32 v84, v84\n"
    "v_mfma_f32_32x32x16_bf16 v[56:71], a[32:35], v[152:155], 0\n"
    "ds_read_b128 v[196:199], v22 offset:6144\n"
    "ds_read_b128 v[200:203], v22 offset:7168\n"
    "v_exp_f32 v85, v85\n"
    "v_exp_f32 v86, v86\n"
    "v_exp_f32 v87, v87\n"
    "v_exp_f32 v24, v24\n"
    "v_exp_f32 v25, v25\n"
    "v_smfmac_f32_32x32x32_bf16 v[104:119], a[20:23], v[80:87], v2\n"
    "buffer_load_dwordx4 v1, s[24:27], s56 offen offset:1024 sc1 lds\n"
    "ds_read_b128 v[204:207], v22 offset:8192\n"
    "v_exp_f32 v26, v26\n"
    "v_exp_f32 v27, v27\n"
    "v_exp_f32 v28, v28\n"
    "v_mfma_f32_32x32x16_bf16 v[72:87], a[32:35], v[156:159], 0\n"
    "s_waitcnt lgkmcnt(6)\n"
    "buffer_store_dwordx4 v[240:243], v1, s[24:27], s58 offen offset:0 sc1\n"
    "v_add_f32 v172, v172, v136\n"
    "v_add_f32 v173, v173, v137\n"
    "v_add_f32 v174, v174, v138\n"
    "v_add_f32 v175, v175, v139\n"
    "v_add_f32 v176, v176, v140\n"
    "v_add_f32 v177, v177, v141\n"
    "v_exp_f32 v29, v29\n"
    "v_exp_f32 v30, v30\n"
    "v_exp_f32 v31, v31\n"
    "v_exp_f32 v32, v32\n"
    "v_exp_f32 v33, v33\n"
    "v_smfmac_f32_32x32x32_bf16 v[88:103], a[36:39], v[24:31], v2\n"
    "s_add_u32 s58, s73, 0x1000\n"
    "v_add_f32 v178, v178, v142\n"
    "v_add_f32 v179, v179, v143\n"
    "v_add_f32 v180, v180, v144\n"
    "v_add_f32 v181, v181, v145\n"
    "v_add_f32 v182, v182, v146\n"
    "v_add_f32 v183, v183, v147\n"
    "v_exp_f32 v34, v34\n"
    "v_exp_f32 v35, v35\n"
    "v_exp_f32 v36, v36\n"
    "v_exp_f32 v37, v37\n"
    "v_mfma_f32_32x32x4_2b_bf16 v[56:87], a[54:55], v[168:169], v[56:87]\n"
    "v_exp_f32 v38, v38\n"
    "v_exp_f32 v39, v39\n"
    "v_exp_f32 v40, v40\n"
    "v_exp_f32 v41, v41\n"
    "v_exp_f32 v42, v42\n"
    "v_exp_f32 v43, v43\n"
    "v_exp_f32 v44, v44\n"
    "v_exp_f32 v45, v45\n"
    "v_smfmac_f32_32x32x32_bf16 v[88:103], a[40:43], v[32:39], v2\n"
    "buffer_load_dwordx4 v1, s[24:27], s56 offen offset:2048 sc1 lds\n"
    "v_mov_b32 v20, 0\n"
    "v_mov_b32 v21, 0\n"
    "v_exp_f32 v46, v46\n"
    "v_exp_f32 v47, v47\n"
    "v_exp_f32 v48, v48\n"
    "v_exp_f32 v49, v49\n"
    "v_smfmac_f32_32x32x32_bf16 v[104:119], a[36:39], v[40:47], v2\n"
    "buffer_load_dwordx4 v1, s[24:27], s56 offen offset:3072 sc1 lds\n"
    "v_exp_f32 v50, v50\n"
    "v_exp_f32 v51, v51\n"
    "v_exp_f32 v52, v52\n"
    "v_mfma_f32_32x32x16_bf16 v[24:39], a[0:3], v[160:163], 0\n"
    "s_add_u32 m0, s79, 0xc000\n"
    "v_exp_f32 v53, v53\n"
    "v_exp_f32 v54, v54\n"
    "v_exp_f32 v55, v55\n"
    "v_mfma_f32_32x32x8_bf16 v[120:135], v[20:21], v[20:21], 0\n"
    "v_exp_f32 v56, v56\n"
    "v_exp_f32 v57, v57\n"
    "v_smfmac_f32_32x32x32_bf16 v[104:119], a[40:43], v[48:55], v2\n"
    "s_add_u32 s56, s77, 0xc000\n"
    "buffer_load_dwordx4 v1, s[24:27], s56 offen offset:0 sc1 lds\n"
    "v_exp_f32 v58, v58\n"
    "v_exp_f32 v59, v59\n"
    "v_exp_f32 v60, v60\n"
    "v_mfma_f32_32x32x16_bf16 v[40:55], a[0:3], v[164:167], 0\n"
    "v_exp_f32 v61, v61\n"
    "v_exp_f32 v62, v62\n"
    "v_exp_f32 v63, v63\n"
    "v_mfma_f32_32x32x8_bf16 v[136:151], v[20:21], v[20:21], 0\n"
    "v_exp_f32 v64, v64\n"
    "v_exp_f32 v65, v65\n"
    "v_smfmac_f32_32x32x32_bf16 v[88:103], a[44:47], v[56:63], v2\n"
    "buffer_load_dwordx4 v1, s[24:27], s56 offen offset:1024 sc1 lds\n"
    "v_exp_f32 v66, v66\n"
    "v_exp_f32 v67, v67\n"
    "v_exp_f32 v68, v68\n"
    "v_exp_f32 v69, v69\n"
    "v_mfma_f32_32x32x4_2b_bf16 v[24:55], a[24:25], v[170:171], v[24:55]\n"
    "v_exp_f32 v70, v70\n"
    "v_exp_f32 v71, v71\n"
    "v_exp_f32 v72, v72\n"
    "v_exp_f32 v73, v73\n"
    "v_exp_f32 v74, v74\n"
    "v_exp_f32 v75, v75\n"
    "v_exp_f32 v76, v76\n"
    "v_exp_f32 v77, v77\n"
    "v_smfmac_f32_32x32x32_bf16 v[88:103], a[48:51], v[64:71], v2\n"
    "buffer_load_dwordx4 v1, s[24:27], s56 offen offset:2048 sc1 lds\n"
    "v_exp_f32 v78, v78\n"
    "v_exp_f32 v79, v79\n"
    "v_exp_f32 v80, v80\n"
    "v_exp_f32 v81, v81\n"
    "v_smfmac_f32_32x32x32_bf16 v[104:119], a[44:47], v[72:79], v2\n"
    "buffer_load_dwordx4 v1, s[24:27], s56 offen offset:3072 sc1 lds\n"
    "v_exp_f32 v82, v82\n"
    "v_exp_f32 v83, v83\n"
    "v_exp_f32 v84, v84\n"
    "v_mfma_f32_32x32x16_bf16 v[56:71], a[4:7], v[160:163], 0\n"
    "buffer_store_dwordx4 v[240:243], v1, s[24:27], s58 offen offset:2048 sc1\n"
    "v_exp_f32 v85, v85\n"
    "v_exp_f32 v86, v86\n"
    "v_exp_f32 v87, v87\n"
    "v_exp_f32 v24, v24\n"
    "v_exp_f32 v25, v25\n"
    "v_smfmac_f32_32x32x32_bf16 v[104:119], a[48:51], v[80:87], v2\n"
    "s_waitcnt lgkmcnt(0)\n"
    "v_add_f32 v184, v184, v196\n"
    "v_add_f32 v185, v185, v197\n"
    "v_add_f32 v186, v186, v198\n"
    "v_add_f32 v187, v187, v199\n"
    "v_add_f32 v188, v188, v200\n"
    "v_add_f32 v189, v189, v201\n"
    "v_exp_f32 v26, v26\n"
    "v_exp_f32 v27, v27\n"
    "v_exp_f32 v28, v28\n"
    "v_mfma_f32_32x32x16_bf16 v[72:87], a[4:7], v[164:167], 0\n"
    "s_add_u32 s58, s73, 0x2000\n"
    "v_add_f32 v190, v190, v202\n"
    "v_add_f32 v191, v191, v203\n"
    "v_add_f32 v192, v192, v204\n"
    "v_add_f32 v193, v193, v205\n"
    "v_add_f32 v194, v194, v206\n"
    "v_add_f32 v195, v195, v207\n"
    "v_exp_f32 v29, v29\n"
    "v_exp_f32 v30, v30\n"
    "v_exp_f32 v31, v31\n"
    "v_exp_f32 v32, v32\n"
    "v_exp_f32 v33, v33\n"
    "v_smfmac_f32_32x32x32_bf16 v[120:135], a[8:11], v[24:31], v2\n"
    "s_add_u32 m0, s79, 0x10000\n"
    "s_add_u32 s56, s77, 0x10000\n"
    "buffer_load_dwordx4 v1, s[24:27], s56 offen offset:0 sc1 lds\n"
    "v_add_f32 v172, v172, v184\n"
    "v_add_f32 v173, v173, v185\n"
    "v_add_f32 v174, v174, v186\n"
    "v_add_f32 v175, v175, v187\n"
    "v_add_f32 v176, v176, v188\n"
    "v_add_f32 v177, v177, v189\n"
    "v_exp_f32 v34, v34\n"
    "v_exp_f32 v35, v35\n"
    "v_exp_f32 v36, v36\n"
    "v_exp_f32 v37, v37\n"
    "v_mfma_f32_32x32x4_2b_bf16 v[56:87], a[26:27], v[170:171], v[56:87]\n"
    "v_exp_f32 v38, v38\n"
    "v_exp_f32 v39, v39\n"
    "v_exp_f32 v40, v40\n"
    "v_exp_f32 v41, v41\n"
    "v_exp_f32 v42, v42\n"
    "v_exp_f32 v43, v43\n"
    "v_exp_f32 v44, v44\n"
    "v_exp_f32 v45, v45\n"
    "v_smfmac_f32_32x32x32_bf16 v[120:135], a[12:15], v[32:39], v2\n"
    "buffer_load_dwordx4 v1, s[24:27], s56 offen offset:1024 sc1 lds\n"
    "v_add_f32 v178, v178, v190\n"
    "v_add_f32 v179, v179, v191\n"
    "v_add_f32 v180, v180, v192\n"
    "v_add_f32 v181, v181, v193\n"
    "v_add_f32 v182, v182, v194\n"
    "v_add_f32 v183, v183, v195\n"
    "v_exp_f32 v46, v46\n"
    "v_exp_f32 v47, v47\n"
    "v_exp_f32 v48, v48\n"
    "v_exp_f32 v49, v49\n"
    "v_smfmac_f32_32x32x32_bf16 v[136:151], a[8:11], v[40:47], v2\n"
    "buffer_load_dwordx4 v1, s[24:27], s56 offen offset:2048 sc1 lds\n"
    "v_mov_b32 v16, v180\n"
    "v_mov_b32 v17, v180\n"
    "v_exp_f32 v50, v50\n"
    "v_exp_f32 v51, v51\n"
    "v_exp_f32 v52, v52\n"
    "v_mfma_f32_32x32x16_bf16 v[24:39], a[28:31], v[160:163], 0\n"
    "s_add_u32 s50, s61, 0\n"
    "v_exp_f32 v53, v53\n"
    "v_exp_f32 v54, v54\n"
    "v_exp_f32 v55, v55\n"
    "v_exp_f32 v56, v56\n"
    "v_exp_f32 v57, v57\n"
    "v_smfmac_f32_32x32x32_bf16 v[136:151], a[12:15], v[48:55], v2\n"
    "s_add_u32 s49, s62, 0\n"
    "buffer_load_dwordx4 v1, s[24:27], s56 offen offset:3072 sc1 lds\n"
    "v_permlane32_swap_b32 v17, v16\n"
    "v_subrev_u32 v17, 0x1f800000, v16\n"
    "v_cmp_lt_u32 vcc, 0x40000000, v17\n"
    "v_rcp_f32 v16, v16\n"
    "s_nop 0\n"
    "v_mul_f32 v172, v172, v16\n"
    "v_mul_f32 v173, v173, v16\n"
    "v_exp_f32 v58, v58\n"
    "v_exp_f32 v59, v59\n"
    "v_exp_f32 v60, v60\n"
    "v_mfma_f32_32x32x16_bf16 v[40:55], a[28:31], v[164:167], 0\n"
    "s_add_u32 m0, s79, 0x14000\n"
    "v_mul_f32 v174, v174, v16\n"
    "v_mul_f32 v175, v175, v16\n"
    "v_mul_f32 v176, v176, v16\n"
    "v_mul_f32 v177, v177, v16\n"
    "v_mul_f32 v178, v178, v16\n"
    "v_mul_f32 v179, v179, v16\n"
    "v_exp_f32 v61, v61\n"
    "v_exp_f32 v62, v62\n"
    "v_exp_f32 v63, v63\n"
    "v_exp_f32 v64, v64\n"
    "v_exp_f32 v65, v65\n"
    "v_smfmac_f32_32x32x32_bf16 v[120:135], a[16:19], v[56:63], v2\n"
    "s_add_u32 s56, s77, 0x14000\n"
    "buffer_load_dwordx4 v1, s[24:27], s56 offen offset:0 sc1 lds\n"
    "v_mul_f32 v180, v180, v16\n"
    "v_mul_f32 v181, v181, v16\n"
    "v_mul_f32 v182, v182, v16\n"
    "v_mul_f32 v183, v183, v16\n"
    "v_exp_f32 v66, v66\n"
    "v_exp_f32 v67, v67\n"
    "v_exp_f32 v68, v68\n"
    "v_exp_f32 v69, v69\n"
    "v_mfma_f32_32x32x4_2b_bf16 v[24:55], a[52:53], v[170:171], v[24:55]\n"
    "v_exp_f32 v70, v70\n"
    "v_exp_f32 v71, v71\n"
    "v_exp_f32 v72, v72\n"
    "v_exp_f32 v73, v73\n"
    "v_exp_f32 v74, v74\n"
    "v_exp_f32 v75, v75\n"
    "v_exp_f32 v76, v76\n"
    "v_exp_f32 v77, v77\n"
    "v_smfmac_f32_32x32x32_bf16 v[120:135], a[20:23], v[64:71], v2\n"
    "s_cmp_eq_u64 vcc, 0\n"
    "s_cselect_b32 s51, 0, 0x40000000\n"
    "s_cselect_b32 s52, 0, 1\n"
    "s_or_b32 s74, s74, s52\n"
    "s_cbranch_scc1 Lqsv11\n"
    "Lqsr12:\n"
    "buffer_load_dwordx4 v1, s[24:27], s56 offen offset:1024 sc1 lds\n"
    "v_or_b32 v18, s51, v5\n"
    "v_or_b32 v19, s51, v6\n"
    "v_cvt_pk_bf16_f32 v172, v172, v173\n"
    "v_cvt_pk_bf16_f32 v173, v174, v175\n"
    "v_cvt_pk_bf16_f32 v174, v176, v177\n"
    "v_cvt_pk_bf16_f32 v175, v178, v179\n"
    "v_exp_f32 v78, v78\n"
    "v_exp_f32 v79, v79\n"
    "v_exp_f32 v80, v80\n"
    "v_exp_f32 v81, v81\n"
    "v_smfmac_f32_32x32x32_bf16 v[136:151], a[16:19], v[72:79], v2\n"
    "buffer_load_dwordx4 v1, s[24:27], s56 offen offset:2048 sc1 lds\n"
    "v_cvt_pk_bf16_f32 v176, v180, v181\n"
    "v_cvt_pk_bf16_f32 v177, v182, v183\n"
    "v_exp_f32 v82, v82\n"
    "v_exp_f32 v83, v83\n"
    "v_exp_f32 v84, v84\n"
    "v_mfma_f32_32x32x16_bf16 v[56:71], a[32:35], v[160:163], 0\n"
    "buffer_store_dwordx4 v[172:175], v18, s[24:27], s50 offen sc1\n"
    "v_exp_f32 v85, v85\n"
    "v_exp_f32 v86, v86\n"
    "v_exp_f32 v87, v87\n"
    "v_exp_f32 v24, v24\n"
    "v_exp_f32 v25, v25\n"
    "v_smfmac_f32_32x32x32_bf16 v[136:151], a[20:23], v[80:87], v2\n"
    "buffer_store_dwordx2 v[176:177], v19, s[24:27], s49 offen sc1\n"
    "v_exp_f32 v26, v26\n"
    "v_exp_f32 v27, v27\n"
    "v_exp_f32 v28, v28\n"
    "v_mfma_f32_32x32x16_bf16 v[72:87], a[32:35], v[164:167], 0\n"
    "buffer_store_dwordx4 v[240:243], v1, s[24:27], s58 offen offset:0 sc1\n"
    "v_exp_f32 v29, v29\n"
    "v_exp_f32 v30, v30\n"
    "v_exp_f32 v31, v31\n"
    "v_exp_f32 v32, v32\n"
    "v_exp_f32 v33, v33\n"
    "v_smfmac_f32_32x32x32_bf16 v[120:135], a[36:39], v[24:31], v2\n"
    "s_add_u32 s58, s73, 0x2000\n"
    "v_exp_f32 v34, v34\n"
    "v_exp_f32 v35, v35\n"
    "v_exp_f32 v36, v36\n"
    "v_exp_f32 v37, v37\n"
    "v_mfma_f32_32x32x4_2b_bf16 v[56:87], a[54:55], v[170:171], v[56:87]\n"
    "v_exp_f32 v38, v38\n"
    "v_exp_f32 v39, v39\n"
    "v_exp_f32 v40, v40\n"
    "v_exp_f32 v41, v41\n"
    "v_exp_f32 v42, v42\n"
    "v_exp_f32 v43, v43\n"
    "v_exp_f32 v44, v44\n"
    "v_exp_f32 v45, v45\n"
    "v_smfmac_f32_32x32x32_bf16 v[120:135], a[40:43], v[32:39], v2\n"
    "buffer_load_dwordx4 v1, s[24:27], s56 offen offset:3072 sc1 lds\n"
    "v_exp_f32 v46, v46\n"
    "v_exp_f32 v47, v47\n"
    "v_exp_f32 v48, v48\n"
    "v_exp_f32 v49, v49\n"
    "v_smfmac_f32_32x32x32_bf16 v[136:151], a[36:39], v[40:47], v2\n"
    "s_mul_i32 s56, s41, 0x400\n"
    "s_add_u32 m0, s56, 0x18000\n"
    "v_exp_f32 v50, v50\n"
    "v_exp_f32 v51, v51\n"
    "v_exp_f32 v52, v52\n"
    "v_mfma_f32_32x32x16_bf16 v[24:39], a[56:59], v[152:155], 0\n"
    "buffer_store_dwordx4 v[240:243], v1, s[24:27], s58 offen offset:2048 sc1\n"
    "v_exp_f32 v53, v53\n"
    "v_exp_f32 v54, v54\n"
    "v_exp_f32 v55, v55\n"
    "v_exp_f32 v56, v56\n"
    "v_exp_f32 v57, v57\n"
    "v_smfmac_f32_32x32x32_bf16 v[136:151], a[40:43], v[48:55], v2\n"
    "s_add_u32 s58, s73, 0x3000\n"
    "s_sub_u32 s56, s56, s79\n"
    "v_exp_f32 v58, v58\n"
    "v_exp_f32 v59, v59\n"
    "v_exp_f32 v60, v60\n"
    "v_mfma_f32_32x32x16_bf16 v[40:55], a[56:59], v[156:159], 0\n"
    "s_add_u32 s56, s56, s77\n"
    "v_exp_f32 v61, v61\n"
    "v_exp_f32 v62, v62\n"
    "v_exp_f32 v63, v63\n"
    "v_exp_f32 v64, v64\n"
    "v_exp_f32 v65, v65\n"
    "v_smfmac_f32_32x32x32_bf16 v[120:135], a[44:47], v[56:63], v2\n"
    "s_add_u32 s56, s56, 0x18000\n"
    "buffer_load_dwordx4 v1, s[24:27], s56 offen offset:0 sc1 lds\n"
    "v_exp_f32 v66, v66\n"
    "v_exp_f32 v67, v67\n"
    "v_exp_f32 v68, v68\n"
    "v_exp_f32 v69, v69\n"
    "v_mfma_f32_32x32x4_2b_bf16 v[24:55], a[80:81], v[168:169], v[24:55]\n"
    "v_exp_f32 v70, v70\n"
    "v_exp_f32 v71, v71\n"
    "v_exp_f32 v72, v72\n"
    "v_exp_f32 v73, v73\n"
    "v_exp_f32 v74, v74\n"
    "v_exp_f32 v75, v75\n"
    "v_exp_f32 v76, v76\n"
    "v_exp_f32 v77, v77\n"
    "v_smfmac_f32_32x32x32_bf16 v[120:135], a[48:51], v[64:71], v2\n"
    "s_mul_i32 s56, s41, 0xc00\n"
    "s_add_u32 m0, s56, 0x19000\n"
    "v_exp_f32 v78, v78\n"
    "v_exp_f32 v79, v79\n"
    "v_exp_f32 v80, v80\n"
    "v_exp_f32 v81, v81\n"
    "v_smfmac_f32_32x32x32_bf16 v[136:151], a[44:47], v[72:79], v2\n"
    "s_sub_u32 s56, s56, s79\n"
    "s_add_u32 s56, s56, s76\n"
    "v_exp_f32 v82, v82\n"
    "v_exp_f32 v83, v83\n"
    "v_exp_f32 v84, v84\n"
    "v_mfma_f32_32x32x16_bf16 v[56:71], a[60:63], v[152:155], 0\n"
    "s_add_u32 s56, s56, 0\n"
    "buffer_load_dwordx4 v1, s[24:27], s56 offen offset:0 sc1 lds\n"
    "v_exp_f32 v85, v85\n"
    "v_exp_f32 v86, v86\n"
    "v_exp_f32 v87, v87\n"
    "v_exp_f32 v24, v24\n"
    "v_exp_f32 v25, v25\n"
    "v_smfmac_f32_32x32x32_bf16 v[136:151], a[48:51], v[80:87], v2\n"
    "buffer_load_dwordx4 v1, s[24:27], s56 offen offset:1024 sc1 lds\n"
    "v_exp_f32 v26, v26\n"
    "v_exp_f32 v27, v27\n"
    "v_exp_f32 v28, v28\n"
    "v_mfma_f32_32x32x16_bf16 v[72:87], a[60:63], v[156:159], 0\n"
    "v_exp_f32 v29, v29\n"
    "v_exp_f32 v30, v30\n"
    "v_exp_f32 v31, v31\n"
    "v_exp_f32 v32, v32\n"
    "v_exp_f32 v33, v33\n"
    "v_smfmac_f32_32x32x32_bf16 v[88:103], a[64:67], v[24:31], v2\n"
    "buffer_load_dwordx4 v1, s[24:27], s56 offen offset:2048 sc1 lds\n"
    "v_exp_f32 v34, v34\n"
    "v_exp_f32 v35, v35\n"
    "v_exp_f32 v36, v36\n"
    "v_exp_f32 v37, v37\n"
    "v_mfma_f32_32x32x4_2b_bf16 v[56:87], a[82:83], v[168:169], v[56:87]\n"
    "v_exp_f32 v38, v38\n"
    "v_exp_f32 v39, v39\n"
    "v_exp_f32 v40, v40\n"
    "v_exp_f32 v41, v41\n"
    "v_exp_f32 v42, v42\n"
    "v_exp_f32 v43, v43\n"
    "v_exp_f32 v44, v44\n"
    "v_exp_f32 v45, v45\n"
    "v_smfmac_f32_32x32x32_bf16 v[88:103], a[68:71], v[32:39], v2\n"
    "s_add_u32 s57, s78, 0xe000\n"
    "buffer_load_dwordx4 a[224:227], v1, s[24:27], s57 offen offset:0 sc1\n"
    "v_exp_f32 v46, v46\n"
    "v_exp_f32 v47, v47\n"
    "v_exp_f32 v48, v48\n"
    "v_exp_f32 v49, v49\n"
    "v_smfmac_f32_32x32x32_bf16 v[104:119], a[64:67], v[40:47], v2\n"
    "buffer_load_dwordx4 a[228:231], v1, s[24:27], s57 offen offset:1024 sc1\n"
    "v_exp_f32 v50, v50\n"
    "v_exp_f32 v51, v51\n"
    "v_exp_f32 v52, v52\n"
    "v_mfma_f32_32x32x16_bf16 v[24:39], a[56:59], v[160:163], 0\n"
    "buffer_store_dwordx4 v[240:243], v9, s[24:27], s58 offen offset:0 sc1\n"
    "v_exp_f32 v53, v53\n"
    "v_exp_f32 v54, v54\n"
    "v_exp_f32 v55, v55\n"
    "v_exp_f32 v56, v56\n"
    "v_exp_f32 v57, v57\n"
    "v_smfmac_f32_32x32x32_bf16 v[104:119], a[68:71], v[48:55], v2\n"
    "v_exp_f32 v58, v58\n"
    "v_exp_f32 v59, v59\n"
    "v_exp_f32 v60, v60\n"
    "v_mfma_f32_32x32x16_bf16 v[40:55], a[56:59], v[164:167], 0\n"
    "v_exp_f32 v61, v61\n"
    "v_exp_f32 v62, v62\n"
    "v_exp_f32 v63, v63\n"
    "v_exp_f32 v64, v64\n"
    "v_exp_f32 v65, v65\n"
    "v_smfmac_f32_32x32x32_bf16 v[88:103], a[72:75], v[56:63], v2\n"
    "buffer_load_dwordx4 a[232:235], v1, s[24:27], s57 offen offset:2048 sc1\n"
    "v_exp_f32 v66, v66\n"
    "v_exp_f32 v67, v67\n"
    "v_exp_f32 v68, v68\n"
    "v_exp_f32 v69, v69\n"
    "v_mfma_f32_32x32x4_2b_bf16 v[24:55], a[80:81], v[170:171], v[24:55]\n"
    "v_exp_f32 v70, v70\n"
    "v_exp_f32 v71, v71\n"
    "v_exp_f32 v72, v72\n"
    "v_exp_f32 v73, v73\n"
    "v_exp_f32 v74, v74\n"
    "v_exp_f32 v75, v75\n"
    "v_exp_f32 v76, v76\n"
    "v_exp_f32 v77, v77\n"
    "v_smfmac_f32_32x32x32_bf16 v[88:103], a[76:79], v[64:71], v2\n"
    "buffer_load_dwordx4 a[236:239], v1, s[24:27], s57 offen offset:3072 sc1\n"
    "v_exp_f32 v78, v78\n"
    "v_exp_f32 v79, v79\n"
    "v_exp_f32 v80, v80\n"
    "v_exp_f32 v81, v81\n"
    "v_smfmac_f32_32x32x32_bf16 v[104:119], a[72:75], v[72:79], v2\n"
    "s_add_u32 s57, s78, 0xf000\n"
    "buffer_load_dwordx4 a[240:243], v1, s[24:27], s57 offen offset:0 sc1\n"
    "v_exp_f32 v82, v82\n"
    "v_exp_f32 v83, v83\n"
    "v_exp_f32 v84, v84\n"
    "v_mfma_f32_32x32x16_bf16 v[56:71], a[60:63], v[160:163], 0\n"
    "v_exp_f32 v85, v85\n"
    "v_exp_f32 v86, v86\n"
    "v_exp_f32 v87, v87\n"
    "v_exp_f32 v24, v24\n"
    "v_exp_f32 v25, v25\n"
    "v_smfmac_f32_32x32x32_bf16 v[104:119], a[76:79], v[80:87], v2\n"
    "buffer_load_dwordx4 a[244:247], v1, s[24:27], s57 offen offset:1024 sc1\n"
    "v_exp_f32 v26, v26\n"
    "v_exp_f32 v27, v27\n"
    "v_exp_f32 v28, v28\n"
    "v_mfma_f32_32x32x16_bf16 v[72:87], a[60:63], v[164:167], 0\n"
    "v_exp_f32 v29, v29\n"
    "v_exp_f32 v30, v30\n"
    "v_exp_f32 v31, v31\n"
    "v_exp_f32 v32, v32\n"
    "v_exp_f32 v33, v33\n"
    "v_smfmac_f32_32x32x32_bf16 v[120:135], a[64:67], v[24:31], v2\n"
    "buffer_load_dwordx4 a[248:251], v1, s[24:27], s57 offen offset:2048 sc1\n"
    "v_exp_f32 v34, v34\n"
    "v_exp_f32 v35, v35\n"
    "v_exp_f32 v36, v36\n"
    "v_exp_f32 v37, v37\n"
    "v_mfma_f32_32x32x4_2b_bf16 v[56:87], a[82:83], v[170:171], v[56:87]\n"
    "v_exp_f32 v38, v38\n"
    "v_exp_f32 v39, v39\n"
    "v_exp_f32 v40, v40\n"
    "v_exp_f32 v41, v41\n"
    "v_exp_f32 v42, v42\n"
    "v_exp_f32 v43, v43\n"
    "v_exp_f32 v44, v44\n"
    "v_exp_f32 v45, v45\n"
    "v_smfmac_f32_32x32x32_bf16 v[120:135], a[68:71], v[32:39], v2\n"
    "buffer_load_dwordx4 a[252:255], v1, s[24:27], s57 offen offset:3072 sc1\n"
    "v_exp_f32 v46, v46\n"
    "v_exp_f32 v47, v47\n"
    "v_exp_f32 v48, v48\n"
    "v_exp_f32 v49, v49\n"
    "v_smfmac_f32_32x32x32_bf16 v[136:151], a[64:67], v[40:47], v2\n"
    "s_add_u32 s57, s78, 0\n"
    "v_exp_f32 v50, v50\n"
    "v_exp_f32 v51, v51\n"
    "v_exp_f32 v52, v52\n"
    "v_mfma_f32_32x32x16_bf16 v[24:39], a[84:87], v[152:155], 0\n"
    "buffer_load_dwordx4 a[0:3], v1, s[24:27], s57 offen offset:0 sc1\n"
    "v_exp_f32 v53, v53\n"
    "v_exp_f32 v54, v54\n"
    "v_exp_f32 v55, v55\n"
    "v_exp_f32 v56, v56\n"
    "v_exp_f32 v57, v57\n"
    "v_smfmac_f32_32x32x32_bf16 v[136:151], a[68:71], v[48:55], v2\n"
    "v_exp_f32 v58, v58\n"
    "v_exp_f32 v59, v59\n"
    "v_exp_f32 v60, v60\n"
    "v_mfma_f32_32x32x16_bf16 v[40:55], a[84:87], v[156:159], 0\n"
    "buffer_load_dwordx4 a[4:7], v1, s[24:27], s57 offen offset:1024 sc1\n"
    "v_exp_f32 v61, v61\n"
    "v_exp_f32 v62, v62\n"
    "v_exp_f32 v63, v63\n"
    "v_exp_f32 v64, v64\n"
    "v_exp_f32 v65, v65\n"
    "v_smfmac_f32_32x32x32_bf16 v[120:135], a[72:75], v[56:63], v2\n"
    "v_exp_f32 v66, v66\n"
    "v_exp_f32 v67, v67\n"
    "v_exp_f32 v68, v68\n"
    "v_exp_f32 v69, v69\n"
    "v_mfma_f32_32x32x4_2b_bf16 v[24:55], a[108:109], v[168:169], v[24:55]\n"
    "v_exp_f32 v70, v70\n"
    "v_exp_f32 v71, v71\n"
    "v_exp_f32 v72, v72\n"
    "v_exp_f32 v73, v73\n"
    "v_exp_f32 v74, v74\n"
    "v_exp_f32 v75, v75\n"
    "v_exp_f32 v76, v76\n"
    "v_exp_f32 v77, v77\n"
    "v_smfmac_f32_32x32x32_bf16 v[120:135], a[76:79], v[64:71], v2\n"
    "buffer_load_dwordx4 a[8:11], v1, s[24:27], s57 offen offset:2048 sc1\n"
    "v_exp_f32 v78, v78\n"
    "v_exp_f32 v79, v79\n"
    "v_exp_f32 v80, v80\n"
    "v_exp_f32 v81, v81\n"
    "v_smfmac_f32_32x32x32_bf16 v[136:151], a[72:75], v[72:79], v2\n"
    "buffer_load_dwordx4 a[12:15], v1, s[24:27], s57 offen offset:3072 sc1\n"
    "v_exp_f32 v82, v82\n"
    "v_exp_f32 v83, v83\n"
    "v_exp_f32 v84, v84\n"
    "v_mfma_f32_32x32x16_bf16 v[56:71], a[88:91], v[152:155], 0\n"
    "s_add_u32 s57, s78, 0x1000\n"
    "v_exp_f32 v85, v85\n"
    "v_exp_f32 v86, v86\n"
    "v_exp_f32 v87, v87\n"
    "v_exp_f32 v24, v24\n"
    "v_exp_f32 v25, v25\n"
    "v_smfmac_f32_32x32x32_bf16 v[136:151], a[76:79], v[80:87], v2\n"
    "buffer_load_dwordx4 a[16:19], v1, s[24:27], s57 offen offset:0 sc1\n"
    "v_exp_f32 v26, v26\n"
    "v_exp_f32 v27, v27\n"
    "v_exp_f32 v28, v28\n"
    "v_mfma_f32_32x32x16_bf16 v[72:87], a[88:91], v[156:159], 0\n"
    "v_exp_f32 v29, v29\n"
    "v_exp_f32 v30, v30\n"
    "v_exp_f32 v31, v31\n"
    "v_exp_f32 v32, v32\n"
    "v_exp_f32 v33, v33\n"
    "v_smfmac_f32_32x32x32_bf16 v[88:103], a[92:95], v[24:31], v2\n"
    "buffer_load_dwordx4 a[20:23], v1, s[24:27], s57 offen offset:1024 sc1\n"
    "v_exp_f32 v34, v34\n"
    "v_exp_f32 v35, v35\n"
    "v_exp_f32 v36, v36\n"
    "v_exp_f32 v37, v37\n"
    "v_mfma_f32_32x32x4_2b_bf16 v[56:87], a[110:111], v[168:169], v[56:87]\n"
    "v_exp_f32 v38, v38\n"
    "v_exp_f32 v39, v39\n"
    "v_exp_f32 v40, v40\n"
    "v_exp_f32 v41, v41\n"
    "v_exp_f32 v42, v42\n"
    "v_exp_f32 v43, v43\n"
    "v_exp_f32 v44, v44\n"
    "v_exp_f32 v45, v45\n"
    "v_smfmac_f32_32x32x32_bf16 v[88:103], a[96:99], v[32:39], v2\n"
    "s_waitcnt vmcnt(12)\n"
    "s_barrier\n"
    "buffer_load_dwordx4 a[24:27], v1, s[24:27], s57 offen offset:2048 sc1\n"
    "v_mov_b32 v16, s6\n"
    "v_mov_b32 v17, s7\n"
    "v_exp_f32 v46, v46\n"
    "v_exp_f32 v47, v47\n"
    "v_exp_f32 v48, v48\n"
    "v_exp_f32 v49, v49\n"
    "v_smfmac_f32_32x32x32_bf16 v[104:119], a[92:95], v[40:47], v2\n"
    "buffer_store_dwordx2 v[16:17], v14, s[24:27], s84 offen offset:72 sc1\n"
    "s_add_u32 s57, s78, 0x1c00\n"
    "v_exp_f32 v50, v50\n"
    "v_exp_f32 v51, v51\n"
    "v_exp_f32 v52, v52\n"
    "v_mfma_f32_32x32x16_bf16 v[24:39], a[84:87], v[160:163], 0\n"
    "v_exp_f32 v53, v53\n"
    "v_exp_f32 v54, v54\n"
    "v_exp_f32 v55, v55\n"
    "v_exp_f32 v56, v56\n"
    "v_exp_f32 v57, v57\n"
    "v_smfmac_f32_32x32x32_bf16 v[104:119], a[96:99], v[48:55], v2\n"
    "buffer_load_dwordx4 a[28:31], v1, s[24:27], s57 offen offset:0 sc1\n"
    "v_exp_f32 v58, v58\n"
    "v_exp_f32 v59, v59\n"
    "v_exp_f32 v60, v60\n"
    "v_mfma_f32_32x32x16_bf16 v[40:55], a[84:87], v[164:167], 0\n"
    "v_exp_f32 v61, v61\n"
    "v_exp_f32 v62, v62\n"
    "v_exp_f32 v63, v63\n"
    "v_exp_f32 v64, v64\n"
    "v_exp_f32 v65, v65\n"
    "v_smfmac_f32_32x32x32_bf16 v[88:103], a[100:103], v[56:63], v2\n"
    "buffer_load_dwordx4 a[32:35], v1, s[24:27], s57 offen offset:1024 sc1\n"
    "v_exp_f32 v66, v66\n"
    "v_exp_f32 v67, v67\n"
    "v_exp_f32 v68, v68\n"
    "v_exp_f32 v69, v69\n"
    "v_mfma_f32_32x32x4_2b_bf16 v[24:55], a[108:109], v[170:171], v[24:55]\n"
    "v_exp_f32 v70, v70\n"
    "v_exp_f32 v71, v71\n"
    "v_exp_f32 v72, v72\n"
    "v_exp_f32 v73, v73\n"
    "v_exp_f32 v74, v74\n"
    "v_exp_f32 v75, v75\n"
    "v_exp_f32 v76, v76\n"
    "v_exp_f32 v77, v77\n"
    "v_smfmac_f32_32x32x32_bf16 v[88:103], a[104:107], v[64:71], v2\n"
    "buffer_load_dwordx4 a[36:39], v1, s[24:27], s57 offen offset:2048 sc1\n"
    "v_exp_f32 v78, v78\n"
    "v_exp_f32 v79, v79\n"
    "v_exp_f32 v80, v80\n"
    "v_exp_f32 v81, v81\n"
    "v_smfmac_f32_32x32x32_bf16 v[104:119], a[100:103], v[72:79], v2\n"
    "buffer_load_dwordx4 a[40:43], v1, s[24:27], s57 offen offset:3072 sc1\n"
    "v_exp_f32 v82, v82\n"
    "v_exp_f32 v83, v83\n"
    "v_exp_f32 v84, v84\n"
    "v_mfma_f32_32x32x16_bf16 v[56:71], a[88:91], v[160:163], 0\n"
    "s_add_u32 s57, s78, 0x2c00\n"
    "v_exp_f32 v85, v85\n"
    "v_exp_f32 v86, v86\n"
    "v_exp_f32 v87, v87\n"
    "v_exp_f32 v24, v24\n"
    "v_exp_f32 v25, v25\n"
    "v_smfmac_f32_32x32x32_bf16 v[104:119], a[104:107], v[80:87], v2\n"
    "buffer_load_dwordx4 a[44:47], v1, s[24:27], s57 offen offset:0 sc1\n"
    "v_exp_f32 v26, v26\n"
    "v_exp_f32 v27, v27\n"
    "v_exp_f32 v28, v28\n"
    "v_mfma_f32_32x32x16_bf16 v[72:87], a[88:91], v[164:167], 0\n"
    "v_exp_f32 v29, v29\n"
    "v_exp_f32 v30, v30\n"
    "v_exp_f32 v31, v31\n"
    "v_exp_f32 v32, v32\n"
    "v_exp_f32 v33, v33\n"
    "v_smfmac_f32_32x32x32_bf16 v[120:135], a[92:95], v[24:31], v2\n"
    "buffer_load_dwordx4 a[48:51], v1, s[24:27], s57 offen offset:1024 sc1\n"
    "v_exp_f32 v34, v34\n"
    "v_exp_f32 v35, v35\n"
    "v_exp_f32 v36, v36\n"
    "v_exp_f32 v37, v37\n"
    "v_mfma_f32_32x32x4_2b_bf16 v[56:87], a[110:111], v[170:171], v[56:87]\n"
    "v_exp_f32 v38, v38\n"
    "v_exp_f32 v39, v39\n"
    "v_exp_f32 v40, v40\n"
    "v_exp_f32 v41, v41\n"
    "v_exp_f32 v42, v42\n"
    "v_exp_f32 v43, v43\n"
    "v_exp_f32 v44, v44\n"
    "v_exp_f32 v45, v45\n"
    "v_smfmac_f32_32x32x32_bf16 v[120:135], a[96:99], v[32:39], v2\n"
    "buffer_load_dwordx4 a[52:55], v1, s[24:27], s57 offen offset:2048 sc1\n"
    "v_exp_f32 v46, v46\n"
    "v_exp_f32 v47, v47\n"
    "v_exp_f32 v48, v48\n"
    "v_exp_f32 v49, v49\n"
    "v_smfmac_f32_32x32x32_bf16 v[136:151], a[92:95], v[40:47], v2\n"
    "s_add_u32 s57, s78, 0x3800\n"
    "v_exp_f32 v50, v50\n"
    "v_exp_f32 v51, v51\n"
    "v_exp_f32 v52, v52\n"
    "v_mfma_f32_32x32x16_bf16 v[24:39], a[112:115], v[152:155], 0\n"
    "buffer_load_dwordx4 a[56:59], v1, s[24:27], s57 offen offset:0 sc1\n"
    "v_exp_f32 v53, v53\n"
    "v_exp_f32 v54, v54\n"
    "v_exp_f32 v55, v55\n"
    "v_exp_f32 v56, v56\n"
    "v_exp_f32 v57, v57\n"
    "v_smfmac_f32_32x32x32_bf16 v[136:151], a[96:99], v[48:55], v2\n"
    "v_exp_f32 v58, v58\n"
    "v_exp_f32 v59, v59\n"
    "v_exp_f32 v60, v60\n"
    "v_mfma_f32_32x32x16_bf16 v[40:55], a[112:115], v[156:159], 0\n"
    "buffer_load_dwordx4 a[60:63], v1, s[24:27], s57 offen offset:1024 sc1\n"
    "v_exp_f32 v61, v61\n"
    "v_exp_f32 v62, v62\n"
    "v_exp_f32 v63, v63\n"
    "v_exp_f32 v64, v64\n"
    "v_exp_f32 v65, v65\n"
    "v_smfmac_f32_32x32x32_bf16 v[120:135], a[100:103], v[56:63], v2\n"
    "v_exp_f32 v66, v66\n"
    "v_exp_f32 v67, v67\n"
    "v_exp_f32 v68, v68\n"
    "v_exp_f32 v69, v69\n"
    "v_mfma_f32_32x32x4_2b_bf16 v[24:55], a[136:137], v[168:169], v[24:55]\n"
    "v_exp_f32 v70, v70\n"
    "v_exp_f32 v71, v71\n"
    "v_exp_f32 v72, v72\n"
    "v_exp_f32 v73, v73\n"
    "v_exp_f32 v74, v74\n"
    "v_exp_f32 v75, v75\n"
    "v_exp_f32 v76, v76\n"
    "v_exp_f32 v77, v77\n"
    "v_smfmac_f32_32x32x32_bf16 v[120:135], a[104:107], v[64:71], v2\n"
    "buffer_load_dwordx4 a[64:67], v1, s[24:27], s57 offen offset:2048 sc1\n"
    "v_exp_f32 v78, v78\n"
    "v_exp_f32 v79, v79\n"
    "v_exp_f32 v80, v80\n"
    "v_exp_f32 v81, v81\n"
    "v_smfmac_f32_32x32x32_bf16 v[136:151], a[100:103], v[72:79], v2\n"
    "buffer_load_dwordx4 a[68:71], v1, s[24:27], s57 offen offset:3072 sc1\n"
    "v_exp_f32 v82, v82\n"
    "v_exp_f32 v83, v83\n"
    "v_exp_f32 v84, v84\n"
    "v_mfma_f32_32x32x16_bf16 v[56:71], a[116:119], v[152:155], 0\n"
    "s_add_u32 s57, s78, 0x4800\n"
    "v_exp_f32 v85, v85\n"
    "v_exp_f32 v86, v86\n"
    "v_exp_f32 v87, v87\n"
    "v_exp_f32 v24, v24\n"
    "v_exp_f32 v25, v25\n"
    "v_smfmac_f32_32x32x32_bf16 v[136:151], a[104:107], v[80:87], v2\n"
    "buffer_load_dwordx4 a[72:75], v1, s[24:27], s57 offen offset:0 sc1\n"
    "v_exp_f32 v26, v26\n"
    "v_exp_f32 v27, v27\n"
    "v_exp_f32 v28, v28\n"
    "v_mfma_f32_32x32x16_bf16 v[72:87], a[116:119], v[156:159], 0\n"
    "v_exp_f32 v29, v29\n"
    "v_exp_f32 v30, v30\n"
    "v_exp_f32 v31, v31\n"
    "v_exp_f32 v32, v32\n"
    "v_exp_f32 v33, v33\n"
    "v_smfmac_f32_32x32x32_bf16 v[88:103], a[120:123], v[24:31], v2\n"
    "buffer_load_dwordx4 a[76:79], v1, s[24:27], s57 offen offset:1024 sc1\n"
    "v_exp_f32 v34, v34\n"
    "v_exp_f32 v35, v35\n"
    "v_exp_f32 v36, v36\n"
    "v_exp_f32 v37, v37\n"
    "v_mfma_f32_32x32x4_2b_bf16 v[56:87], a[138:139], v[168:169], v[56:87]\n"
    "v_exp_f32 v38, v38\n"
    "v_exp_f32 v39, v39\n"
    "v_exp_f32 v40, v40\n"
    "v_exp_f32 v41, v41\n"
    "v_exp_f32 v42, v42\n"
    "v_exp_f32 v43, v43\n"
    "v_exp_f32 v44, v44\n"
    "v_exp_f32 v45, v45\n"
    "v_smfmac_f32_32x32x32_bf16 v[88:103], a[124:127], v[32:39], v2\n"
    "buffer_load_dwordx4 a[80:83], v1, s[24:27], s57 offen offset:2048 sc1\n"
    "v_exp_f32 v46, v46\n"
    "v_exp_f32 v47, v47\n"
    "v_exp_f32 v48, v48\n"
    "v_exp_f32 v49, v49\n"
    "v_smfmac_f32_32x32x32_bf16 v[104:119], a[120:123], v[40:47], v2\n"
    "s_mul_i32 s56, s41, 0xc00\n"
    "s_add_u32 m0, s56, 0x1c000\n"
    "v_exp_f32 v50, v50\n"
    "v_exp_f32 v51, v51\n"
    "v_exp_f32 v52, v52\n"
    "v_mfma_f32_32x32x16_bf16 v[24:39], a[112:115], v[160:163], 0\n"
    "s_sub_u32 s56, s56, s79\n"
    "v_exp_f32 v53, v53\n"
    "v_exp_f32 v54, v54\n"
    "v_exp_f32 v55, v55\n"
    "v_exp_f32 v56, v56\n"
    "v_exp_f32 v57, v57\n"
    "v_smfmac_f32_32x32x32_bf16 v[104:119], a[124:127], v[48:55], v2\n"
    "s_add_u32 s56, s56, s76\n"
    "s_add_u32 s56, s56, 0x3000\n"
    "buffer_load_dwordx4 v1, s[24:27], s56 offen offset:0 sc1 lds\n"
    "v_exp_f32 v58, v58\n"
    "v_exp_f32 v59, v59\n"
    "v_exp_f32 v60, v60\n"
    "v_mfma_f32_32x32x16_bf16 v[40:55], a[112:115], v[164:167], 0\n"
    "v_exp_f32 v61, v61\n"
    "v_exp_f32 v62, v62\n"
    "v_exp_f32 v63, v63\n"
    "v_exp_f32 v64, v64\n"
    "v_exp_f32 v65, v65\n"
    "v_smfmac_f32_32x32x32_bf16 v[88:103], a[128:131], v[56:63], v2\n"
    "Lhop5:\n"
    "s_cbranch_execz LhopA\n"
    "buffer_load_dwordx4 v1, s[24:27], s56 offen offset:1024 sc1 lds\n"
    "v_exp_f32 v66, v66\n"
    "v_exp_f32 v67, v67\n"
    "v_exp_f32 v68, v68\n"
    "v_exp_f32 v69, v69\n"
    "v_mfma_f32_32x32x4_2b_bf16 v[24:55], a[136:137], v[170:171], v[24:55]\n"
    "v_exp_f32 v70, v70\n"
    "v_exp_f32 v71, v71\n"
    "v_exp_f32 v72, v72\n"
    "v_exp_f32 v73, v73\n"
    "v_exp_f32 v74, v74\n"
    "v_exp_f32 v75, v75\n"
    "v_exp_f32 v76, v76\n"
    "v_exp_f32 v77, v77\n"
    "v_smfmac_f32_32x32x32_bf16 v[88:103], a[132:135], v[64:71], v2\n"
    "buffer_load_dwordx4 v1, s[24:27], s56 offen offset:2048 sc1 lds\n"
    "v_lshlrev_b32 v23, 3, v1\n"
    "v_and_b32 v23, 0x3ff, v23\n"
    "v_exp_f32 v78, v78\n"
    "v_exp_f32 v79, v79\n"
    "v_exp_f32 v80, v80\n"
    "v_exp_f32 v81, v81\n"
    "v_smfmac_f32_32x32x32_bf16 v[104:119], a[128:131], v[72:79], v2\n"
    "s_and_b32 s59, s84, 0xfffffc00\n"
    "buffer_load_dwordx2 v[244:245], v23, s[24:27], s59 offen offset:72 sc1\n"
    "v_exp_f32 v82, v82\n"
    "v_exp_f32 v83, v83\n"
    "v_exp_f32 v84, v84\n"
    "v_mfma_f32_32x32x16_bf16 v[56:71], a[116:119], v[160:163], 0\n"
    "s_add_u32 s57, s78, 0x5400\n"
    "v_exp_f32 v85, v85\n"
    "v_exp_f32 v86, v86\n"
    "v_exp_f32 v87, v87\n"
    "v_exp_f32 v24, v24\n"
    "v_exp_f32 v25, v25\n"
    "v_smfmac_f32_32x32x32_bf16 v[104:119], a[132:135], v[80:87], v2\n"
    "buffer_load_dwordx4 a[84:87], v1, s[24:27], s57 offen offset:0 sc1\n"
    "v_exp_f32 v26, v26\n"
    "v_exp_f32 v27, v27\n"
    "v_exp_f32 v28, v28\n"
    "v_mfma_f32_32x32x16_bf16 v[72:87], a[116:119], v[164:167], 0\n"
    "v_exp_f32 v29, v29\n"
    "v_exp_f32 v30, v30\n"
    "v_exp_f32 v31, v31\n"
    "v_exp_f32 v32, v32\n"
    "v_exp_f32 v33, v33\n"
    "v_smfmac_f32_32x32x32_bf16 v[120:135], a[120:123], v[24:31], v2\n"
    "buffer_load_dwordx4 a[88:91], v1, s[24:27], s57 offen offset:1024 sc1\n"
    "v_exp_f32 v34, v34\n"
    "v_exp_f32 v35, v35\n"
    "v_exp_f32 v36, v36\n"
    "v_exp_f32 v37, v37\n"
    "v_mfma_f32_32x32x4_2b_bf16 v[56:87], a[138:139], v[170:171], v[56:87]\n"
    "v_exp_f32 v38, v38\n"
    "v_exp_f32 v39, v39\n"
    "v_exp_f32 v40, v40\n"
    "v_exp_f32 v41, v41\n"
    "v_exp_f32 v42, v42\n"
    "v_exp_f32 v43, v43\n"
    "v_exp_f32 v44, v44\n"
    "v_exp_f32 v45, v45\n"
    "v_smfmac_f32_32x32x32_bf16 v[120:135], a[124:127], v[32:39], v2\n"
    "buffer_load_dwordx4 a[92:95], v1, s[24:27], s57 offen offset:2048 sc1\n"
    "v_exp_f32 v46, v46\n"
    "v_exp_f32 v47, v47\n"
    "v_exp_f32 v48, v48\n"
    "v_exp_f32 v49, v49\n"
    "v_smfmac_f32_32x32x32_bf16 v[136:151], a[120:123], v[40:47], v2\n"
    "buffer_load_dwordx4 a[96:99], v1, s[24:27], s57 offen offset:3072 sc1\n"
    "v_exp_f32 v50, v50\n"
    "v_exp_f32 v51, v51\n"
    "v_exp_f32 v52, v52\n"
    "v_mfma_f32_32x32x16_bf16 v[24:39], a[140:143], v[152:155], 0\n"
    "s_add_u32 s57, s78, 0x6400\n"
    "v_exp_f32 v53, v53\n"
    "v_exp_f32 v54, v54\n"
    "v_exp_f32 v55, v55\n"
    "v_exp_f32 v56, v56\n"
    "v_exp_f32 v57, v57\n"
    "v_smfmac_f32_32x32x32_bf16 v[136:151], a[124:127], v[48:55], v2\n"
    "buffer_load_dwordx4 a[100:103], v1, s[24:27], s57 offen offset:0 sc1\n"
    "v_exp_f32 v58, v58\n"
    "v_exp_f32 v59, v59\n"
    "v_exp_f32 v60, v60\n"
    "v_mfma_f32_32x32x16_bf16 v[40:55], a[140:143], v[156:159], 0\n"
    "v_exp_f32 v61, v61\n"
    "v_exp_f32 v62, v62\n"
    "v_exp_f32 v63, v63\n"
    "v_exp_f32 v64, v64\n"
    "v_exp_f32 v65, v65\n"
    "v_smfmac_f32_32x32x32_bf16 v[120:135], a[128:131], v[56:63], v2\n"
    "buffer_load_dwordx4 a[104:107], v1, s[24:27], s57 offen offset:1024 sc1\n"
    "v_exp_f32 v66, v66\n"
    "v_exp_f32 v67, v67\n"
    "v_exp_f32 v68, v68\n"
    "v_exp_f32 v69, v69\n"
    "v_mfma_f32_32x32x4_2b_bf16 v[24:55], a[164:165], v[168:169], v[24:55]\n"
    "v_exp_f32 v70, v70\n"
    "v_exp_f32 v71, v71\n"
    "v_exp_f32 v72, v72\n"
    "v_exp_f32 v73, v73\n"
    "v_exp_f32 v74, v74\n"
    "v_exp_f32 v75, v75\n"
    "v_exp_f32 v76, v76\n"
    "v_exp_f32 v77, v77\n"
    "v_smfmac_f32_32x32x32_bf16 v[120:135], a[132:135], v[64:71], v2\n"
    "buffer_load_dwordx4 a[108:111], v1, s[24:27], s57 offen offset:2048 sc1\n"
    "v_exp_f32 v78, v78\n"
    "v_exp_f32 v79, v79\n"
    "v_exp_f32 v80, v80\n"
    "v_exp_f32 v81, v81\n"
    "v_smfmac_f32_32x32x32_bf16 v[136:151], a[128:131], v[72:79], v2\n"
    "s_waitcnt vmcnt(7)\n"
    "v_cmp_ne_u64 vcc, s[6:7], v[244:245]\n"
    "s_nop 0\n"
    "s_cbranch_vccnz Lspin13\n"
    "Lseen14:\n"
    "s_add_u32 s55, s63, 0\n"
    "buffer_load_dwordx4 v[172:175], v1, s[24:27], s55 offen offset:0 sc1\n"
    "v_exp_f32 v82, v82\n"
    "v_exp_f32 v83, v83\n"
    "v_exp_f32 v84, v84\n"
    "v_mfma_f32_32x32x16_bf16 v[56:71], a[144:147], v[152:155], 0\n"
    "v_exp_f32 v85, v85\n"
    "v_exp_f32 v86, v86\n"
    "v_exp_f32 v87, v87\n"
    "v_exp_f32 v24, v24\n"
    "v_exp_f32 v25, v25\n"
    "v_smfmac_f32_32x32x32_bf16 v[136:151], a[132:135], v[80:87], v2\n"
    "buffer_load_dwordx4 v[176:179], v1, s[24:27], s55 offen offset:1024 sc1\n"
    "v_exp_f32 v26, v26\n"
    "v_exp_f32 v27, v27\n"
    "v_exp_f32 v28, v28\n"
    "v_mfma_f32_32x32x16_bf16 v[72:87], a[144:147], v[156:159], 0\n"
    "v_exp_f32 v29, v29\n"
    "v_exp_f32 v30, v30\n"
    "v_exp_f32 v31, v31\n"
    "v_exp_f32 v32, v32\n"
    "v_exp_f32 v33, v33\n"
    "v_smfmac_f32_32x32x32_bf16 v[88:103], a[148:151], v[24:31], v2\n"
    "buffer_load_dwordx4 v[180:183], v1, s[24:27], s55 offen offset:2048 sc1\n"
    "v_exp_f32 v34, v34\n"
    "v_exp_f32 v35, v35\n"
    "v_exp_f32 v36, v36\n"
    "v_exp_f32 v37, v37\n"
    "v_mfma_f32_32x32x4_2b_bf16 v[56:87], a[166:167], v[168:169], v[56:87]\n"
    "v_exp_f32 v38, v38\n"
    "v_exp_f32 v39, v39\n"
    "v_exp_f32 v40, v40\n"
    "v_exp_f32 v41, v41\n"
    "v_exp_f32 v42, v42\n"
    "v_exp_f32 v43, v43\n"
    "v_exp_f32 v44, v44\n"
    "v_exp_f32 v45, v45\n"
    "v_smfmac_f32_32x32x32_bf16 v[88:103], a[152:155], v[32:39], v2\n"
    "buffer_load_dwordx4 v[184:187], v1, s[24:27], s55 offen offset:3072 sc1\n"
    "v_exp_f32 v46, v46\n"
    "v_exp_f32 v47, v47\n"
    "v_exp_f32 v48, v48\n"
    "v_exp_f32 v49, v49\n"
    "v_smfmac_f32_32x32x32_bf16 v[104:119], a[148:151], v[40:47], v2\n"
    "s_add_u32 s55, s63, 0x1000\n"
    "buffer_load_dwordx4 v[188:191], v1, s[24:27], s55 offen offset:0 sc1\n"
    "v_exp_f32 v50, v50\n"
    "v_exp_f32 v51, v51\n"
    "v_exp_f32 v52, v52\n"
    "v_mfma_f32_32x32x16_bf16 v[24:39], a[140:143], v[160:163], 0\n"
    "v_exp_f32 v53, v53\n"
    "v_exp_f32 v54, v54\n"
    "v_exp_f32 v55, v55\n"
    "v_exp_f32 v56, v56\n"
    "v_exp_f32 v57, v57\n"
    "v_smfmac_f32_32x32x32_bf16 v[104:119], a[152:155], v[48:55], v2\n"
    "buffer_load_dwordx4 v[192:195], v1, s[24:27], s55 offen offset:1024 sc1\n"
    "v_exp_f32 v58, v58\n"
    "v_exp_f32 v59, v59\n"
    "v_exp_f32 v60, v60\n"
    "v_mfma_f32_32x32x16_bf16 v[40:55], a[140:143], v[164:167], 0\n"
    "v_exp_f32 v61, v61\n"
    "v_exp_f32 v62, v62\n"
    "v_exp_f32 v63, v63\n"
    "v_exp_f32 v64, v64\n"
    "v_exp_f32 v65, v65\n"
    "v_smfmac_f32_32x32x32_bf16 v[88:103], a[156:159], v[56:63], v2\n"
    "buffer_load_dwordx4 v[196:199], v1, s[24:27], s55 offen offset:2048 sc1\n"
    "v_exp_f32 v66, v66\n"
    "v_exp_f32 v67, v67\n"
    "v_exp_f32 v68, v68\n"
    "v_exp_f32 v69, v69\n"
    "v_mfma_f32_32x32x4_2b_bf16 v[24:55], a[164:165], v[170:171], v[24:55]\n"
    "v_exp_f32 v70, v70\n"
    "v_exp_f32 v71, v71\n"
    "v_exp_f32 v72, v72\n"
    "v_exp_f32 v73, v73\n"
    "v_exp_f32 v74, v74\n"
    "v_exp_f32 v75, v75\n"
    "v_exp_f32 v76, v76\n"
    "v_exp_f32 v77, v77\n"
    "v_smfmac_f32_32x32x32_bf16 v[88:103], a[160:163], v[64:71], v2\n"
    "buffer_load_dwordx4 v[200:203], v1, s[24:27], s55 offen offset:3072 sc1\n"
    "v_exp_f32 v78, v78\n"
    "v_exp_f32 v79, v79\n"
    "v_exp_f32 v80, v80\n"
    "v_exp_f32 v81, v81\n"
    "v_smfmac_f32_32x32x32_bf16 v[104:119], a[156:159], v[72:79], v2\n"
    "s_add_u32 s55, s63, 0x2000\n"
    "v_exp_f32 v82, v82\n"
    "v_exp_f32 v83, v83\n"
    "v_exp_f32 v84, v84\n"
    "v_mfma_f32_32x32x16_bf16 v[56:71], a[144:147], v[160:163], 0\n"
    "buffer_load_dwordx4 v[204:207], v1, s[24:27], s55 offen offset:0 sc1\n"
    "v_exp_f32 v85, v85\n"
    "v_exp_f32 v86, v86\n"
    "v_exp_f32 v87, v87\n"
    "v_exp_f32 v24, v24\n"
    "v_exp_f32 v25, v25\n"
    "v_smfmac_f32_32x32x32_bf16 v[104:119], a[160:163], v[80:87], v2\n"
    "v_exp_f32 v26, v26\n"
    "v_exp_f32 v27, v27\n"
    "v_exp_f32 v28, v28\n"
    "v_mfma_f32_32x32x16_bf16 v[72:87], a[144:147], v[164:167], 0\n"
    "buffer_load_dwordx4 v[208:211], v1, s[24:27], s55 offen offset:1024 sc1\n"
    "v_exp_f32 v29, v29\n"
    "v_exp_f32 v30, v30\n"
    "v_exp_f32 v31, v31\n"
    "v_exp_f32 v32, v32\n"
    "v_exp_f32 v33, v33\n"
    "v_smfmac_f32_32x32x32_bf16 v[120:135], a[148:151], v[24:31], v2\n"
    "s_add_u32 s57, s78, 0x10000\n"
    "v_exp_f32 v34, v34\n"
    "v_exp_f32 v35, v35\n"
    "v_exp_f32 v36, v36\n"
    "v_exp_f32 v37, v37\n"
    "v_mfma_f32_32x32x4_2b_bf16 v[56:87], a[166:167], v[170:171], v[56:87]\n"
    "v_exp_f32 v38, v38\n"
    "v_exp_f32 v39, v39\n"
    "v_exp_f32 v40, v40\n"
    "v_exp_f32 v41, v41\n"
    "v_exp_f32 v42, v42\n"
    "v_exp_f32 v43, v43\n"
    "v_exp_f32 v44, v44\n"
    "v_exp_f32 v45, v45\n"
    "v_smfmac_f32_32x32x32_bf16 v[120:135], a[152:155], v[32:39], v2\n"
    "buffer_load_dwordx4 v[232:235], v1, s[24:27], s57 offen offset:0 sc1\n"
    "v_exp_f32 v46, v46\n"
    "v_exp_f32 v47, v47\n"
    "v_exp_f32 v48, v48\n"
    "v_exp_f32 v49, v49\n"
    "v_smfmac_f32_32x32x32_bf16 v[136:151], a[148:151], v[40:47], v2\n"
    "buffer_load_dwordx4 v[236:239], v10, s[24:27], s57 offen offset:1024 sc1\n"
    "v_exp_f32 v50, v50\n"
    "v_exp_f32 v51, v51\n"
    "v_exp_f32 v52, v52\n"
    "v_mfma_f32_32x32x16_bf16 v[24:39], a[168:171], v[152:155], 0\n"
    "v_exp_f32 v53, v53\n"
    "v_exp_f32 v54, v54\n"
    "v_exp_f32 v55, v55\n"
    "v_exp_f32 v56, v56\n"
    "v_exp_f32 v57, v57\n"
    "v_smfmac_f32_32x32x32_bf16 v[136:151], a[152:155], v[48:55], v2\n"
    "buffer_load_dwordx4 v[240:243], v10, s[24:27], s57 offen offset:2048 sc1\n"
    "v_exp_f32 v58, v58\n"
    "v_exp_f32 v59, v59\n"
    "v_exp_f32 v60, v60\n"
    "v_mfma_f32_32x32x16_bf16 v[40:55], a[168:171], v[156:159], 0\n"
    "v_exp_f32 v61, v61\n"
    "v_exp_f32 v62, v62\n"
    "v_exp_f32 v63, v63\n"
    "v_exp_f32 v64, v64\n"
    "v_exp_f32 v65, v65\n"
    "v_smfmac_f32_32x32x32_bf16 v[120:135], a[156:159], v[56:63], v2\n"
    "buffer_load_dwordx4 v[244:247], v10, s[24:27], s57 offen offset:3072 sc1\n"
    "v_exp_f32 v66, v66\n"
    "v_exp_f32 v67, v67\n"
    "v_exp_f32 v68, v68\n"
    "v_exp_f32 v69, v69\n"
    "v_mfma_f32_32x32x4_2b_bf16 v[24:55], a[192:193], v[168:169], v[24:55]\n"
    "v_exp_f32 v70, v70\n"
    "v_exp_f32 v71, v71\n"
    "v_exp_f32 v72, v72\n"
    "v_exp_f32 v73, v73\n"
    "v_exp_f32 v74, v74\n"
    "v_exp_f32 v75, v75\n"
    "v_exp_f32 v76, v76\n"
    "v_exp_f32 v77, v77\n"
    "v_smfmac_f32_32x32x32_bf16 v[120:135], a[160:163], v[64:71], v2\n"
    "s_add_u32 s57, s78, 0x11000\n"
    "buffer_load_dwordx4 v[248:251], v10, s[24:27], s57 offen offset:0 sc1\n"
    "v_exp_f32 v78, v78\n"
    "v_exp_f32 v79, v79\n"
    "v_exp_f32 v80, v80\n"
    "v_exp_f32 v81, v81\n"
    "v_smfmac_f32_32x32x32_bf16 v[136:151], a[156:159], v[72:79], v2\n"
    "v_exp_f32 v82, v82\n"
    "v_exp_f32 v83, v83\n"
    "v_exp_f32 v84, v84\n"
    "v_mfma_f32_32x32x16_bf16 v[56:71], a[172:175], v[152:155], 0\n"
    "buffer_load_dwordx4 v[252:255], v10, s[24:27], s57 offen offset:1024 sc1\n"
    "v_exp_f32 v85, v85\n"
    "v_exp_f32 v86, v86\n"
    "v_exp_f32 v87, v87\n"
    "v_exp_f32 v24, v24\n"
    "v_exp_f32 v25, v25\n"
    "v_smfmac_f32_32x32x32_bf16 v[136:151], a[160:163], v[80:87], v2\n"
    "s_getpc_b64 s[20:21]\n"
    "Lrw:\n"
    "s_mov_b32 s22, Lend-Lrw\n"
    "v_exp_f32 v26, v26\n"
    "v_exp_f32 v27, v27\n"
    "v_exp_f32 v28, v28\n"
    "v_mfma_f32_32x32x16_bf16 v[72:87], a[172:175], v[156:159], 0\n"
    "s_bfe_u32 s54, s40, 0x40003\n"
    "v_exp_f32 v29, v29\n"
    "v_exp_f32 v30, v30\n"
    "v_exp_f32 v31, v31\n"
    "v_exp_f32 v32, v32\n"
    "v_exp_f32 v33, v33\n"
    "v_smfmac_f32_32x32x32_bf16 v[88:103], a[176:179], v[24:31], v2\n"
    "s_lshl_b32 s54, s54, 2\n"
    "s_add_u32 s54, s54, s41\n"
    "v_exp_f32 v34, v34\n"
    "v_exp_f32 v35, v35\n"
    "v_exp_f32 v36, v36\n"
    "v_exp_f32 v37, v37\n"
    "v_mfma_f32_32x32x4_2b_bf16 v[56:87], a[194:195], v[168:169], v[56:87]\n"
    "v_exp_f32 v38, v38\n"
    "v_exp_f32 v39, v39\n"
    "v_exp_f32 v40, v40\n"
    "v_exp_f32 v41, v41\n"
    "v_exp_f32 v42, v42\n"
    "v_exp_f32 v43, v43\n"
    "v_exp_f32 v44, v44\n"
    "v_exp_f32 v45, v45\n"
    "v_smfmac_f32_32x32x32_bf16 v[88:103], a[180:183], v[32:39], v2\n"
    "s_lshl_b32 s54, s54, 9\n"
    "s_add_u32 s57, s78, 0x7000\n"
    "buffer_load_dwordx4 a[112:115], v1, s[24:27], s57 offen offset:0 sc1\n"
    "v_min_u32 v14, 48, v1\n"
    "v_lshlrev_b32 v14, 3, v14\n"
    "v_exp_f32 v46, v46\n"
    "v_exp_f32 v47, v47\n"
    "v_exp_f32 v48, v48\n"
    "v_exp_f32 v49, v49\n"
    "v_smfmac_f32_32x32x32_bf16 v[104:119], a[176:179], v[40:47], v2\n"
    "buffer_load_dword v15, v14, s[20:23], s54 offen\n"
    "v_exp_f32 v50, v50\n"
    "v_exp_f32 v51, v51\n"
    "v_exp_f32 v52, v52\n"
    "v_mfma_f32_32x32x16_bf16 v[24:39], a[168:171], v[160:163], 0\n"
    "v_exp_f32 v53, v53\n"
    "v_exp_f32 v54, v54\n"
    "v_exp_f32 v55, v55\n"
    "v_exp_f32 v56, v56\n"
    "v_exp_f32 v57, v57\n"
    "v_smfmac_f32_32x32x32_bf16 v[104:119], a[180:183], v[48:55], v2\n"
    "buffer_load_dwordx4 a[116:119], v1, s[24:27], s57 offen offset:1024 sc1\n"
    "v_exp_f32 v58, v58\n"
    "v_exp_f32 v59, v59\n"
    "v_exp_f32 v60, v60\n"
    "v_mfma_f32_32x32x16_bf16 v[40:55], a[168:171], v[164:167], 0\n"
    "v_exp_f32 v61, v61\n"
    "v_exp_f32 v62, v62\n"
    "v_exp_f32 v63, v63\n"
    "v_exp_f32 v64, v64\n"
    "v_exp_f32 v65, v65\n"
    "v_smfmac_f32_32x32x32_bf16 v[88:103], a[184:187], v[56:63], v2\n"
    "buffer_load_dwordx4 a[120:123], v1, s[24:27], s57 offen offset:2048 sc1\n"
    "v_exp_f32 v66, v66\n"
    "v_exp_f32 v67, v67\n"
    "v_exp_f32 v68, v68\n"
    "v_exp_f32 v69, v69\n"
    "v_mfma_f32_32x32x4_2b_bf16 v[24:55], a[192:193], v[170:171], v[24:55]\n"
    "v_exp_f32 v70, v70\n"
    "v_exp_f32 v71, v71\n"
    "v_exp_f32 v72, v72\n"
    "v_exp_f32 v73, v73\n"
    "v_exp_f32 v74, v74\n"
    "v_exp_f32 v75, v75\n"
    "v_exp_f32 v76, v76\n"
    "v_exp_f32 v77, v77\n"
    "v_smfmac_f32_32x32x32_bf16 v[88:103], a[188:191], v[64:71], v2\n"
    "buffer_load_dwordx4 a[124:127], v1, s[24:27], s57 offen offset:3072 sc1\n"
    "v_exp_f32 v78, v78\n"
    "v_exp_f32 v79, v79\n"
    "v_exp_f32 v80, v80\n"
    "v_exp_f32 v81, v81\n"
    "v_smfmac_f32_32x32x32_bf16 v[104:119], a[184:187], v[72:79], v2\n"
    "s_add_u32 s57, s78, 0x8000\n"
    "buffer_load_dwordx4 a[128:131], v1, s[24:27], s57 offen offset:0 sc1\n"
    "v_exp_f32 v82, v82\n"
    "v_exp_f32 v83, v83\n"
    "v_exp_f32 v84, v84\n"
    "v_mfma_f32_32x32x16_bf16 v[56:71], a[172:175], v[160:163], 0\n"
    "v_exp_f32 v85, v85\n"
    "v_exp_f32 v86, v86\n"
    "v_exp_f32 v87, v87\n"
    "v_exp_f32 v24, v24\n"
    "v_exp_f32 v25, v25\n"
    "v_smfmac_f32_32x32x32_bf16 v[104:119], a[188:191], v[80:87], v2\n"
    "buffer_load_dwordx4 a[132:135], v1, s[24:27], s57 offen offset:1024 sc1\n"
    "v_exp_f32 v26, v26\n"
    "v_exp_f32 v27, v27\n"
    "v_exp_f32 v28, v28\n"
    "v_mfma_f32_32x32x16_bf16 v[72:87], a[172:175], v[164:167], 0\n"
    "v_exp_f32 v29, v29\n"
    "v_exp_f32 v30, v30\n"
    "v_exp_f32 v31, v31\n"
    "v_exp_f32 v32, v32\n"
    "v_exp_f32 v33, v33\n"
    "v_smfmac_f32_32x32x32_bf16 v[120:135], a[176:179], v[24:31], v2\n"
    "buffer_load_dwordx4 a[136:139], v1, s[24:27], s57 offen offset:2048 sc1\n"
    "v_exp_f32 v34, v34\n"
    "v_exp_f32 v35, v35\n"
    "v_exp_f32 v36, v36\n"
    "v_exp_f32 v37, v37\n"
    "v_mfma_f32_32x32x4_2b_bf16 v[56:87], a[194:195], v[170:171], v[56:87]\n"
    "v_exp_f32 v38, v38\n"
    "v_exp_f32 v39, v39\n"
    "v_exp_f32 v40, v40\n"
    "v_exp_f32 v41, v41\n"
    "v_exp_f32 v42, v42\n"
    "v_exp_f32 v43, v43\n"
    "v_exp_f32 v44, v44\n"
    "v_exp_f32 v45, v45\n"
    "v_smfmac_f32_32x32x32_bf16 v[120:135], a[180:183], v[32:39], v2\n"
    "s_add_u32 s57, s78, 0x8c00\n"
    "buffer_load_dwordx4 a[140:143], v1, s[24:27], s57 offen offset:0 sc1\n"
    "v_exp_f32 v46, v46\n"
    "v_exp_f32 v47, v47\n"
    "v_exp_f32 v48, v48\n"
    "v_exp_f32 v49, v49\n"
    "v_smfmac_f32_32x32x32_bf16 v[136:151], a[176:179], v[40:47], v2\n"
    "v_exp_f32 v50, v50\n"
    "v_exp_f32 v51, v51\n"
    "v_exp_f32 v52, v52\n"
    "v_mfma_f32_32x32x16_bf16 v[24:39], a[196:199], v[152:155], 0\n"
    "buffer_load_dwordx4 a[144:147], v1, s[24:27], s57 offen offset:1024 sc1\n"
    "v_exp_f32 v53, v53\n"
    "v_exp_f32 v54, v54\n"
    "v_exp_f32 v55, v55\n"
    "v_exp_f32 v56, v56\n"
    "v_exp_f32 v57, v57\n"
    "v_smfmac_f32_32x32x32_bf16 v[136:151], a[180:183], v[48:55], v2\n"
    "v_exp_f32 v58, v58\n"
    "v_exp_f32 v59, v59\n"
    "v_exp_f32 v60, v60\n"
    "v_mfma_f32_32x32x16_bf16 v[40:55], a[196:199], v[156:159], 0\n"
    "buffer_load_dwordx4 a[148:151], v1, s[24:27], s57 offen offset:2048 sc1\n"
    "v_exp_f32 v61, v61\n"
    "v_exp_f32 v62, v62\n"
    "v_exp_f32 v63, v63\n"
    "v_exp_f32 v64, v64\n"
    "v_exp_f32 v65, v65\n"
    "v_smfmac_f32_32x32x32_bf16 v[120:135], a[184:187], v[56:63], v2\n"
    "v_exp_f32 v66, v66\n"
    "v_exp_f32 v67, v67\n"
    "v_exp_f32 v68, v68\n"
    "v_exp_f32 v69, v69\n"
    "v_mfma_f32_32x32x4_2b_bf16 v[24:55], a[220:221], v[168:169], v[24:55]\n"
    "v_exp_f32 v70, v70\n"
    "v_exp_f32 v71, v71\n"
    "v_exp_f32 v72, v72\n"
    "v_exp_f32 v73, v73\n"
    "v_exp_f32 v74, v74\n"
    "v_exp_f32 v75, v75\n"
    "v_exp_f32 v76, v76\n"
    "v_exp_f32 v77, v77\n"
    "v_smfmac_f32_32x32x32_bf16 v[120:135], a[188:191], v[64:71], v2\n"
    "buffer_load_dwordx4 a[152:155], v1, s[24:27], s57 offen offset:3072 sc1\n"
    "v_exp_f32 v78, v78\n"
    "v_exp_f32 v79, v79\n"
    "v_exp_f32 v80, v80\n"
    "v_exp_f32 v81, v81\n"
    "v_smfmac_f32_32x32x32_bf16 v[136:151], a[184:187], v[72:79], v2\n"
    "s_add_u32 s57, s78, 0x9c00\n"
    "buffer_load_dwordx4 a[156:159], v1, s[24:27], s57 offen offset:0 sc1\n"
    "v_exp_f32 v82, v82\n"
    "v_exp_f32 v83, v83\n"
    "v_exp_f32 v84, v84\n"
    "v_mfma_f32_32x32x16_bf16 v[56:71], a[200:203], v[152:155], 0\n"
    "v_exp_f32 v85, v85\n"
    "v_exp_f32 v86, v86\n"
    "v_exp_f32 v87, v87\n"
    "v_exp_f32 v24, v24\n"
    "v_exp_f32 v25, v25\n"
    "v_smfmac_f32_32x32x32_bf16 v[136:151], a[188:191], v[80:87], v2\n"
    "buffer_load_dwordx4 a[160:163], v1, s[24:27], s57 offen offset:1024 sc1\n"
    "v_exp_f32 v26, v26\n"
    "v_exp_f32 v27, v27\n"
    "v_exp_f32 v28, v28\n"
    "v_mfma_f32_32x32x16_bf16 v[72:87], a[200:203], v[156:159], 0\n"
    "v_exp_f32 v29, v29\n"
    "v_exp_f32 v30, v30\n"
    "v_exp_f32 v31, v31\n"
    "v_exp_f32 v32, v32\n"
    "v_exp_f32 v33, v33\n"
    "v_smfmac_f32_32x32x32_bf16 v[88:103], a[204:207], v[24:31], v2\n"
    "buffer_load_dwordx4 a[164:167], v1, s[24:27], s57 offen offset:2048 sc1\n"
    "v_exp_f32 v34, v34\n"
    "v_exp_f32 v35, v35\n"
    "v_exp_f32 v36, v36\n"
    "v_exp_f32 v37, v37\n"
    "v_mfma_f32_32x32x4_2b_bf16 v[56:87], a[222:223], v[168:169], v[56:87]\n"
    "v_exp_f32 v38, v38\n"
    "v_exp_f32 v39, v39\n"
    "v_exp_f32 v40, v40\n"
    "v_exp_f32 v41, v41\n"
    "v_exp_f32 v42, v42\n"
    "v_exp_f32 v43, v43\n"
    "v_exp_f32 v44, v44\n"
    "v_exp_f32 v45, v45\n"
    "v_smfmac_f32_32x32x32_bf16 v[88:103], a[208:211], v[32:39], v2\n"
    "s_mul_i32 s50, s41, 0x2400\n"
    "s_add_u32 s50, s50, 0x1f000\n"
    "v_add_u32 v21, s50, v1\n"
    "v_exp_f32 v46, v46\n"
    "v_exp_f32 v47, v47\n"
    "v_exp_f32 v48, v48\n"
    "v_exp_f32 v49, v49\n"
    "v_smfmac_f32_32x32x32_bf16 v[104:119], a[204:207], v[40:47], v2\n"
    "s_xor_b32 s50, s75, 2\n"
    "s_mul_i32 s50, s50, 0x2400\n"
    "v_exp_f32 v50, v50\n"
    "v_exp_f32 v51, v51\n"
    "v_exp_f32 v52, v52\n"
    "v_mfma_f32_32x32x16_bf16 v[24:39], a[196:199], v[160:163], 0\n"
    "s_add_u32 s50, s50, 0x20800\n"
    "v_add_u32 v20, s50, v1\n"
    "v_exp_f32 v53, v53\n"
    "v_exp_f32 v54, v54\n"
    "v_exp_f32 v55, v55\n"
    "v_exp_f32 v56, v56\n"
    "v_exp_f32 v57, v57\n"
    "v_smfmac_f32_32x32x32_bf16 v[104:119], a[208:211], v[48:55], v2\n"
    "s_add_u32 s57, s78, 0xa800\n"
    "buffer_load_dwordx4 a[168:171], v1, s[24:27], s57 offen offset:0 sc1\n"
    "v_exp_f32 v58, v58\n"
    "v_exp_f32 v59, v59\n"
    "v_exp_f32 v60, v60\n"
    "v_mfma_f32_32x32x16_bf16 v[40:55], a[196:199], v[164:167], 0\n"
    "v_exp_f32 v61, v61\n"
    "v_exp_f32 v62, v62\n"
    "v_exp_f32 v63, v63\n"
    "v_exp_f32 v64, v64\n"
    "v_exp_f32 v65, v65\n"
    "v_smfmac_f32_32x32x32_bf16 v[88:103], a[212:215], v[56:63], v2\n"
    "buffer_load_dwordx4 a[172:175], v1, s[24:27], s57 offen offset:1024 sc1\n"
    "v_exp_f32 v66, v66\n"
    "v_exp_f32 v67, v67\n"
    "v_exp_f32 v68, v68\n"
    "v_exp_f32 v69, v69\n"
    "v_mfma_f32_32x32x4_2b_bf16 v[24:55], a[220:221], v[170:171], v[24:55]\n"
    "v_exp_f32 v70, v70\n"
    "v_exp_f32 v71, v71\n"
    "v_exp_f32 v72, v72\n"
    "v_exp_f32 v73, v73\n"
    "v_exp_f32 v74, v74\n"
    "v_exp_f32 v75, v75\n"
    "v_exp_f32 v76, v76\n"
    "v_exp_f32 v77, v77\n"
    "v_smfmac_f32_32x32x32_bf16 v[88:103], a[216:219], v[64:71], v2\n"
    "buffer_load_dwordx4 a[176:179], v1, s[24:27], s57 offen offset:2048 sc1\n"
    "v_exp_f32 v78, v78\n"
    "v_exp_f32 v79, v79\n"
    "v_exp_f32 v80, v80\n"
    "v_exp_f32 v81, v81\n"
    "v_smfmac_f32_32x32x32_bf16 v[104:119], a[212:215], v[72:79], v2\n"
    "buffer_load_dwordx4 a[180:183], v1, s[24:27], s57 offen offset:3072 sc1\n"
    "v_exp_f32 v82, v82\n"
    "v_exp_f32 v83, v83\n"
    "v_exp_f32 v84, v84\n"
    "v_mfma_f32_32x32x16_bf16 v[56:71], a[200:203], v[160:163], 0\n"
    "s_xor_b32 s50, s75, 0\n"
    "v_exp_f32 v85, v85\n"
    "v_exp_f32 v86, v86\n"
    "v_exp_f32 v87, v87\n"
    "v_exp_f32 v24, v24\n"
    "v_exp_f32 v25, v25\n"
    "v_smfmac_f32_32x32x32_bf16 v[104:119], a[216:219], v[80:87], v2\n"
    "s_mul_i32 s50, s50, 0x2400\n"
    "s_add_u32 s50, s50, 0x1f000\n"
    "v_add_u32 v22, s50, v1\n"
    "v_exp_f32 v26, v26\n"
    "v_exp_f32 v27, v27\n"
    "v_exp_f32 v28, v28\n"
    "v_mfma_f32_32x32x16_bf16 v[72:87], a[200:203], v[164:167], 0\n"
    "s_add_u32 s57, s78, 0xb800\n"
    "buffer_load_dwordx4 a[184:187], v1, s[24:27], s57 offen offset:0 sc1\n"
    "ds_write_b128 v22, v[88:91] offset:0\n"
    "v_exp_f32 v29, v29\n"
    "v_exp_f32 v30, v30\n"
    "v_exp_f32 v31, v31\n"
    "v_exp_f32 v32, v32\n"
    "v_exp_f32 v33, v33\n"
    "v_smfmac_f32_32x32x32_bf16 v[120:135], a[204:207], v[24:31], v2\n"
    "buffer_load_dwordx4 a[188:191], v1, s[24:27], s57 offen offset:1024 sc1\n"
    "ds_write_b128 v22, v[92:95] offset:1024\n"
    "v_exp_f32 v34, v34\n"
    "v_exp_f32 v35, v35\n"
    "v_exp_f32 v36, v36\n"
    "v_exp_f32 v37, v37\n"
    "v_mfma_f32_32x32x4_2b_bf16 v[56:87], a[222:223], v[170:171], v[56:87]\n"
    "v_exp_f32 v38, v38\n"
    "v_exp_f32 v39, v39\n"
    "v_exp_f32 v40, v40\n"
    "v_exp_f32 v41, v41\n"
    "v_exp_f32 v42, v42\n"
    "v_exp_f32 v43, v43\n"
    "v_exp_f32 v44, v44\n"
    "v_exp_f32 v45, v45\n"
    "v_smfmac_f32_32x32x32_bf16 v[120:135], a[208:211], v[32:39], v2\n"
    "buffer_load_dwordx4 a[192:195], v1, s[24:27], s57 offen offset:2048 sc1\n"
    "ds_write_b128 v22, v[96:99] offset:2048\n"
    "v_exp_f32 v46, v46\n"
    "v_exp_f32 v47, v47\n"
    "v_exp_f32 v48, v48\n"
    "v_exp_f32 v49, v49\n"
    "v_smfmac_f32_32x32x32_bf16 v[136:151], a[204:207], v[40:47], v2\n"
    "s_xor_b32 s50, s75, 1\n"
    "s_mul_i32 s50, s50, 0x2400\n"
    "v_exp_f32 v50, v50\n"
    "v_exp_f32 v51, v51\n"
    "v_exp_f32 v52, v52\n"
    "s_add_u32 s50, s50, 0x1fc00\n"
    "v_add_u32 v22, s50, v1\n"
    "v_exp_f32 v53, v53\n"
    "v_exp_f32 v54, v54\n"
    "v_exp_f32 v55, v55\n"
    "v_exp_f32 v56, v56\n"
    "v_exp_f32 v57, v57\n"
    "v_smfmac_f32_32x32x32_bf16 v[136:151], a[208:211], v[48:55], v2\n"
    "s_add_u32 s57, s78, 0xc400\n"
    "buffer_load_dwordx4 a[196:199], v1, s[24:27], s57 offen offset:0 sc1\n"
    "ds_write_b128 v22, v[104:107] offset:0\n"
    "v_exp_f32 v58, v58\n"
    "v_exp_f32 v59, v59\n"
    "v_exp_f32 v60, v60\n"
    "s_add_u32 s57, s78, 0xc800\n"
    "buffer_load_dwordx4 a[200:203], v1, s[24:27], s57 offen offset:0 sc1\n"
    "ds_write_b128 v22, v[108:111] offset:1024\n"
    "v_exp_f32 v61, v61\n"
    "v_exp_f32 v62, v62\n"
    "v_exp_f32 v63, v63\n"
    "v_exp_f32 v64, v64\n"
    "v_exp_f32 v65, v65\n"
    "v_smfmac_f32_32x32x32_bf16 v[120:135], a[212:215], v[56:63], v2\n"
    "s_add_u32 s57, s78, 0xdc00\n"
    "ds_write_b128 v22, v[112:115] offset:2048\n"
    "v_exp_f32 v66, v66\n"
    "v_exp_f32 v67, v67\n"
    "v_exp_f32 v68, v68\n"
    "v_exp_f32 v69, v69\n"
    "v_exp_f32 v70, v70\n"
    "v_exp_f32 v71, v71\n"
    "v_exp_f32 v72, v72\n"
    "v_exp_f32 v73, v73\n"
    "v_exp_f32 v74, v74\n"
    "v_exp_f32 v75, v75\n"
    "v_exp_f32 v76, v76\n"
    "v_exp_f32 v77, v77\n"
    "v_smfmac_f32_32x32x32_bf16 v[120:135], a[216:219], v[64:71], v2\n"
    "s_waitcnt lgkmcnt(0)\n"
    "s_barrier\n"
    "buffer_load_dwordx4 a[220:223], v1, s[24:27], s57 offen offset:0 sc1\n"
    "ds_read_b128 v[88:91], v21 offset:0\n"
    "ds_read_b128 v[92:95], v21 offset:1024\n"
    "v_exp_f32 v78, v78\n"
    "v_exp_f32 v79, v79\n"
    "v_exp_f32 v80, v80\n"
    "v_exp_f32 v81, v81\n"
    "v_smfmac_f32_32x32x32_bf16 v[136:151], a[212:215], v[72:79], v2\n"
    "s_add_u32 s57, s78, 0xcc00\n"
    "buffer_load_dwordx4 a[204:207], v1, s[24:27], s57 offen offset:0 sc1\n"
    "ds_read_b128 v[96:99], v21 offset:2048\n"
    "ds_read_b128 v[100:103], v21 offset:3072\n"
    "v_exp_f32 v82, v82\n"
    "v_exp_f32 v83, v83\n"
    "v_exp_f32 v84, v84\n"
    "ds_read_b128 v[104:107], v21 offset:4096\n"
    "ds_read_b128 v[108:111], v21 offset:5120\n"
    "v_exp_f32 v85, v85\n"
    "v_exp_f32 v86, v86\n"
    "v_exp_f32 v87, v87\n"
    "s_nop 1\n"
    "v_smfmac_f32_32x32x32_bf16 v[136:151], a[216:219], v[80:87], v2\n"
    "ds_write_b128 v20, v[120:123] offset:0\n"
    "ds_write_b128 v20, v[124:127] offset:1024\n"
    "ds_write_b128 v20, v[128:131] offset:2048\n"
    "s_waitcnt lgkmcnt(5)\n"
    "v_pk_add_f32 v[88:89], v[88:89], v[100:101]\n"
    "v_pk_add_f32 v[90:91], v[90:91], v[102:103]\n"
    "s_waitcnt lgkmcnt(4)\n"
    "v_pk_add_f32 v[92:93], v[92:93], v[104:105]\n"
    "v_pk_add_f32 v[94:95], v[94:95], v[106:107]\n"
    "s_waitcnt lgkmcnt(3)\n"
    "v_pk_add_f32 v[96:97], v[96:97], v[108:109]\n"
    "v_pk_add_f32 v[98:99], v[98:99], v[110:111]\n"
    "v_pk_add_f32 v[88:89], v[88:89], v[136:137]\n"
    "v_pk_add_f32 v[90:91], v[90:91], v[138:139]\n"
    "v_pk_add_f32 v[92:93], v[92:93], v[140:141]\n"
    "v_pk_add_f32 v[94:95], v[94:95], v[142:143]\n"
    "v_pk_add_f32 v[96:97], v[96:97], v[144:145]\n"
    "v_pk_add_f32 v[98:99], v[98:99], v[146:147]\n"
    "s_waitcnt lgkmcnt(0)\n"
    "s_barrier\n"
    "ds_read_b128 v[160:163], v21 offset:8192\n"
    "ds_read_b128 v[152:155], v21 offset:6144\n"
    "ds_read_b128 v[156:159], v21 offset:7168\n"
    "s_waitcnt lgkmcnt(2)\n"
    "v_pk_add_f32 v[96:97], v[96:97], v[160:161]\n"
    "v_pk_add_f32 v[98:99], v[98:99], v[162:163]\n"
    "v_mov_b32 v16, v96\n"
    "v_mov_b32 v17, v96\n"
    "s_add_u32 s50, s61, 0x2800\n"
    "s_add_u32 s49, s62, 0x2800\n"
    "v_permlane32_swap_b32 v17, v16\n"
    "v_subrev_u32 v17, 0x1f800000, v16\n"
    "v_cmp_lt_u32 vcc, 0x40000000, v17\n"
    "v_rcp_f32 v16, v16\n"
    "s_waitcnt lgkmcnt(1)\n"
    "v_pk_add_f32 v[88:89], v[88:89], v[152:153]\n"
    "v_pk_add_f32 v[90:91], v[90:91], v[154:155]\n"
    "s_waitcnt lgkmcnt(0)\n"
    "v_pk_add_f32 v[92:93], v[92:93], v[156:157]\n"
    "v_pk_add_f32 v[94:95], v[94:95], v[158:159]\n"
    "v_pk_mul_f32 v[88:89], v[88:89], v[16:17] op_sel_hi:[1,0]\n"
    "v_pk_mul_f32 v[90:91], v[90:91], v[16:17] op_sel_hi:[1,0]\n"
    "v_pk_mul_f32 v[92:93], v[92:93], v[16:17] op_sel_hi:[1,0]\n"
    "v_pk_mul_f32 v[94:95], v[94:95], v[16:17] op_sel_hi:[1,0]\n"
    "v_pk_mul_f32 v[96:97], v[96:97], v[16:17] op_sel_hi:[1,0]\n"
    "v_pk_mul_f32 v[98:99], v[98:99], v[16:17] op_sel_hi:[1,0]\n"
    "s_cmp_eq_u64 vcc, 0\n"
    "s_cselect_b32 s51, 0, 0x40000000\n"
    "s_cselect_b32 s52, 0, 2\n"
    "s_or_b32 s74, s74, s52\n"
    "v_or_b32 v18, s51, v5\n"
    "v_or_b32 v19, s51, v6\n"
    "v_cvt_pk_bf16_f32 v88, v88, v89\n"
    "v_cvt_pk_bf16_f32 v89, v90, v91\n"
    "v_cvt_pk_bf16_f32 v90, v92, v93\n"
    "v_cvt_pk_bf16_f32 v91, v94, v95\n"
    "v_cvt_pk_bf16_f32 v92, v96, v97\n"
    "v_cvt_pk_bf16_f32 v93, v98, v99\n"
    "buffer_store_dwordx4 v[88:91], v18, s[24:27], s50 offen sc1\n"
    "buffer_store_dwordx2 v[92:93], v19, s[24:27], s49 offen sc1\n"
    "s_cmp_lg_u32 s74, 0\n"
    "s_cbranch_scc1 Lfb15\n"
    "Lfbret16:\n"
    "s_barrier\n"
    "ds_read_b128 v[56:59], v1 offset:0\n"
    "ds_read_b128 v[60:63], v1 offset:5120\n"
    "ds_read_b128 v[64:67], v1 offset:1024\n"
    "ds_read_b128 v[68:71], v1 offset:6144\n"
    "ds_read_b128 v[72:75], v1 offset:2048\n"
    "ds_read_b128 v[76:79], v1 offset:7168\n"
    "ds_read_b128 v[80:83], v1 offset:3072\n"
    "ds_read_b128 v[84:87], v1 offset:8192\n"
    "s_waitcnt vmcnt(42)\n"
    "v_max3_u32 v16, v172, v173, v174\n"
    "v_max3_u32 v16, v16, v175, v176\n"
    "v_max3_u32 v16, v16, v177, v178\n"
    "v_max3_u32 v16, v16, v179, v179\n"
    "v_cmp_eq_u32 vcc, -1, v16\n"
    "v_lshlrev_b32 v24, 16, v212\n"
    "v_and_b32 v25, 0xffff0000, v212\n"
    "v_lshlrev_b32 v26, 16, v213\n"
    "v_and_b32 v27, 0xffff0000, v213\n"
    "v_lshlrev_b32 v28, 16, v214\n"
    "v_and_b32 v29, 0xffff0000, v214\n"
    "v_lshlrev_b32 v30, 16, v215\n"
    "v_and_b32 v31, 0xffff0000, v215\n"
    "v_lshlrev_b32 v32, 16, v216\n"
    "v_and_b32 v33, 0xffff0000, v216\n"
    "v_lshlrev_b32 v34, 16, v217\n"
    "v_and_b32 v35, 0xffff0000, v217\n"
    "v_lshlrev_b32 v36, 16, v218\n"
    "v_and_b32 v37, 0xffff0000, v218\n"
    "v_lshlrev_b32 v38, 16, v219\n"
    "v_and_b32 v39, 0xffff0000, v219\n"
    "v_lshlrev_b32 v40, 16, v220\n"
    "Lhop6:\n"
    "s_cbranch_execz LhopA\n"
    "v_and_b32 v41, 0xffff0000, v220\n"
    "v_lshlrev_b32 v42, 16, v221\n"
    "v_and_b32 v43, 0xffff0000, v221\n"
    "s_cbranch_vccnz Lrty37\n"
    "Lok38:\n"
    "s_waitcnt vmcnt(34) lgkmcnt(4)\n"
    "v_mfma_f32_16x16x32_bf16 v[24:27], v[56:59], v[172:175], v[24:27]\n"
    "ds_read_b128 v[88:91], v1 offset:4096\n"
    "ds_read_b128 v[92:95], v1 offset:9216\n"
    "v_mfma_f32_16x16x32_bf16 v[24:27], v[60:63], v[176:179], v[24:27]\n"
    "s_add_u32 m0, s79, 0x1f000\n"
    "v_max3_u32 v16, v180, v181, v182\n"
    "v_max3_u32 v16, v16, v183, v184\n"
    "v_mfma_f32_16x16x32_bf16 v[28:31], v[64:67], v[172:175], v[28:31]\n"
    "s_add_u32 s56, s76, 0x6000\n"
    "ds_read_b128 v[56:59], v1 offset:10240\n"
    "ds_read_b128 v[60:63], v1 offset:15360\n"
    "buffer_load_dwordx4 v1, s[24:27], s56 offen offset:0 sc1 lds\n"
    "v_mfma_f32_16x16x32_bf16 v[28:31], v[68:71], v[176:179], v[28:31]\n"
    "s_waitcnt lgkmcnt(4)\n"
    "v_max3_u32 v16, v16, v185, v186\n"
    "v_max3_u32 v16, v16, v187, v187\n"
    "v_cmp_eq_u32 vcc, -1, v16\n"
    "v_mfma_f32_16x16x32_bf16 v[32:35], v[72:75], v[172:175], v[32:35]\n"
    "ds_read_b128 v[64:67], v1 offset:11264\n"
    "ds_read_b128 v[68:71], v1 offset:16384\n"
    "buffer_load_dwordx4 v1, s[24:27], s56 offen offset:1024 sc1 lds\n"
    "v_mfma_f32_16x16x32_bf16 v[32:35], v[76:79], v[176:179], v[32:35]\n"
    "s_cbranch_vccnz Lrty21\n"
    "Lok22:\n"
    "v_mfma_f32_16x16x32_bf16 v[36:39], v[80:83], v[172:175], v[36:39]\n"
    "ds_read_b128 v[72:75], v1 offset:12288\n"
    "ds_read_b128 v[76:79], v1 offset:17408\n"
    "buffer_load_dwordx4 v1, s[24:27], s56 offen offset:2048 sc1 lds\n"
    "v_mfma_f32_16x16x32_bf16 v[36:39], v[84:87], v[176:179], v[36:39]\n"
    "s_waitcnt lgkmcnt(4)\n"
    "v_mfma_f32_16x16x32_bf16 v[40:43], v[88:91], v[172:175], v[40:43]\n"
    "ds_read_b128 v[80:83], v1 offset:13312\n"
    "ds_read_b128 v[84:87], v1 offset:18432\n"
    "v_mfma_f32_16x16x32_bf16 v[40:43], v[92:95], v[176:179], v[40:43]\n"
    "v_mfma_f32_16x16x32_bf16 v[24:27], v[56:59], v[180:183], v[24:27]\n"
    "ds_read_b128 v[88:91], v1 offset:14336\n"
    "ds_read_b128 v[92:95], v1 offset:19456\n"
    "buffer_load_dwordx4 v1, s[24:27], s56 offen offset:3072 sc1 lds\n"
    "v_mfma_f32_16x16x32_bf16 v[24:27], v[60:63], v[184:187], v[24:27]\n"
    "s_waitcnt lgkmcnt(4)\n"
    "v_max3_u32 v16, v188, v189, v190\n"
    "v_max3_u32 v16, v16, v191, v192\n"
    "v_mfma_f32_16x16x32_bf16 v[28:31], v[64:67], v[180:183], v[28:31]\n"
    "s_add_u32 m0, s79, 0x23000\n"
    "ds_read_b128 v[56:59], v1 offset:20480\n"
    "ds_read_b128 v[60:63], v1 offset:25600\n"
    "v_mfma_f32_16x16x32_bf16 v[28:31], v[68:71], v[184:187], v[28:31]\n"
    "s_add_u32 s56, s76, 0xa000\n"
    "v_max3_u32 v16, v16, v193, v194\n"
    "v_max3_u32 v16, v16, v195, v195\n"
    "v_cmp_eq_u32 vcc, -1, v16\n"
    "v_mfma_f32_16x16x32_bf16 v[32:35], v[72:75], v[180:183], v[32:35]\n"
    "ds_read_b128 v[64:67], v1 offset:21504\n"
    "ds_read_b128 v[68:71], v1 offset:26624\n"
    "buffer_load_dwordx4 v1, s[24:27], s56 offen offset:0 sc1 lds\n"
    "v_mfma_f32_16x16x32_bf16 v[32:35], v[76:79], v[184:187], v[32:35]\n"
    "s_waitcnt lgkmcnt(4)\n"
    "v_mfma_f32_16x16x32_bf16 v[36:39], v[80:83], v[180:183], v[36:39]\n"
    "s_cbranch_vccnz Lrty23\n"
    "Lok24:\n"
    "ds_read_b128 v[72:75], v1 offset:22528\n"
    "ds_read_b128 v[76:79], v1 offset:27648\n"
    "buffer_load_dwordx4 v1, s[24:27], s56 offen offset:1024 sc1 lds\n"
    "v_mfma_f32_16x16x32_bf16 v[36:39], v[84:87], v[184:187], v[36:39]\n"
    "v_mfma_f32_16x16x32_bf16 v[40:43], v[88:91], v[180:183], v[40:43]\n"
    "ds_read_b128 v[80:83], v1 offset:23552\n"
    "ds_read_b128 v[84:87], v1 offset:28672\n"
    "v_mfma_f32_16x16x32_bf16 v[40:43], v[92:95], v[184:187], v[40:43]\n"
    "s_waitcnt lgkmcnt(4)\n"
    "v_mfma_f32_16x16x32_bf16 v[24:27], v[56:59], v[188:191], v[24:27]\n"
    "ds_read_b128 v[88:91], v1 offset:24576\n"
    "ds_read_b128 v[92:95], v1 offset:29696\n"
    "buffer_load_dwordx4 v1, s[24:27], s56 offen offset:2048 sc1 lds\n"
    "v_mfma_f32_16x16x32_bf16 v[24:27], v[60:63], v[192:195], v[24:27]\n"
    "v_max3_u32 v16, v196, v197, v198\n"
    "v_max3_u32 v16, v16, v199, v200\n"
    "v_mfma_f32_16x16x32_bf16 v[28:31], v[64:67], v[188:191], v[28:31]\n"
    "ds_read_b128 v[56:59], v1 offset:30720\n"
    "ds_read_b128 v[60:63], v1 offset:35840\n"
    "v_mfma_f32_16x16x32_bf16 v[28:31], v[68:71], v[192:195], v[28:31]\n"
    "s_waitcnt lgkmcnt(4)\n"
    "v_max3_u32 v16, v16, v201, v202\n"
    "v_max3_u32 v16, v16, v203, v203\n"
    "v_cmp_eq_u32 vcc, -1, v16\n"
    "v_mfma_f32_16x16x32_bf16 v[32:35], v[72:75], v[188:191], v[32:35]\n"
    "ds_read_b128 v[64:67], v1 offset:31744\n"
    "ds_read_b128 v[68:71], v1 offset:36864\n"
    "buffer_load_dwordx4 v1, s[24:27], s56 offen offset:3072 sc1 lds\n"
    "v_mfma_f32_16x16x32_bf16 v[32:35], v[76:79], v[192:195], v[32:35]\n"
    "s_cbranch_vccnz Lrty25\n"
    "Lok26:\n"
    "v_mfma_f32_16x16x32_bf16 v[36:39], v[80:83], v[188:191], v[36:39]\n"
    "ds_read_b128 v[72:75], v1 offset:32768\n"
    "ds_read_b128 v[76:79], v1 offset:37888\n"
    "v_mfma_f32_16x16x32_bf16 v[36:39], v[84:87], v[192:195], v[36:39]\n"
    "s_waitcnt lgkmcnt(4)\n"
    "v_mfma_f32_16x16x32_bf16 v[40:43], v[88:91], v[188:191], v[40:43]\n"
    "s_add_u32 s55, s63, 0x2800\n"
    "ds_read_b128 v[80:83], v1 offset:33792\n"
    "ds_read_b128 v[84:87], v1 offset:38912\n"
    "buffer_load_dwordx4 v[132:135], v1, s[24:27], s55 offen offset:0 sc1\n"
    "v_mfma_f32_16x16x32_bf16 v[40:43], v[92:95], v[192:195], v[40:43]\n"
    "v_mfma_f32_16x16x32_bf16 v[24:27], v[56:59], v[196:199], v[24:27]\n"
    "ds_read_b128 v[88:91], v1 offset:34816\n"
    "ds_read_b128 v[92:95], v1 offset:39936\n"
    "v_mfma_f32_16x16x32_bf16 v[24:27], v[60:63], v[200:203], v[24:27]\n"
    "s_waitcnt lgkmcnt(4)\n"
    "v_max3_u32 v16, v204, v205, v206\n"
    "v_max3_u32 v16, v16, v207, v208\n"
    "v_mfma_f32_16x16x32_bf16 v[28:31], v[64:67], v[196:199], v[28:31]\n"
    "ds_read_b128 v[56:59], v1 offset:40960\n"
    "ds_read_b128 v[60:63], v1 offset:46080\n"
    "buffer_load_dwordx4 v[136:139], v1, s[24:27], s55 offen offset:1024 sc1\n"
    "v_mfma_f32_16x16x32_bf16 v[28:31], v[68:71], v[200:203], v[28:31]\n"
    "v_max3_u32 v16, v16, v209, v210\n"
    "v_max3_u32 v16, v16, v211, v211\n"
    "v_cmp_eq_u32 vcc, -1, v16\n"
    "v_mfma_f32_16x16x32_bf16 v[32:35], v[72:75], v[196:199], v[32:35]\n"
    "ds_read_b128 v[64:67], v1 offset:41984\n"
    "ds_read_b128 v[68:71], v1 offset:47104\n"
    "buffer_load_dwordx4 v[140:143], v1, s[24:27], s55 offen offset:2048 sc1\n"
    "v_mfma_f32_16x16x32_bf16 v[32:35], v[76:79], v[200:203], v[32:35]\n"
    "s_waitcnt lgkmcnt(4)\n"
    "v_mfma_f32_16x16x32_bf16 v[36:39], v[80:83], v[196:199], v[36:39]\n"
    "s_cbranch_vccnz Lrty27\n"
    "Lok28:\n"
    "ds_read_b128 v[72:75], v1 offset:43008\n"
    "ds_read_b128 v[76:79], v1 offset:48128\n"
    "v_mfma_f32_16x16x32_bf16 v[36:39], v[84:87], v[200:203], v[36:39]\n"
    "v_mfma_f32_16x16x32_bf16 v[40:43], v[88:91], v[196:199], v[40:43]\n"
    "ds_read_b128 v[80:83], v1 offset:44032\n"
    "ds_read_b128 v[84:87], v1 offset:49152\n"
    "buffer_load_dwordx4 v[144:147], v1, s[24:27], s55 offen offset:3072 sc1\n"
    "v_mfma_f32_16x16x32_bf16 v[40:43], v[92:95], v[200:203], v[40:43]\n"
    "s_waitcnt lgkmcnt(4)\n"
    "v_mfma_f32_16x16x32_bf16 v[24:27], v[56:59], v[204:207], v[24:27]\n"
    "s_add_u32 s55, s63, 0x3800\n"
    "ds_read_b128 v[88:91], v1 offset:45056\n"
    "ds_read_b128 v[92:95], v1 offset:50176\n"
    "v_mfma_f32_16x16x32_bf16 v[24:27], v[60:63], v[208:211], v[24:27]\n"
    "v_mfma_f32_16x16x32_bf16 v[28:31], v[64:67], v[204:207], v[28:31]\n"
    "ds_read_b128 v[56:59], v1 offset:51200\n"
    "ds_read_b128 v[60:63], v1 offset:56320\n"
    "buffer_load_dwordx4 v[148:151], v1, s[24:27], s55 offen offset:0 sc1\n"
    "v_mfma_f32_16x16x32_bf16 v[28:31], v[68:71], v[208:211], v[28:31]\n"
    "s_waitcnt lgkmcnt(4)\n"
    "v_mfma_f32_16x16x32_bf16 v[32:35], v[72:75], v[204:207], v[32:35]\n"
    "ds_read_b128 v[64:67], v1 offset:52224\n"
    "ds_read_b128 v[68:71], v1 offset:57344\n"
    "v_mfma_f32_16x16x32_bf16 v[32:35], v[76:79], v[208:211], v[32:35]\n"
    "v_mfma_f32_16x16x32_bf16 v[36:39], v[80:83], v[204:207], v[36:39]\n"
    "ds_read_b128 v[72:75], v1 offset:53248\n"
    "ds_read_b128 v[76:79], v1 offset:58368\n"
    "buffer_load_dwordx4 v[152:155], v1, s[24:27], s55 offen offset:1024 sc1\n"
    "v_mfma_f32_16x16x32_bf16 v[36:39], v[84:87], v[208:211], v[36:39]\n"
    "s_waitcnt lgkmcnt(4)\n"
    "v_mfma_f32_16x16x32_bf16 v[40:43], v[88:91], v[204:207], v[40:43]\n"
    "ds_read_b128 v[80:83], v1 offset:54272\n"
    "ds_read_b128 v[84:87], v1 offset:59392\n"
    "v_mfma_f32_16x16x32_bf16 v[40:43], v[92:95], v[208:211], v[40:43]\n"
    "buffer_load_dwordx4 v[156:159], v1, s[24:27], s55 offen offset:2048 sc1\n"
    "s_barrier\n"
    "buffer_load_dwordx4 v[160:163], v1, s[24:27], s55 offen offset:3072 sc1\n"
    "s_add_u32 s55, s63, 0x4800\n"
    "buffer_load_dwordx4 v[164:167], v1, s[24:27], s55 offen offset:0 sc1\n"
    "buffer_load_dwordx4 v[168:171], v1, s[24:27], s55 offen offset:1024 sc1\n"
    "s_waitcnt vmcnt(8)\n"
    "v_max3_u32 v16, v132, v133, v134\n"
    "v_max3_u32 v16, v16, v135, v136\n"
    "v_max3_u32 v16, v16, v137, v138\n"
    "v_max3_u32 v16, v16, v139, v139\n"
    "v_cmp_eq_u32 vcc, -1, v16\n"
    "s_nop 0\n"
    "s_cbranch_vccnz Lrty39\n"
    "Lok40:\n"
    "v_mfma_f32_16x16x32_bf16 v[24:27], v[56:59], v[132:135], v[24:27]\n"
    "s_waitcnt vmcnt(6)\n"
    "ds_read_b128 v[88:91], v1 offset:55296\n"
    "ds_read_b128 v[92:95], v1 offset:60416\n"
    "v_mfma_f32_16x16x32_bf16 v[24:27], v[60:63], v[136:139], v[24:27]\n"
    "s_waitcnt lgkmcnt(4)\n"
    "v_max3_u32 v16, v140, v141, v142\n"
    "v_max3_u32 v16, v16, v143, v144\n"
    "v_mfma_f32_16x16x32_bf16 v[28:31], v[64:67], v[132:135], v[28:31]\n"
    "s_add_u32 s56, s79, 0x27000\n"
    "ds_read_b128 v[56:59], v1 offset:61440\n"
    "ds_read_b128 v[60:63], v3 offset:1024\n"
    "v_mfma_f32_16x16x32_bf16 v[28:31], v[68:71], v[136:139], v[28:31]\n"
    "s_sub_u32 m0, s56, 0x28000\n"
    "v_max3_u32 v16, v16, v145, v146\n"
    "v_max3_u32 v16, v16, v147, v147\n"
    "v_cmp_eq_u32 vcc, -1, v16\n"
    "v_mfma_f32_16x16x32_bf16 v[32:35], v[72:75], v[132:135], v[32:35]\n"
    "s_min_u32 m0, m0, s56\n"
    "ds_read_b128 v[64:67], v1 offset:62464\n"
    "ds_read_b128 v[68:71], v3 offset:2048\n"
    "v_mfma_f32_16x16x32_bf16 v[32:35], v[76:79], v[136:139], v[32:35]\n"
    "s_waitcnt lgkmcnt(4)\n"
    "v_mfma_f32_16x16x32_bf16 v[36:39], v[80:83], v[132:135], v[36:39]\n"
    "s_cbranch_vccnz Lrty29\n"
    "Lok30:\n"
    "ds_read_b128 v[72:75], v1 offset:63488\n"
    "ds_read_b128 v[76:79], v3 offset:3072\n"
    "v_mfma_f32_16x16x32_bf16 v[36:39], v[84:87], v[136:139], v[36:39]\n"
    "s_add_u32 s56, s76, 0xe000\n"
    "v_mfma_f32_16x16x32_bf16 v[40:43], v[88:91], v[132:135], v[40:43]\n"
    "ds_read_b128 v[80:83], v1 offset:64512\n"
    "ds_read_b128 v[84:87], v3 offset:4096\n"
    "buffer_load_dwordx4 v1, s[24:27], s56 offen offset:0 sc1 lds\n"
    "v_mfma_f32_16x16x32_bf16 v[40:43], v[92:95], v[136:139], v[40:43]\n"
    "s_waitcnt lgkmcnt(4)\n"
    "v_mfma_f32_16x16x32_bf16 v[24:27], v[56:59], v[140:143], v[24:27]\n"
    "s_waitcnt vmcnt(5)\n"
    "ds_read_b128 v[88:91], v3 offset:0\n"
    "ds_read_b128 v[92:95], v3 offset:5120\n"
    "buffer_load_dwordx4 v1, s[24:27], s56 offen offset:1024 sc1 lds\n"
    "v_mfma_f32_16x16x32_bf16 v[24:27], v[60:63], v[144:147], v[24:27]\n"
    "v_max3_u32 v16, v148, v149, v150\n"
    "v_max3_u32 v16, v16, v151, v152\n"
    "v_mfma_f32_16x16x32_bf16 v[28:31], v[64:67], v[140:143], v[28:31]\n"
    "ds_read_b128 v[56:59], v3 offset:6144\n"
    "ds_read_b128 v[60:63], v3 offset:11264\n"
    "v_mfma_f32_16x16x32_bf16 v[28:31], v[68:71], v[144:147], v[28:31]\n"
    "s_waitcnt lgkmcnt(4)\n"
    "v_max3_u32 v16, v16, v153, v154\n"
    "v_max3_u32 v16, v16, v155, v155\n"
    "v_cmp_eq_u32 vcc, -1, v16\n"
    "v_mfma_f32_16x16x32_bf16 v[32:35], v[72:75], v[140:143], v[32:35]\n"
    "ds_read_b128 v[64:67], v3 offset:7168\n"
    "ds_read_b128 v[68:71], v3 offset:12288\n"
    "buffer_load_dwordx4 v1, s[24:27], s56 offen offset:2048 sc1 lds\n"
    "v_mfma_f32_16x16x32_bf16 v[32:35], v[76:79], v[144:147], v[32:35]\n"
    "s_cbranch_vccnz Lrty31\n"
    "Lok32:\n"
    "v_mfma_f32_16x16x32_bf16 v[36:39], v[80:83], v[140:143], v[36:39]\n"
    "ds_read_b128 v[72:75], v3 offset:8192\n"
    "ds_read_b128 v[76:79], v3 offset:13312\n"
    "v_mfma_f32_16x16x32_bf16 v[36:39], v[84:87], v[144:147], v[36:39]\n"
    "s_waitcnt lgkmcnt(4)\n"
    "v_mfma_f32_16x16x32_bf16 v[40:43], v[88:91], v[140:143], v[40:43]\n"
    "ds_read_b128 v[80:83], v3 offset:9216\n"
    "ds_read_b128 v[84:87], v3 offset:14336\n"
    "buffer_load_dwordx4 v1, s[24:27], s56 offen offset:3072 sc1 lds\n"
    "v_mfma_f32_16x16x32_bf16 v[40:43], v[92:95], v[144:147], v[40:43]\n"
    "s_add_u32 m0, s79, 0x3000\n"
    "v_mfma_f32_16x16x32_bf16 v[24:27], v[56:59], v[148:151], v[24:27]\n"
    "s_waitcnt vmcnt(6)\n"
    "ds_read_b128 v[88:91], v3 offset:10240\n"
    "ds_read_b128 v[92:95], v3 offset:15360\n"
    "v_mfma_f32_16x16x32_bf16 v[24:27], v[60:63], v[152:155], v[24:27]\n"
    "s_waitcnt lgkmcnt(4)\n"
    "v_max3_u32 v16, v156, v157, v158\n"
    "v_max3_u32 v16, v16, v159, v160\n"
    "v_mfma_f32_16x16x32_bf16 v[28:31], v[64:67], v[148:151], v[28:31]\n"
    "s_add_u32 s56, s76, 0x12000\n"
    "ds_read_b128 v[56:59], v3 offset:16384\n"
    "ds_read_b128 v[60:63], v3 offset:21504\n"
    "v_mfma_f32_16x16x32_bf16 v[28:31], v[68:71], v[152:155], v[28:31]\n"
    "v_max3_u32 v16, v16, v161, v162\n"
    "v_max3_u32 v16, v16, v163, v163\n"
    "v_cmp_eq_u32 vcc, -1, v16\n"
    "v_mfma_f32_16x16x32_bf16 v[32:35], v[72:75], v[148:151], v[32:35]\n"
    "ds_read_b128 v[64:67], v3 offset:26624\n"
    "ds_read_b128 v[68:71], v3 offset:31744\n"
    "buffer_load_dwordx4 v1, s[24:27], s56 offen offset:0 sc1 lds\n"
    "v_mfma_f32_16x16x32_bf16 v[32:35], v[76:79], v[152:155], v[32:35]\n"
    "s_waitcnt lgkmcnt(4)\n"
    "v_mfma_f32_16x16x32_bf16 v[36:39], v[80:83], v[148:151], v[36:39]\n"
    "s_cbranch_vccnz Lrty33\n"
    "Lok34:\n"
    "ds_read_b128 v[72:75], v3 offset:17408\n"
    "ds_read_b128 v[76:79], v3 offset:22528\n"
    "v_mfma_f32_16x16x32_bf16 v[36:39], v[84:87], v[152:155], v[36:39]\n"
    "s_waitcnt vmcnt(5)\n"
    "v_max3_u32 v16, v164, v165, v166\n"
    "v_max3_u32 v16, v16, v167, v168\n"
    "v_mfma_f32_16x16x32_bf16 v[40:43], v[88:91], v[148:151], v[40:43]\n"
    "ds_read_b128 v[80:83], v3 offset:27648\n"
    "ds_read_b128 v[84:87], v3 offset:32768\n"
    "buffer_load_dwordx4 v1, s[24:27], s56 offen offset:1024 sc1 lds\n"
    "v_mfma_f32_16x16x32_bf16 v[40:43], v[92:95], v[152:155], v[40:43]\n"
    "s_waitcnt lgkmcnt(4)\n"
    "v_max3_u32 v16, v16, v169, v170\n"
    "v_max3_u32 v16, v16, v171, v171\n"
    "v_cmp_eq_u32 vcc, -1, v16\n"
    "s_nop 0\n"
    "s_cbranch_vccnz Lrty35\n"
    "Lok36:\n"
    "v_mfma_f32_16x16x32_bf16 v[24:27], v[56:59], v[156:159], v[24:27]\n"
    "ds_read_b128 v[88:91], v3 offset:18432\n"
    "ds_read_b128 v[92:95], v3 offset:23552\n"
    "buffer_load_dwordx4 v1, s[24:27], s56 offen offset:2048 sc1 lds\n"
    "v_mfma_f32_16x16x32_bf16 v[24:27], v[60:63], v[160:163], v[24:27]\n"
    "v_mfma_f32_16x16x32_bf16 v[24:27], v[64:67], v[164:167], v[24:27]\n"
    "ds_read_b128 v[56:59], v3 offset:28672\n"
    "ds_read_b128 v[60:63], v3 offset:33792\n"
    "v_mfma_f32_16x16x32_bf16 v[24:27], v[68:71], v[168:171], v[24:27]\n"
    "s_waitcnt lgkmcnt(4)\n"
    "v_mfma_f32_16x16x32_bf16 v[28:31], v[72:75], v[156:159], v[28:31]\n"
    "ds_read_b128 v[64:67], v3 offset:19456\n"
    "ds_read_b128 v[68:71], v3 offset:24576\n"
    "buffer_load_dwordx4 v1, s[24:27], s56 offen offset:3072 sc1 lds\n"
    "v_mfma_f32_16x16x32_bf16 v[28:31], v[76:79], v[160:163], v[28:31]\n"
    "s_add_u32 m0, s79, 0x7000\n"
    "v_mfma_f32_16x16x32_bf16 v[28:31], v[80:83], v[164:167], v[28:31]\n"
    "s_add_u32 s56, s76, 0x16000\n"
    "ds_read_b128 v[72:75], v3 offset:29696\n"
    "ds_read_b128 v[76:79], v3 offset:34816\n"
    "v_mfma_f32_16x16x32_bf16 v[28:31], v[84:87], v[168:171], v[28:31]\n"
    "s_waitcnt lgkmcnt(4)\n"
    "v_mfma_f32_16x16x32_bf16 v[32:35], v[88:91], v[156:159], v[32:35]\n"
    "ds_read_b128 v[80:83], v3 offset:20480\n"
    "ds_read_b128 v[84:87], v3 offset:25600\n"
    "buffer_load_dwordx4 v1, s[24:27], s56 offen offset:0 sc1 lds\n"
    "v_mfma_f32_16x16x32_bf16 v[32:35], v[92:95], v[160:163], v[32:35]\n"
    "v_mfma_f32_16x16x32_bf16 v[32:35], v[56:59], v[164:167], v[32:35]\n"
    "ds_read_b128 v[88:91], v3 offset:30720\n"
    "ds_read_b128 v[92:95], v3 offset:35840\n"
    "v_cvt_pk_bf16_f32 v106, v24, v25\n"
    "v_cvt_pk_bf16_f32 v107, v26, v27\n"
    "v_mfma_f32_16x16x32_bf16 v[32:35], v[60:63], v[168:171], v[32:35]\n"
    "s_waitcnt lgkmcnt(4)\n"
    "v_cvt_pk_bf16_f32 v108, v28, v29\n"
    "v_cvt_pk_bf16_f32 v109, v30, v31\n"
    "v_mfma_f32_16x16x32_bf16 v[36:39], v[64:67], v[156:159], v[36:39]\n"
    "buffer_store_dwordx4 v[106:109], v1, s[24:27], s65 offen offset:0 sc1\n"
    "v_mfma_f32_16x16x32_bf16 v[36:39], v[68:71], v[160:163], v[36:39]\n"
    "v_mfma_f32_16x16x32_bf16 v[36:39], v[72:75], v[164:167], v[36:39]\n"
    "v_mfma_f32_16x16x32_bf16 v[36:39], v[76:79], v[168:171], v[36:39]\n"
    "s_waitcnt lgkmcnt(0)\n"
    "v_mfma_f32_16x16x32_bf16 v[40:43], v[80:83], v[156:159], v[40:43]\n"
    "v_mfma_f32_16x16x32_bf16 v[40:43], v[84:87], v[160:163], v[40:43]\n"
    "buffer_load_dwordx4 v1, s[24:27], s56 offen offset:1024 sc1 lds\n"
    "v_mfma_f32_16x16x32_bf16 v[40:43], v[88:91], v[164:167], v[40:43]\n"
    "v_cvt_pk_bf16_f32 v110, v32, v33\n"
    "v_cvt_pk_bf16_f32 v111, v34, v35\n"
    "s_nop 0\n"
    "v_cvt_pk_bf16_f32 v112, v36, v37\n"
    "v_cvt_pk_bf16_f32 v113, v38, v39\n"
    "v_mfma_f32_16x16x32_bf16 v[40:43], v[92:95], v[168:171], v[40:43]\n"
    "s_add_u32 s54, s65, 0x2000\n"
    "buffer_store_dwordx4 v[110:113], v1, s[24:27], s54 offen offset:0 sc1\n"
    "s_nop 5\n"
    "v_cvt_pk_bf16_f32 v114, v40, v41\n"
    "v_cvt_pk_bf16_f32 v115, v42, v43\n"
    "buffer_store_dwordx2 v[114:115], v7, s[24:27], s66 offen offset:0 sc1\n"
    "v_add_f32 v16, v24, v25\n"
    "v_mul_f32 v17, v24, v24\n"
    "v_fmac_f32 v17, v25, v25\n"
    "v_add_f32 v16, v16, v26\n"
    "v_fmac_f32 v17, v26, v26\n"
    "v_add_f32 v16, v16, v27\n"
    "v_fmac_f32 v17, v27, v27\n"
    "v_add_f32 v16, v16, v28\n"
    "v_fmac_f32 v17, v28, v28\n"
    "v_add_f32 v16, v16, v29\n"
    "v_fmac_f32 v17, v29, v29\n"
    "v_add_f32 v16, v16, v30\n"
    "v_fmac_f32 v17, v30, v30\n"
    "v_add_f32 v16, v16, v31\n"
    "v_fmac_f32 v17, v31, v31\n"
    "v_add_f32 v16, v16, v32\n"
    "v_fmac_f32 v17, v32, v32\n"
    "v_add_f32 v16, v16, v33\n"
    "v_fmac_f32 v17, v33, v33\n"
    "v_add_f32 v16, v16, v34\n"
    "v_fmac_f32 v17, v34, v34\n"
    "v_add_f32 v16, v16, v35\n"
    "v_fmac_f32 v17, v35, v35\n"
    "v_add_f32 v16, v16, v36\n"
    "v_fmac_f32 v17, v36, v36\n"
    "v_add_f32 v16, v16, v37\n"
    "v_fmac_f32 v17, v37, v37\n"
    "v_add_f32 v16, v16, v38\n"
    "v_fmac_f32 v17, v38, v38\n"
    "v_add_f32 v16, v16, v39\n"
    "v_fmac_f32 v17, v39, v39\n"
    "v_add_f32 v16, v16, v40\n"
    "v_fmac_f32 v17, v40, v40\n"
    "v_add_f32 v16, v16, v41\n"
    "v_fmac_f32 v17, v41, v41\n"
    "v_add_f32 v16, v16, v42\n"
    "v_fmac_f32 v17, v42, v42\n"
    "v_add_f32 v16, v16, v43\n"
    "v_fmac_f32 v17, v43, v43\n"
    "v_mov_b32 v18, v16\n"
    "v_mov_b32 v19, v17\n"
    "s_nop 0\n"
    "v_permlane32_swap_b32 v18, v16\n"
    "v_permlane32_swap_b32 v19, v17\n"
    "v_add_f32 v16, v16, v18\n"
    "v_add_f32 v17, v17, v19\n"
    "v_mov_b32 v18, v16\n"
    "v_mov_b32 v19, v17\n"
    "s_nop 0\n"
    "v_permlane16_swap_b32 v18, v16\n"
    "v_permlane16_swap_b32 v19, v17\n"
    "v_add_f32 v16, v16, v18\n"
    "v_add_f32 v17, v17, v19\n"
    "buffer_store_dwordx2 v[16:17], v8, s[24:27], s72 offen offset:0 sc1\n"
    "s_mov_b64 exec, 0\n"
    "s_cmp_eq_u32 s41, 0\n"
    "s_cbranch_scc1 Lhop7\n"
    "s_cmp_eq_u32 s41, 1\n"
    "s_cbranch_scc1 Lhop8\n"
    "s_cmp_eq_u32 s41, 2\n"
    "s_cbranch_scc1 Lhop9\n"
    "s_branch Lhop10\n"
    "LhopB:\n"
    "s_mov_b64 exec, -1\n"
    "s_waitcnt vmcnt(6)\n"
    "s_barrier\n"
    "ds_read_b128 v[56:59], v3 offset:36864\n"
    "ds_read_b128 v[60:63], v3 offset:39936\n"
    "ds_read_b128 v[64:67], v3 offset:58368\n"
    "ds_read_b128 v[68:71], v3 offset:61440\n"
    "ds_read_b128 v[72:75], v4 offset:14336\n"
    "ds_read_b128 v[76:79], v4 offset:17408\n"
    "ds_read_b128 v[80:83], v1 offset:3072\n"
    "ds_read_b128 v[84:87], v1 offset:6144\n"
    "s_add_u32 s55, s64, 0\n"
    "buffer_load_dwordx4 v[132:135], v1, s[24:27], s55 offen offset:0 sc1\n"
    "buffer_load_dwordx4 v[136:139], v1, s[24:27], s55 offen offset:1024 sc1\n"
    "buffer_load_dwordx4 v[140:143], v1, s[24:27], s55 offen offset:2048 sc1\n"
    "buffer_load_dwordx4 v[144:147], v1, s[24:27], s55 offen offset:3072 sc1\n"
    "s_add_u32 s55, s64, 0x1000\n"
    "buffer_load_dwordx4 v[148:151], v1, s[24:27], s55 offen offset:0 sc1\n"
    "v_lshlrev_b32 v18, 16, v222\n"
    "v_and_b32 v19, 0xffff0000, v222\n"
    "v_add_f32 v212, v24, v18\n"
    "v_add_f32 v213, v25, v19\n"
    "v_lshlrev_b32 v18, 16, v223\n"
    "v_and_b32 v19, 0xffff0000, v223\n"
    "v_add_f32 v214, v26, v18\n"
    "v_add_f32 v215, v27, v19\n"
    "v_lshlrev_b32 v18, 16, v224\n"
    "v_and_b32 v19, 0xffff0000, v224\n"
    "v_add_f32 v216, v28, v18\n"
    "v_add_f32 v217, v29, v19\n"
    "buffer_load_dwordx4 v[152:155], v1, s[24:27], s55 offen offset:1024 sc1\n"
    "v_lshlrev_b32 v18, 16, v225\n"
    "v_and_b32 v19, 0xffff0000, v225\n"
    "v_add_f32 v218, v30, v18\n"
    "v_add_f32 v219, v31, v19\n"
    "v_lshlrev_b32 v18, 16, v226\n"
    "v_and_b32 v19, 0xffff0000, v226\n"
    "v_add_f32 v220, v32, v18\n"
    "v_add_f32 v221, v33, v19\n"
    "v_lshlrev_b32 v18, 16, v227\n"
    "v_and_b32 v19, 0xffff0000, v227\n"
    "v_add_f32 v222, v34, v18\n"
    "v_add_f32 v223, v35, v19\n"
    "buffer_load_dwordx4 v[156:159], v1, s[24:27], s55 offen offset:2048 sc1\n"
    "v_lshlrev_b32 v18, 16, v228\n"
    "v_and_b32 v19, 0xffff0000, v228\n"
    "v_add_f32 v224, v36, v18\n"
    "v_add_f32 v225, v37, v19\n"
    "v_lshlrev_b32 v18, 16, v229\n"
    "v_and_b32 v19, 0xffff0000, v229\n"
    "v_add_f32 v226, v38, v18\n"
    "v_add_f32 v227, v39, v19\n"
    "v_lshlrev_b32 v18, 16, v230\n"
    "v_and_b32 v19, 0xffff0000, v230\n"
    "v_add_f32 v228, v40, v18\n"
    "v_add_f32 v229, v41, v19\n"
    "buffer_load_dwordx4 v[160:163], v1, s[24:27], s55 offen offset:3072 sc1\n"
    "v_lshlrev_b32 v18, 16, v231\n"
    "v_and_b32 v19, 0xffff0000, v231\n"
    "v_add_f32 v230, v42, v18\n"
    "v_add_f32 v231, v43, v19\n"
    "s_add_u32 s55, s64, 0x2000\n"
    "buffer_load_dwordx4 v[164:167], v1, s[24:27], s55 offen offset:0 sc1\n"
    "buffer_load_dwordx4 v[168:171], v1, s[24:27], s55 offen offset:1024 sc1\n"
    "buffer_load_dwordx4 v[172:175], v1, s[24:27], s55 offen offset:2048 sc1\n"
    "s_waitcnt vmcnt(9)\n"
    "v_max3_u32 v23, v132, v133, v134\n"
    "v_max3_u32 v23, v23, v135, v136\n"
    "v_max3_u32 v23, v23, v137, v138\n"
    "v_max3_u32 v23, v23, v139, v139\n"
    "v_cmp_eq_u32 vcc, -1, v23\n"
    "s_nop 0\n"
    "s_cbranch_vccnz Lrty59\n"
    "Lok60:\n"
    "s_waitcnt lgkmcnt(7)\n"
    "v_mfma_f32_16x16x32_bf16 v[96:99], v[56:59], v[132:135], 0\n"
    "s_waitcnt vmcnt(7)\n"
    "ds_read_b128 v[88:91], v3 offset:41984\n"
    "ds_read_b128 v[92:95], v3 offset:45056\n"
    "buffer_load_dwordx4 v[176:179], v1, s[24:27], s55 offen offset:3072 sc1\n"
    "v_max3_u32 v23, v140, v141, v142\n"
    "s_waitcnt lgkmcnt(8)\n"
    "v_mfma_f32_16x16x32_bf16 v[96:99], v[60:63], v[136:139], v[96:99]\n"
    "s_add_u32 s55, s64, 0x3000\n"
    "v_max3_u32 v23, v23, v143, v144\n"
    "v_max3_u32 v23, v23, v145, v146\n"
    "v_max3_u32 v23, v23, v147, v147\n"
    "v_cmp_eq_u32 vcc, -1, v23\n"
    "s_waitcnt lgkmcnt(7)\n"
    "v_mfma_f32_16x16x32_bf16 v[100:103], v[64:67], v[132:135], 0\n"
    "ds_read_b128 v[56:59], v3 offset:63488\n"
    "ds_read_b128 v[60:63], v4 offset:1024\n"
    "buffer_load_dwordx4 v[180:183], v1, s[24:27], s55 offen offset:0 sc1\n"
    "s_waitcnt lgkmcnt(8)\n"
    "v_mfma_f32_16x16x32_bf16 v[100:103], v[68:71], v[136:139], v[100:103]\n"
    "s_waitcnt lgkmcnt(4)\n"
    "buffer_load_dwordx4 v[184:187], v1, s[24:27], s55 offen offset:1024 sc1\n"
    "v_mfma_f32_16x16x32_bf16 v[104:107], v[72:75], v[132:135], 0\n"
    "s_cbranch_vccnz Lrty41\n"
    "Lok42:\n"
    "ds_read_b128 v[64:67], v4 offset:19456\n"
    "ds_read_b128 v[68:71], v4 offset:22528\n"
    "v_mfma_f32_16x16x32_bf16 v[104:107], v[76:79], v[136:139], v[104:107]\n"
    "v_mfma_f32_16x16x32_bf16 v[108:111], v[80:83], v[132:135], 0\n"
    "ds_read_b128 v[72:75], v1 offset:8192\n"
    "ds_read_b128 v[76:79], v1 offset:11264\n"
    "v_mfma_f32_16x16x32_bf16 v[108:111], v[84:87], v[136:139], v[108:111]\n"
    "s_waitcnt lgkmcnt(4)\n"
    "buffer_load_dwordx4 v[188:191], v1, s[24:27], s55 offen offset:2048 sc1\n"
    "v_mfma_f32_16x16x32_bf16 v[96:99], v[88:91], v[140:143], v[96:99]\n"
    "s_waitcnt vmcnt(9)\n"
    "ds_read_b128 v[80:83], v3 offset:47104\n"
    "ds_read_b128 v[84:87], v3 offset:50176\n"
    "v_max3_u32 v23, v148, v149, v150\n"
    "v_mfma_f32_16x16x32_bf16 v[96:99], v[92:95], v[144:147], v[96:99]\n"
    "v_max3_u32 v23, v23, v151, v152\n"
    "v_max3_u32 v23, v23, v153, v154\n"
    "v_max3_u32 v23, v23, v155, v155\n"
    "v_cmp_eq_u32 vcc, -1, v23\n"
    "v_mfma_f32_16x16x32_bf16 v[100:103], v[56:59], v[140:143], v[100:103]\n"
    "ds_read_b128 v[88:91], v4 offset:3072\n"
    "ds_read_b128 v[92:95], v4 offset:6144\n"
    "buffer_load_dwordx4 v[192:195], v1, s[24:27], s55 offen offset:3072 sc1\n"
    "v_mfma_f32_16x16x32_bf16 v[100:103], v[60:63], v[144:147], v[100:103]\n"
    "s_waitcnt lgkmcnt(4)\n"
    "v_mfma_f32_16x16x32_bf16 v[104:107], v[64:67], v[140:143], v[104:107]\n"
    "s_cbranch_vccnz Lrty43\n"
    "Lok44:\n"
    "ds_read_b128 v[56:59], v4 offset:24576\n"
    "ds_read_b128 v[60:63], v4 offset:27648\n"
    "v_mfma_f32_16x16x32_bf16 v[104:107], v[68:71], v[144:147], v[104:107]\n"
    "s_add_u32 s55, s64, 0x4000\n"
    "v_mfma_f32_16x16x32_bf16 v[108:111], v[72:75], v[140:143], v[108:111]\n"
    "ds_read_b128 v[64:67], v1 offset:13312\n"
    "ds_read_b128 v[68:71], v1 offset:16384\n"
    "buffer_load_dwordx4 v[196:199], v1, s[24:27], s55 offen offset:0 sc1\n"
    "v_mfma_f32_16x16x32_bf16 v[108:111], v[76:79], v[144:147], v[108:111]\n"
    "s_waitcnt lgkmcnt(4)\n"
    "v_mfma_f32_16x16x32_bf16 v[96:99], v[80:83], v[148:151], v[96:99]\n"
    "s_waitcnt vmcnt(9)\n"
    "ds_read_b128 v[72:75], v3 offset:52224\n"
    "ds_read_b128 v[76:79], v3 offset:55296\n"
    "v_max3_u32 v23, v156, v157, v158\n"
    "v_mfma_f32_16x16x32_bf16 v[96:99], v[84:87], v[152:155], v[96:99]\n"
    "v_max3_u32 v23, v23, v159, v160\n"
    "v_max3_u32 v23, v23, v161, v162\n"
    "v_max3_u32 v23, v23, v163, v163\n"
    "v_cmp_eq_u32 vcc, -1, v23\n"
    "v_mfma_f32_16x16x32_bf16 v[100:103], v[88:91], v[148:151], v[100:103]\n"
    "ds_read_b128 v[80:83], v4 offset:8192\n"
    "ds_read_b128 v[84:87], v4 offset:11264\n"
    "buffer_load_dwordx4 v[200:203], v1, s[24:27], s55 offen offset:1024 sc1\n"
    "v_mfma_f32_16x16x32_bf16 v[100:103], v[92:95], v[152:155], v[100:103]\n"
    "s_waitcnt lgkmcnt(4)\n"
    "v_mfma_f32_16x16x32_bf16 v[104:107], v[56:59], v[148:151], v[104:107]\n"
    "s_cbranch_vccnz Lrty45\n"
    "Lok46:\n"
    "ds_read_b128 v[88:91], v4 offset:29696\n"
    "ds_read_b128 v[92:95], v1 offset:0\n"
    "buffer_load_dwordx4 v[204:207], v1, s[24:27], s55 offen offset:2048 sc1\n"
    "v_mfma_f32_16x16x32_bf16 v[104:107], v[60:63], v[152:155], v[104:107]\n"
    "v_mfma_f32_16x16x32_bf16 v[108:111], v[64:67], v[148:151], v[108:111]\n"
    "ds_read_b128 v[56:59], v1 offset:18432\n"
    "ds_read_b128 v[60:63], v1 offset:21504\n"
    "v_mfma_f32_16x16x32_bf16 v[108:111], v[68:71], v[152:155], v[108:111]\n"
    "s_waitcnt lgkmcnt(4)\n"
    "v_mfma_f32_16x16x32_bf16 v[96:99], v[72:75], v[156:159], v[96:99]\n"
    "s_waitcnt vmcnt(9)\n"
    "ds_read_b128 v[64:67], v3 offset:37888\n"
    "ds_read_b128 v[68:71], v3 offset:40960\n"
    "buffer_load_dwordx4 v[208:211], v1, s[24:27], s55 offen offset:3072 sc1\n"
    "v_max3_u32 v23, v164, v165, v166\n"
    "v_mfma_f32_16x16x32_bf16 v[96:99], v[76:79], v[160:163], v[96:99]\n"
    "v_max3_u32 v23, v23, v167, v168\n"
    "v_max3_u32 v23, v23, v169, v170\n"
    "v_max3_u32 v23, v23, v171, v171\n"
    "v_cmp_eq_u32 vcc, -1, v23\n"
    "v_mfma_f32_16x16x32_bf16 v[100:103], v[80:83], v[156:159], v[100:103]\n"
    "ds_read_b128 v[72:75], v3 offset:59392\n"
    "ds_read_b128 v[76:79], v3 offset:62464\n"
    "v_mfma_f32_16x16x32_bf16 v[100:103], v[84:87], v[160:163], v[100:103]\n"
    "s_waitcnt lgkmcnt(4)\n"
    "buffer_load_dwordx4 v[124:127], v1, s[24:27], s67 offen offset:0 sc1\n"
    "v_mfma_f32_16x16x32_bf16 v[104:107], v[88:91], v[156:159], v[104:107]\n"
    "s_cbranch_vccnz Lrty47\n"
    "Lok48:\n"
    "ds_read_b128 v[80:83], v4 offset:15360\n"
    "ds_read_b128 v[84:87], v4 offset:18432\n"
    "v_mfma_f32_16x16x32_bf16 v[104:107], v[92:95], v[160:163], v[104:107]\n"
    "v_mfma_f32_16x16x32_bf16 v[108:111], v[56:59], v[156:159], v[108:111]\n"
    "ds_read_b128 v[88:91], v1 offset:4096\n"
    "ds_read_b128 v[92:95], v1 offset:7168\n"
    "v_mfma_f32_16x16x32_bf16 v[108:111], v[60:63], v[160:163], v[108:111]\n"
    "s_waitcnt lgkmcnt(4)\n"
    "buffer_load_dwordx4 v1, s[24:27], s56 offen offset:2048 sc1 lds\n"
    "v_mfma_f32_16x16x32_bf16 v[96:99], v[64:67], v[164:167], v[96:99]\n"
    "s_waitcnt vmcnt(10)\n"
    "ds_read_b128 v[56:59], v3 offset:43008\n"
    "ds_read_b128 v[60:63], v3 offset:46080\n"
    "v_max3_u32 v23, v172, v173, v174\n"
    "v_mfma_f32_16x16x32_bf16 v[96:99], v[68:71], v[168:171], v[96:99]\n"
    "Lhop7:\n"
    "s_cbranch_execz LhopB\n"
    "v_max3_u32 v23, v23, v175, v176\n"
    "v_max3_u32 v23, v23, v177, v178\n"
    "v_max3_u32 v23, v23, v179, v179\n"
    "v_cmp_eq_u32 vcc, -1, v23\n"
    "v_mfma_f32_16x16x32_bf16 v[100:103], v[72:75], v[164:167], v[100:103]\n"
    "ds_read_b128 v[64:67], v3 offset:64512\n"
    "ds_read_b128 v[68:71], v4 offset:2048\n"
    "buffer_load_dwordx4 v1, s[24:27], s56 offen offset:3072 sc1 lds\n"
    "v_mfma_f32_16x16x32_bf16 v[100:103], v[76:79], v[168:171], v[100:103]\n"
    "s_waitcnt lgkmcnt(4)\n"
    "v_mfma_f32_16x16x32_bf16 v[104:107], v[80:83], v[164:167], v[104:107]\n"
    "s_cbranch_vccnz Lrty49\n"
    "Lok50:\n"
    "ds_read_b128 v[72:75], v4 offset:20480\n"
    "ds_read_b128 v[76:79], v4 offset:23552\n"
    "v_mfma_f32_16x16x32_bf16 v[104:107], v[84:87], v[168:171], v[104:107]\n"
    "s_add_u32 m0, s79, 0xb000\n"
    "v_mfma_f32_16x16x32_bf16 v[108:111], v[88:91], v[164:167], v[108:111]\n"
    "s_add_u32 s56, s76, 0x1a000\n"
    "ds_read_b128 v[80:83], v1 offset:9216\n"
    "ds_read_b128 v[84:87], v1 offset:12288\n"
    "buffer_load_dwordx4 v1, s[24:27], s56 offen offset:0 sc1 lds\n"
    "v_mfma_f32_16x16x32_bf16 v[108:111], v[92:95], v[168:171], v[108:111]\n"
    "s_waitcnt lgkmcnt(4)\n"
    "v_mfma_f32_16x16x32_bf16 v[96:99], v[56:59], v[172:175], v[96:99]\n"
    "s_waitcnt vmcnt(10)\n"
    "ds_read_b128 v[88:91], v3 offset:48128\n"
    "ds_read_b128 v[92:95], v3 offset:51200\n"
    "v_max3_u32 v23, v180, v181, v182\n"
    "v_mfma_f32_16x16x32_bf16 v[96:99], v[60:63], v[176:179], v[96:99]\n"
    "v_max3_u32 v23, v23, v183, v184\n"
    "v_max3_u32 v23, v23, v185, v186\n"
    "v_max3_u32 v23, v23, v187, v187\n"
    "v_cmp_eq_u32 vcc, -1, v23\n"
    "v_mfma_f32_16x16x32_bf16 v[100:103], v[64:67], v[172:175], v[100:103]\n"
    "ds_read_b128 v[56:59], v4 offset:4096\n"
    "ds_read_b128 v[60:63], v4 offset:7168\n"
    "buffer_load_dwordx4 v1, s[24:27], s56 offen offset:1024 sc1 lds\n"
    "v_mfma_f32_16x16x32_bf16 v[100:103], v[68:71], v[176:179], v[100:103]\n"
    "s_waitcnt lgkmcnt(4)\n"
    "v_mfma_f32_16x16x32_bf16 v[104:107], v[72:75], v[172:175], v[104:107]\n"
    "s_cbranch_vccnz Lrty51\n"
    "Lok52:\n"
    "ds_read_b128 v[64:67], v4 offset:25600\n"
    "ds_read_b128 v[68:71], v4 offset:28672\n"
    "buffer_load_dwordx4 v1, s[24:27], s56 offen offset:2048 sc1 lds\n"
    "v_mfma_f32_16x16x32_bf16 v[104:107], v[76:79], v[176:179], v[104:107]\n"
    "v_mfma_f32_16x16x32_bf16 v[108:111], v[80:83], v[172:175], v[108:111]\n"
    "ds_read_b128 v[72:75], v1 offset:14336\n"
    "ds_read_b128 v[76:79], v1 offset:17408\n"
    "v_mfma_f32_16x16x32_bf16 v[108:111], v[84:87], v[176:179], v[108:111]\n"
    "s_waitcnt lgkmcnt(4)\n"
    "v_mfma_f32_16x16x32_bf16 v[96:99], v[88:91], v[180:183], v[96:99]\n"
    "s_waitcnt vmcnt(10)\n"
    "ds_read_b128 v[80:83], v3 offset:53248\n"
    "ds_read_b128 v[84:87], v3 offset:56320\n"
    "buffer_load_dwordx4 v1, s[24:27], s56 offen offset:3072 sc1 lds\n"
    "v_max3_u32 v23, v188, v189, v190\n"
    "v_mfma_f32_16x16x32_bf16 v[96:99], v[92:95], v[184:187], v[96:99]\n"
    "s_add_u32 m0, s79, 0xf000\n"
    "v_max3_u32 v23, v23, v191, v192\n"
    "v_max3_u32 v23, v23, v193, v194\n"
    "v_max3_u32 v23, v23, v195, v195\n"
    "v_cmp_eq_u32 vcc, -1, v23\n"
    "v_mfma_f32_16x16x32_bf16 v[100:103], v[56:59], v[180:183], v[100:103]\n"
    "s_add_u32 s56, s76, 0x1e000\n"
    "ds_read_b128 v[88:91], v4 offset:9216\n"
    "ds_read_b128 v[92:95], v4 offset:12288\n"
    "v_mfma_f32_16x16x32_bf16 v[100:103], v[60:63], v[184:187], v[100:103]\n"
    "s_waitcnt lgkmcnt(4)\n"
    "buffer_load_dwordx4 v1, s[24:27], s56 offen offset:0 sc1 lds\n"
    "v_mfma_f32_16x16x32_bf16 v[104:107], v[64:67], v[180:183], v[104:107]\n"
    "s_cbranch_vccnz Lrty53\n"
    "Lok54:\n"
    "ds_read_b128 v[56:59], v4 offset:30720\n"
    "ds_read_b128 v[60:63], v1 offset:1024\n"
    "v_mfma_f32_16x16x32_bf16 v[104:107], v[68:71], v[184:187], v[104:107]\n"
    "v_mfma_f32_16x16x32_bf16 v[108:111], v[72:75], v[180:183], v[108:111]\n"
    "ds_read_b128 v[64:67], v1 offset:19456\n"
    "ds_read_b128 v[68:71], v1 offset:22528\n"
    "v_mfma_f32_16x16x32_bf16 v[108:111], v[76:79], v[184:187], v[108:111]\n"
    "s_waitcnt lgkmcnt(4)\n"
    "buffer_load_dwordx4 v1, s[24:27], s56 offen offset:1024 sc1 lds\n"
    "v_mfma_f32_16x16x32_bf16 v[96:99], v[80:83], v[188:191], v[96:99]\n"
    "s_waitcnt vmcnt(11)\n"
    "ds_read_b128 v[72:75], v3 offset:38912\n"
    "ds_read_b128 v[76:79], v3 offset:44032\n"
    "v_max3_u32 v23, v196, v197, v198\n"
    "v_mfma_f32_16x16x32_bf16 v[96:99], v[84:87], v[192:195], v[96:99]\n"
    "v_max3_u32 v23, v23, v199, v200\n"
    "v_max3_u32 v23, v23, v201, v202\n"
    "v_max3_u32 v23, v23, v203, v203\n"
    "v_cmp_eq_u32 vcc, -1, v23\n"
    "v_mfma_f32_16x16x32_bf16 v[100:103], v[88:91], v[188:191], v[100:103]\n"
    "ds_read_b128 v[80:83], v3 offset:60416\n"
    "ds_read_b128 v[84:87], v4 offset:0\n"
    "buffer_load_dwordx4 v1, s[24:27], s56 offen offset:2048 sc1 lds\n"
    "v_mfma_f32_16x16x32_bf16 v[100:103], v[92:95], v[192:195], v[100:103]\n"
    "s_waitcnt lgkmcnt(4)\n"
    "v_mfma_f32_16x16x32_bf16 v[104:107], v[56:59], v[188:191], v[104:107]\n"
    "s_cbranch_vccnz Lrty55\n"
    "Lok56:\n"
    "ds_read_b128 v[88:91], v4 offset:16384\n"
    "ds_read_b128 v[92:95], v4 offset:21504\n"
    "v_mfma_f32_16x16x32_bf16 v[104:107], v[60:63], v[192:195], v[104:107]\n"
    "s_waitcnt vmcnt(9)\n"
    "v_max3_u32 v22, v124, v125, v126\n"
    "v_max3_u32 v22, v22, v127, v127\n"
    "v_cmp_eq_u32 vcc, -1, v22\n"
    "s_nop 0\n"
    "s_cbranch_vccnz Lrty61\n"
    "Lok62:\n"
    "buffer_load_dwordx4 v1, s[24:27], s56 offen offset:3072 sc1 lds\n"
    "v_add_f32 v16, v124, v126\n"
    "v_mfma_f32_16x16x32_bf16 v[108:111], v[64:67], v[188:191], v[108:111]\n"
    "s_add_u32 m0, s79, 0x13000\n"
    "ds_read_b128 v[56:59], v1 offset:5120\n"
    "ds_read_b128 v[60:63], v1 offset:10240\n"
    "v_add_f32 v17, v125, v127\n"
    "v_mfma_f32_16x16x32_bf16 v[108:111], v[68:71], v[192:195], v[108:111]\n"
    "s_waitcnt lgkmcnt(4)\n"
    "v_mov_b32 v18, v16\n"
    "v_mov_b32 v19, v17\n"
    "s_nop 0\n"
    "v_permlane32_swap_b32 v18, v16\n"
    "v_mfma_f32_16x16x32_bf16 v[96:99], v[72:75], v[196:199], v[96:99]\n"
    "ds_read_b128 v[64:67], v3 offset:49152\n"
    "ds_read_b128 v[68:71], v3 offset:54272\n"
    "v_max3_u32 v23, v204, v205, v206\n"
    "v_mfma_f32_16x16x32_bf16 v[96:99], v[76:79], v[200:203], v[96:99]\n"
    "s_add_u32 s56, s76, 0x22000\n"
    "v_max3_u32 v23, v23, v207, v208\n"
    "v_max3_u32 v23, v23, v209, v210\n"
    "v_max3_u32 v23, v23, v211, v211\n"
    "v_cmp_eq_u32 vcc, -1, v23\n"
    "v_mfma_f32_16x16x32_bf16 v[100:103], v[80:83], v[196:199], v[100:103]\n"
    "ds_read_b128 v[72:75], v4 offset:5120\n"
    "ds_read_b128 v[76:79], v4 offset:10240\n"
    "buffer_load_dwordx4 v1, s[24:27], s56 offen offset:0 sc1 lds\n"
    "v_permlane32_swap_b32 v19, v17\n"
    "v_mfma_f32_16x16x32_bf16 v[100:103], v[84:87], v[200:203], v[100:103]\n"
    "s_waitcnt lgkmcnt(4)\n"
    "buffer_load_dwordx4 v1, s[24:27], s56 offen offset:1024 sc1 lds\n"
    "v_add_f32 v16, v16, v18\n"
    "v_mfma_f32_16x16x32_bf16 v[104:107], v[88:91], v[196:199], v[104:107]\n"
    "s_cbranch_vccnz Lrty57\n"
    "Lok58:\n"
    "ds_read_b128 v[80:83], v4 offset:26624\n"
    "ds_read_b128 v[84:87], v4 offset:31744\n"
    "v_add_f32 v17, v17, v19\n"
    "v_mfma_f32_16x16x32_bf16 v[104:107], v[92:95], v[200:203], v[104:107]\n"
    "v_mov_b32 v18, v16\n"
    "v_mov_b32 v19, v17\n"
    "s_nop 0\n"
    "v_permlane16_swap_b32 v18, v16\n"
    "v_mfma_f32_16x16x32_bf16 v[108:111], v[56:59], v[196:199], v[108:111]\n"
    "ds_read_b128 v[88:91], v1 offset:15360\n"
    "ds_read_b128 v[92:95], v1 offset:20480\n"
    "buffer_load_dwordx4 v1, s[24:27], s56 offen offset:2048 sc1 lds\n"
    "v_permlane16_swap_b32 v19, v17\n"
    "v_mfma_f32_16x16x32_bf16 v[108:111], v[60:63], v[200:203], v[108:111]\n"
    "s_waitcnt lgkmcnt(4)\n"
    "v_add_f32 v16, v16, v18\n"
    "v_add_f32 v17, v17, v19\n"
    "v_mul_f32 v16, 0x3acccccd, v16\n"
    "s_waitcnt vmcnt(11)\n"
    "s_barrier\n"
    "v_mfma_f32_16x16x32_bf16 v[96:99], v[64:67], v[204:207], v[96:99]\n"
    "ds_read_b128 v[56:59], v3 offset:57344\n"
    "ds_read_b128 v[60:63], v4 offset:13312\n"
    "buffer_load_dwordx4 v1, s[24:27], s56 offen offset:3072 sc1 lds\n"
    "v_mul_f32 v18, v16, v16\n"
    "v_mfma_f32_16x16x32_bf16 v[96:99], v[68:71], v[208:211], v[96:99]\n"
    "v_mul_f32 v17, 0x3acccccd, v17\n"
    "v_sub_f32 v17, v17, v18\n"
    "v_max_f32 v17, 0, v17\n"
    "v_mfma_f32_16x16x32_bf16 v[100:103], v[72:75], v[204:207], v[100:103]\n"
    "ds_read_b128 v[64:67], v1 offset:2048\n"
    "ds_read_b128 v[68:71], v1 offset:23552\n"
    "v_add_f32 v18, 0x3727c5ac, v17\n"
    "v_mfma_f32_16x16x32_bf16 v[100:103], v[76:79], v[208:211], v[100:103]\n"
    "s_waitcnt lgkmcnt(4)\n"
    "v_rsq_f32 v18, v18\n"
    "v_cvt_pk_bf16_f32 v128, -v16, -v16\n"
    "v_mfma_f32_16x16x32_bf16 v[104:107], v[80:83], v[204:207], v[104:107]\n"
    "ds_read_b128 v[72:75], v1 offset:24576\n"
    "ds_read_b128 v[76:79], v1 offset:25600\n"
    "v_mul_f32 v17, v17, v18\n"
    "v_mfma_f32_16x16x32_bf16 v[104:107], v[84:87], v[208:211], v[104:107]\n"
    "v_fmaak_f32 v17, v17, v18, 0x3727c5ac\n"
    "v_rsq_f32 v17, v17\n"
    "v_mfma_f32_16x16x32_bf16 v[108:111], v[88:91], v[204:207], v[108:111]\n"
    "ds_read_b128 v[80:83], v1 offset:26624\n"
    "ds_read_b128 v[84:87], v1 offset:27648\n"
    "v_lshlrev_b32 v19, 16, v128\n"
    "v_mfma_f32_16x16x32_bf16 v[108:111], v[92:95], v[208:211], v[108:111]\n"
    "s_waitcnt lgkmcnt(4)\n"
    "v_mul_f32 v124, v18, v17\n"
    "v_rcp_f32 v17, v124\n"
    "v_sub_f32 v19, -v16, v19\n"
    "v_cvt_pk_bf16_f32 v129, v19, v17\n"
    "v_and_b32 v18, 0xffff0000, v129\n"
    "v_sub_f32 v17, v17, v18\n"
    "v_cvt_pk_bf16_f32 v130, v17, 0\n"
    "v_lshlrev_b32 v18, 16, v130\n"
    "v_sub_f32 v18, v17, v18\n"
    "v_cvt_pk_bf16_f32 v130, v17, v18\n"
    "v_mov_b32 v131, 0\n"
    "v_mul_f32 v125, 0xc0135761, v124\n"
    "v_mul_f32 v126, v124, v124\n"
    "v_mul_f32 v126, 0x3d372713, v126\n"
    "v_mul_f32 v126, v126, v125\n"
    "v_mfma_f32_16x16x32_bf16 v[96:99], v[56:59], v[128:131], v[96:99]\n"
    "ds_read_b128 v[88:91], v1 offset:28672\n"
    "ds_read_b128 v[92:95], v1 offset:29696\n"
    "v_mfma_f32_16x16x32_bf16 v[100:103], v[60:63], v[128:131], v[100:103]\n"
    "v_mfma_f32_16x16x32_bf16 v[104:107], v[64:67], v[128:131], v[104:107]\n"
    "ds_read_b128 v[56:59], v1 offset:30720\n"
    "ds_read_b128 v[60:63], v1 offset:31744\n"
    "v_mfma_f32_16x16x32_bf16 v[108:111], v[68:71], v[128:131], v[108:111]\n"
    "s_waitcnt lgkmcnt(4)\n"
    "v_mul_f32 v16, v96, v96\n"
    "v_mul_f32 v17, v97, v97\n"
    "v_mul_f32 v18, v98, v98\n"
    "s_waitcnt vmcnt(8)\n"
    "s_barrier\n"
    "v_mfma_f32_16x16x32_bf16 v[112:115], v[72:75], v[132:135], 0\n"
    "s_add_u32 m0, s79, 0x17000\n"
    "ds_read_b128 v[64:67], v1 offset:32768\n"
    "ds_read_b128 v[68:71], v1 offset:33792\n"
    "v_mul_f32 v19, v99, v99\n"
    "v_fma_f32 v16, v16, v126, v125\n"
    "v_mfma_f32_16x16x32_bf16 v[112:115], v[76:79], v[164:167], v[112:115]\n"
    "s_add_u32 s56, s76, 0x26000\n"
    "v_fma_f32 v17, v17, v126, v125\n"
    "v_fma_f32 v18, v18, v126, v125\n"
    "v_fma_f32 v19, v19, v126, v125\n"
    "v_mfma_f32_16x16x32_bf16 v[112:115], v[80:83], v[196:199], v[112:115]\n"
    "ds_read_b128 v[72:75], v1 offset:34816\n"
    "ds_read_b128 v[76:79], v1 offset:35840\n"
    "buffer_load_dwordx4 v1, s[24:27], s56 offen offset:0 sc1 lds\n"
    "v_mul_f32 v16, v16, v96\n"
    "v_mul_f32 v17, v17, v97\n"
    "v_mfma_f32_16x16x32_bf16 v[112:115], v[84:87], v[136:139], v[112:115]\n"
    "s_waitcnt lgkmcnt(4)\n"
    "buffer_load_dwordx4 v1, s[24:27], s56 offen offset:1024 sc1 lds\n"
    "v_mul_f32 v18, v18, v98\n"
    "v_mfma_f32_16x16x32_bf16 v[112:115], v[88:91], v[168:171], v[112:115]\n"
    "ds_read_b128 v[80:83], v1 offset:36864\n"
    "ds_read_b128 v[84:87], v1 offset:37888\n"
    "v_mul_f32 v19, v19, v99\n"
    "v_exp_f32 v16, v16\n"
    "v_mfma_f32_16x16x32_bf16 v[112:115], v[92:95], v[140:143], v[112:115]\n"
    "v_exp_f32 v17, v17\n"
    "v_mfma_f32_16x16x32_bf16 v[112:115], v[56:59], v[172:175], v[112:115]\n"
    "ds_read_b128 v[88:91], v1 offset:38912\n"
    "ds_read_b128 v[92:95], v1 offset:39936\n"
    "buffer_load_dwordx4 v1, s[24:27], s56 offen offset:2048 sc1 lds\n"
    "v_exp_f32 v18, v18\n"
    "v_mfma_f32_16x16x32_bf16 v[112:115], v[60:63], v[200:203], v[112:115]\n"
    "s_waitcnt lgkmcnt(4)\n"
    "v_exp_f32 v19, v19\n"
    "v_add_f32 v16, 1.0, v16\n"
    "v_mfma_f32_16x16x32_bf16 v[112:115], v[64:67], v[144:147], v[112:115]\n"
    "ds_read_b128 v[56:59], v1 offset:40960\n"
    "ds_read_b128 v[60:63], v1 offset:41984\n"
    "buffer_load_dwordx4 v1, s[24:27], s56 offen offset:3072 sc1 lds\n"
    "v_add_f32 v17, 1.0, v17\n"
    "v_add_f32 v18, 1.0, v18\n"
    "v_mfma_f32_16x16x32_bf16 v[112:115], v[68:71], v[176:179], v[112:115]\n"
    "s_add_u32 m0, s79, 0x1b000\n"
    "v_add_f32 v19, 1.0, v19\n"
    "v_rcp_f32 v16, v16\n"
    "v_mfma_f32_16x16x32_bf16 v[112:115], v[72:75], v[148:151], v[112:115]\n"
    "s_add_u32 s56, s76, 0x2a000\n"
    "ds_read_b128 v[64:67], v1 offset:43008\n"
    "ds_read_b128 v[68:71], v1 offset:44032\n"
    "v_rcp_f32 v17, v17\n"
    "v_mfma_f32_16x16x32_bf16 v[112:115], v[76:79], v[180:183], v[112:115]\n"
    "s_waitcnt lgkmcnt(4)\n"
    "buffer_load_dwordx4 v1, s[24:27], s56 offen offset:0 sc1 lds\n"
    "v_rcp_f32 v18, v18\n"
    "v_mfma_f32_16x16x32_bf16 v[112:115], v[80:83], v[204:207], v[112:115]\n"
    "ds_read_b128 v[72:75], v1 offset:45056\n"
    "ds_read_b128 v[76:79], v1 offset:46080\n"
    "v_rcp_f32 v19, v19\n"
    "v_mfma_f32_16x16x32_bf16 v[112:115], v[84:87], v[152:155], v[112:115]\n"
    "v_mul_f32 v16, v16, v96\n"
    "v_mul_f32 v17, v17, v97\n"
    "v_mfma_f32_16x16x32_bf16 v[112:115], v[88:91], v[184:187], v[112:115]\n"
    "ds_read_b128 v[80:83], v1 offset:47104\n"
    "ds_read_b128 v[84:87], v1 offset:48128\n"
    "buffer_load_dwordx4 v1, s[24:27], s56 offen offset:1024 sc1 lds\n"
    "v_mul_f32 v18, v18, v98\n"
    "v_mul_f32 v19, v19, v99\n"
    "v_mfma_f32_16x16x32_bf16 v[112:115], v[92:95], v[156:159], v[112:115]\n"
    "s_waitcnt lgkmcnt(4)\n"
    "v_cvt_pk_bf16_f32 v96, v16, v17\n"
    "v_cvt_pk_bf16_f32 v97, v18, v19\n"
    "v_mul_f32 v20, v100, v100\n"
    "v_mfma_f32_16x16x32_bf16 v[112:115], v[56:59], v[188:191], v[112:115]\n"
    "ds_read_b128 v[88:91], v1 offset:49152\n"
    "ds_read_b128 v[92:95], v1 offset:50176\n"
    "buffer_load_dwordx4 v1, s[24:27], s56 offen offset:2048 sc1 lds\n"
    "v_mul_f32 v21, v101, v101\n"
    "v_mul_f32 v22, v102, v102\n"
    "v_mfma_f32_16x16x32_bf16 v[112:115], v[60:63], v[208:211], v[112:115]\n"
    "v_mul_f32 v23, v103, v103\n"
    "v_fma_f32 v20, v20, v126, v125\n"
    "v_fma_f32 v21, v21, v126, v125\n"
    "v_mfma_f32_16x16x32_bf16 v[112:115], v[64:67], v[160:163], v[112:115]\n"
    "ds_read_b128 v[56:59], v1 offset:51200\n"
    "ds_read_b128 v[60:63], v1 offset:52224\n"
    "v_fma_f32 v22, v22, v126, v125\n"
    "v_fma_f32 v23, v23, v126, v125\n"
    "v_mfma_f32_16x16x32_bf16 v[112:115], v[68:71], v[192:195], v[112:115]\n"
    "s_waitcnt lgkmcnt(4)\n"
    "buffer_load_dwordx4 v1, s[24:27], s56 offen offset:3072 sc1 lds\n"
    "v_mul_f32 v20, v20, v100\n"
    "v_mfma_f32_16x16x32_bf16 v[112:115], v[72:75], v[128:131], v[112:115]\n"
    "s_add_u32 m0, s79, 0x1f000\n"
    "ds_read_b128 v[64:67], v1 offset:53248\n"
    "ds_read_b128 v[68:71], v1 offset:54272\n"
    "v_mul_f32 v21, v21, v101\n"
    "v_mul_f32 v22, v22, v102\n"
    "s_waitcnt vmcnt(12)\n"
    "s_barrier\n"
    "v_mfma_f32_16x16x32_bf16 v[116:119], v[76:79], v[132:135], 0\n"
    "s_add_u32 s56, s76, 0x2e000\n"
    "v_mul_f32 v23, v23, v103\n"
    "v_exp_f32 v20, v20\n"
    "v_mfma_f32_16x16x32_bf16 v[116:119], v[80:83], v[164:167], v[116:119]\n"
    "ds_read_b128 v[72:75], v1 offset:55296\n"
    "ds_read_b128 v[76:79], v1 offset:56320\n"
    "buffer_load_dwordx4 v1, s[24:27], s56 offen offset:0 sc1 lds\n"
    "v_exp_f32 v21, v21\n"
    "v_mfma_f32_16x16x32_bf16 v[116:119], v[84:87], v[196:199], v[116:119]\n"
    "s_waitcnt lgkmcnt(4)\n"
    "buffer_load_dwordx4 v1, s[24:27], s56 offen offset:1024 sc1 lds\n"
    "v_exp_f32 v22, v22\n"
    "v_mfma_f32_16x16x32_bf16 v[116:119], v[88:91], v[136:139], v[116:119]\n"
    "ds_read_b128 v[80:83], v1 offset:57344\n"
    "ds_read_b128 v[84:87], v1 offset:58368\n"
    "v_exp_f32 v23, v23\n"
    "v_mfma_f32_16x16x32_bf16 v[116:119], v[92:95], v[168:171], v[116:119]\n"
    "v_add_f32 v20, 1.0, v20\n"
    "v_add_f32 v21, 1.0, v21\n"
    "v_mfma_f32_16x16x32_bf16 v[116:119], v[56:59], v[140:143], v[116:119]\n"
    "ds_read_b128 v[88:91], v1 offset:59392\n"
    "ds_read_b128 v[92:95], v1 offset:60416\n"
    "buffer_load_dwordx4 v1, s[24:27], s56 offen offset:2048 sc1 lds\n"
    "v_add_f32 v22, 1.0, v22\n"
    "v_add_f32 v23, 1.0, v23\n"
    "v_mfma_f32_16x16x32_bf16 v[116:119], v[60:63], v[172:175], v[116:119]\n"
    "s_waitcnt lgkmcnt(4)\n"
    "v_rcp_f32 v20, v20\n"
    "v_rcp_f32 v21, v21\n"
    "v_mfma_f32_16x16x32_bf16 v[116:119], v[64:67], v[200:203], v[116:119]\n"
    "ds_read_b128 v[56:59], v1 offset:61440\n"
    "ds_read_b128 v[60:63], v1 offset:62464\n"
    "buffer_load_dwordx4 v1, s[24:27], s56 offen offset:3072 sc1 lds\n"
    "v_rcp_f32 v22, v22\n"
    "v_mfma_f32_16x16x32_bf16 v[116:119], v[68:71], v[144:147], v[116:119]\n"
    "s_add_u32 m0, s79, 0x23000\n"
    "v_rcp_f32 v23, v23\n"
    "v_mfma_f32_16x16x32_bf16 v[116:119], v[72:75], v[176:179], v[116:119]\n"
    "s_add_u32 s56, s76, 0x32000\n"
    "ds_read_b128 v[64:67], v1 offset:63488\n"
    "ds_read_b128 v[68:71], v1 offset:64512\n"
    "v_mul_f32 v20, v20, v100\n"
    "v_mul_f32 v21, v21, v101\n"
    "v_mfma_f32_16x16x32_bf16 v[116:119], v[76:79], v[148:151], v[116:119]\n"
    "s_waitcnt lgkmcnt(4)\n"
    "buffer_load_dwordx4 v1, s[24:27], s56 offen offset:0 sc1 lds\n"
    "v_mul_f32 v22, v22, v102\n"
    "v_mfma_f32_16x16x32_bf16 v[116:119], v[80:83], v[180:183], v[116:119]\n"
    "ds_read_b128 v[72:75], v3 offset:0\n"
    "ds_read_b128 v[76:79], v3 offset:1024\n"
    "v_mul_f32 v23, v23, v103\n"
    "v_cvt_pk_bf16_f32 v98, v20, v21\n"
    "v_mfma_f32_16x16x32_bf16 v[116:119], v[84:87], v[204:207], v[116:119]\n"
    "v_cvt_pk_bf16_f32 v99, v22, v23\n"
    "v_mul_f32 v16, v104, v104\n"
    "v_mul_f32 v17, v105, v105\n"
    "v_mfma_f32_16x16x32_bf16 v[116:119], v[88:91], v[152:155], v[116:119]\n"
    "ds_read_b128 v[80:83], v3 offset:2048\n"
    "ds_read_b128 v[84:87], v3 offset:3072\n"
    "buffer_store_dwordx4 v[96:99], v1, s[24:27], s73 offen offset:0 sc1\n"
    "v_mul_f32 v18, v106, v106\n"
    "v_mul_f32 v19, v107, v107\n"
    "v_mfma_f32_16x16x32_bf16 v[116:119], v[92:95], v[184:187], v[116:119]\n"
    "s_waitcnt lgkmcnt(4)\n"
    "v_fma_f32 v16, v16, v126, v125\n"
    "v_fma_f32 v17, v17, v126, v125\n"
    "v_fma_f32 v18, v18, v126, v125\n"
    "v_mfma_f32_16x16x32_bf16 v[116:119], v[56:59], v[156:159], v[116:119]\n"
    "ds_read_b128 v[88:91], v3 offset:4096\n"
    "ds_read_b128 v[92:95], v3 offset:5120\n"
    "buffer_load_dwordx4 v1, s[24:27], s56 offen offset:1024 sc1 lds\n"
    "v_fma_f32 v19, v19, v126, v125\n"
    "v_mul_f32 v16, v16, v104\n"
    "v_mfma_f32_16x16x32_bf16 v[116:119], v[60:63], v[188:191], v[116:119]\n"
    "v_mul_f32 v17, v17, v105\n"
    "v_mul_f32 v18, v18, v106\n"
    "v_mul_f32 v19, v19, v107\n"
    "v_mfma_f32_16x16x32_bf16 v[116:119], v[64:67], v[208:211], v[116:119]\n"
    "ds_read_b128 v[56:59], v3 offset:6144\n"
    "ds_read_b128 v[60:63], v3 offset:7168\n"
    "buffer_load_dwordx4 v1, s[24:27], s56 offen offset:2048 sc1 lds\n"
    "v_exp_f32 v16, v16\n"
    "v_mfma_f32_16x16x32_bf16 v[116:119], v[68:71], v[160:163], v[116:119]\n"
    "s_waitcnt lgkmcnt(4)\n"
    "v_exp_f32 v17, v17\n"
    "v_exp_f32 v18, v18\n"
    "v_mfma_f32_16x16x32_bf16 v[116:119], v[72:75], v[192:195], v[116:119]\n"
    "ds_read_b128 v[64:67], v3 offset:8192\n"
    "ds_read_b128 v[68:71], v3 offset:9216\n"
    "v_exp_f32 v19, v19\n"
    "v_mfma_f32_16x16x32_bf16 v[116:119], v[76:79], v[128:131], v[116:119]\n"
    "buffer_load_dwordx4 v1, s[24:27], s56 offen offset:3072 sc1 lds\n"
    "s_waitcnt vmcnt(13)\n"
    "s_barrier\n"
    "v_mfma_f32_16x16x32_bf16 v[120:123], v[80:83], v[132:135], 0\n"
    "s_add_u32 s56, s79, 0x27000\n"
    "ds_read_b128 v[72:75], v3 offset:10240\n"
    "ds_read_b128 v[76:79], v3 offset:11264\n"
    "v_add_f32 v16, 1.0, v16\n"
    "v_add_f32 v17, 1.0, v17\n"
    "v_mfma_f32_16x16x32_bf16 v[120:123], v[84:87], v[164:167], v[120:123]\n"
    "s_waitcnt lgkmcnt(4)\n"
    "v_add_f32 v18, 1.0, v18\n"
    "v_add_f32 v19, 1.0, v19\n"
    "v_rcp_f32 v16, v16\n"
    "v_mfma_f32_16x16x32_bf16 v[120:123], v[88:91], v[196:199], v[120:123]\n"
    "s_sub_u32 m0, s56, 0x28000\n"
    "ds_read_b128 v[80:83], v3 offset:12288\n"
    "ds_read_b128 v[84:87], v3 offset:13312\n"
    "v_rcp_f32 v17, v17\n"
    "v_mfma_f32_16x16x32_bf16 v[120:123], v[92:95], v[136:139], v[120:123]\n"
    "s_min_u32 m0, m0, s56\n"
    "v_rcp_f32 v18, v18\n"
    "v_mfma_f32_16x16x32_bf16 v[120:123], v[56:59], v[168:171], v[120:123]\n"
    "s_add_u32 s56, s76, 0x36000\n"
    "ds_read_b128 v[88:91], v3 offset:14336\n"
    "ds_read_b128 v[92:95], v3 offset:15360\n"
    "buffer_load_dwordx4 v1, s[24:27], s56 offen offset:0 sc1 lds\n"
    "v_rcp_f32 v19, v19\n"
    "v_mfma_f32_16x16x32_bf16 v[120:123], v[60:63], v[140:143], v[120:123]\n"
    "s_waitcnt lgkmcnt(4)\n"
    "buffer_load_dwordx4 v1, s[24:27], s56 offen offset:1024 sc1 lds\n"
    "v_mul_f32 v16, v16, v104\n"
    "v_mfma_f32_16x16x32_bf16 v[120:123], v[64:67], v[172:175], v[120:123]\n"
    "ds_read_b128 v[56:59], v3 offset:16384\n"
    "ds_read_b128 v[60:63], v3 offset:17408\n"
    "v_mul_f32 v17, v17, v105\n"
    "v_mul_f32 v18, v18, v106\n"
    "v_mfma_f32_16x16x32_bf16 v[120:123], v[68:71], v[200:203], v[120:123]\n"
    "v_mul_f32 v19, v19, v107\n"
    "v_cvt_pk_bf16_f32 v104, v16, v17\n"
    "v_cvt_pk_bf16_f32 v105, v18, v19\n"
    "v_mfma_f32_16x16x32_bf16 v[120:123], v[72:75], v[144:147], v[120:123]\n"
    "ds_read_b128 v[64:67], v3 offset:18432\n"
    "ds_read_b128 v[68:71], v3 offset:19456\n"
    "buffer_load_dwordx4 v1, s[24:27], s56 offen offset:2048 sc1 lds\n"
    "v_mul_f32 v20, v108, v108\n"
    "v_mul_f32 v21, v109, v109\n"
    "v_mfma_f32_16x16x32_bf16 v[120:123], v[76:79], v[176:179], v[120:123]\n"
    "s_waitcnt lgkmcnt(4)\n"
    "v_mul_f32 v22, v110, v110\n"
    "v_mul_f32 v23, v111, v111\n"
    "v_fma_f32 v20, v20, v126, v125\n"
    "v_mfma_f32_16x16x32_bf16 v[120:123], v[80:83], v[148:151], v[120:123]\n"
    "ds_read_b128 v[72:75], v3 offset:20480\n"
    "ds_read_b128 v[76:79], v3 offset:21504\n"
    "buffer_load_dwordx4 v1, s[24:27], s56 offen offset:3072 sc1 lds\n"
    "v_fma_f32 v21, v21, v126, v125\n"
    "v_fma_f32 v22, v22, v126, v125\n"
    "v_mfma_f32_16x16x32_bf16 v[120:123], v[84:87], v[180:183], v[120:123]\n"
    "s_add_u32 m0, s79, 0x3000\n"
    "v_fma_f32 v23, v23, v126, v125\n"
    "v_mul_f32 v20, v20, v108\n"
    "v_mul_f32 v21, v21, v109\n"
    "v_mfma_f32_16x16x32_bf16 v[120:123], v[88:91], v[204:207], v[120:123]\n"
    "s_add_u32 s56, s76, 0x3a000\n"
    "ds_read_b128 v[80:83], v3 offset:22528\n"
    "ds_read_b128 v[84:87], v3 offset:23552\n"
    "v_mul_f32 v22, v22, v110\n"
    "v_mul_f32 v23, v23, v111\n"
    "v_mfma_f32_16x16x32_bf16 v[120:123], v[92:95], v[152:155], v[120:123]\n"
    "s_waitcnt lgkmcnt(4)\n"
    "buffer_load_dwordx4 v1, s[24:27], s56 offen offset:0 sc1 lds\n"
    "v_exp_f32 v20, v20\n"
    "v_mfma_f32_16x16x32_bf16 v[120:123], v[56:59], v[184:187], v[120:123]\n"
    "ds_read_b128 v[88:91], v3 offset:24576\n"
    "ds_read_b128 v[92:95], v3 offset:25600\n"
    "v_exp_f32 v21, v21\n"
    "v_mfma_f32_16x16x32_bf16 v[120:123], v[60:63], v[156:159], v[120:123]\n"
    "v_exp_f32 v22, v22\n"
    "v_mfma_f32_16x16x32_bf16 v[120:123], v[64:67], v[188:191], v[120:123]\n"
    "ds_read_b128 v[56:59], v3 offset:26624\n"
    "ds_read_b128 v[60:63], v3 offset:27648\n"
    "buffer_load_dwordx4 v1, s[24:27], s56 offen offset:1024 sc1 lds\n"
    "v_exp_f32 v23, v23\n"
    "v_mfma_f32_16x16x32_bf16 v[120:123], v[68:71], v[208:211], v[120:123]\n"
    "s_waitcnt lgkmcnt(4)\n"
    "v_add_f32 v20, 1.0, v20\n"
    "v_add_f32 v21, 1.0, v21\n"
    "v_add_f32 v22, 1.0, v22\n"
    "v_mfma_f32_16x16x32_bf16 v[120:123], v[72:75], v[160:163], v[120:123]\n"
    "ds_read_b128 v[64:67], v3 offset:28672\n"
    "ds_read_b128 v[68:71], v3 offset:29696\n"
    "buffer_load_dwordx4 v1, s[24:27], s56 offen offset:2048 sc1 lds\n"
    "v_add_f32 v23, 1.0, v23\n"
    "v_rcp_f32 v20, v20\n"
    "v_mfma_f32_16x16x32_bf16 v[120:123], v[76:79], v[192:195], v[120:123]\n"
    "v_rcp_f32 v21, v21\n"
    "v_mfma_f32_16x16x32_bf16 v[120:123], v[80:83], v[128:131], v[120:123]\n"
    "ds_read_b128 v[72:75], v3 offset:30720\n"
    "ds_read_b128 v[76:79], v3 offset:31744\n"
    "v_rcp_f32 v22, v22\n"
    "s_waitcnt vmcnt(12)\n"
    "s_barrier\n"
    "v_mfma_f32_16x16x32_bf16 v[96:99], v[84:87], v[132:135], 0\n"
    "s_waitcnt lgkmcnt(4)\n"
    "buffer_load_dwordx4 v1, s[24:27], s56 offen offset:3072 sc1 lds\n"
    "v_rcp_f32 v23, v23\n"
    "v_mfma_f32_16x16x32_bf16 v[96:99], v[88:91], v[164:167], v[96:99]\n"
    "s_mul_i32 s56, s41, 0x400\n"
    "ds_read_b128 v[80:83], v3 offset:32768\n"
    "ds_read_b128 v[84:87], v3 offset:33792\n"
    "v_mul_f32 v20, v20, v108\n"
    "v_mfma_f32_16x16x32_bf16 v[96:99], v[92:95], v[196:199], v[96:99]\n"
    "s_add_u32 m0, s56, 0x7000\n"
    "v_mul_f32 v21, v21, v109\n"
    "v_mul_f32 v22, v22, v110\n"
    "v_mul_f32 v23, v23, v111\n"
    "v_mfma_f32_16x16x32_bf16 v[96:99], v[56:59], v[136:139], v[96:99]\n"
    "s_sub_u32 s56, s56, s79\n"
    "ds_read_b128 v[88:91], v3 offset:34816\n"
    "ds_read_b128 v[92:95], v3 offset:35840\n"
    "v_cvt_pk_bf16_f32 v106, v20, v21\n"
    "v_cvt_pk_bf16_f32 v107, v22, v23\n"
    "v_mfma_f32_16x16x32_bf16 v[96:99], v[60:63], v[168:171], v[96:99]\n"
    "s_waitcnt lgkmcnt(4)\n"
    "buffer_store_dwordx4 v[104:107], v1, s[24:27], s73 offen offset:2048 sc1\n"
    "v_mul_f32 v16, v112, v112\n"
    "v_mfma_f32_16x16x32_bf16 v[96:99], v[64:67], v[140:143], v[96:99]\n"
    "s_add_u32 s56, s56, s76\n"
    "ds_read_b128 v[56:59], v3 offset:36864\n"
    "ds_read_b128 v[60:63], v3 offset:37888\n"
    "v_mul_f32 v17, v113, v113\n"
    "v_mul_f32 v18, v114, v114\n"
    "v_mfma_f32_16x16x32_bf16 v[96:99], v[68:71], v[172:175], v[96:99]\n"
    "s_add_u32 s56, s56, 0x3e000\n"
    "v_mul_f32 v19, v115, v115\n"
    "v_fma_f32 v16, v16, v126, v125\n"
    "v_fma_f32 v17, v17, v126, v125\n"
    "v_mfma_f32_16x16x32_bf16 v[96:99], v[72:75], v[200:203], v[96:99]\n"
    "ds_read_b128 v[64:67], v3 offset:38912\n"
    "ds_read_b128 v[68:71], v3 offset:39936\n"
    "buffer_load_dwordx4 v1, s[24:27], s56 offen offset:0 sc1 lds\n"
    "v_fma_f32 v18, v18, v126, v125\n"
    "v_fma_f32 v19, v19, v126, v125\n"
    "v_mfma_f32_16x16x32_bf16 v[96:99], v[76:79], v[144:147], v[96:99]\n"
    "s_waitcnt lgkmcnt(4)\n"
    "v_mul_f32 v16, v16, v112\n"
    "v_mul_f32 v17, v17, v113\n"
    "v_mul_f32 v18, v18, v114\n"
    "v_mfma_f32_16x16x32_bf16 v[96:99], v[80:83], v[176:179], v[96:99]\n"
    "s_add_u32 m0, s79, 0x8000\n"
    "ds_read_b128 v[72:75], v3 offset:40960\n"
    "ds_read_b128 v[76:79], v3 offset:41984\n"
    "v_mul_f32 v19, v19, v115\n"
    "v_exp_f32 v16, v16\n"
    "v_mfma_f32_16x16x32_bf16 v[96:99], v[84:87], v[148:151], v[96:99]\n"
    "s_add_u32 s56, s76, 0x3f000\n"
    "v_exp_f32 v17, v17\n"
    "v_mfma_f32_16x16x32_bf16 v[96:99], v[88:91], v[180:183], v[96:99]\n"
    "ds_read_b128 v[80:83], v3 offset:43008\n"
    "ds_read_b128 v[84:87], v3 offset:44032\n"
    "buffer_load_dwordx4 v9, s[24:27], s56 offen offset:0 sc1 lds\n"
    "v_exp_f32 v18, v18\n"
    "v_mfma_f32_16x16x32_bf16 v[96:99], v[92:95], v[204:207], v[96:99]\n"
    "s_waitcnt lgkmcnt(4)\n"
    "buffer_load_dwordx4 v9, s[24:27], s56 offen offset:1024 sc1 lds\n"
    "v_exp_f32 v19, v19\n"
    "v_mfma_f32_16x16x32_bf16 v[96:99], v[56:59], v[152:155], v[96:99]\n"
    "ds_read_b128 v[88:91], v3 offset:45056\n"
    "ds_read_b128 v[92:95], v3 offset:46080\n"
    "v_add_f32 v16, 1.0, v16\n"
    "v_mfma_f32_16x16x32_bf16 v[96:99], v[60:63], v[184:187], v[96:99]\n"
    "v_add_f32 v17, 1.0, v17\n"
    "v_add_f32 v18, 1.0, v18\n"
    "v_add_f32 v19, 1.0, v19\n"
    "v_mfma_f32_16x16x32_bf16 v[96:99], v[64:67], v[156:159], v[96:99]\n"
    "ds_read_b128 v[56:59], v3 offset:47104\n"
    "ds_read_b128 v[60:63], v3 offset:48128\n"
    "buffer_load_dwordx4 v9, s[24:27], s56 offen offset:2048 sc1 lds\n"
    "v_rcp_f32 v16, v16\n"
    "v_mfma_f32_16x16x32_bf16 v[96:99], v[68:71], v[188:191], v[96:99]\n"
    "s_waitcnt lgkmcnt(4)\n"
    "v_rcp_f32 v17, v17\n"
    "v_rcp_f32 v18, v18\n"
    "v_mfma_f32_16x16x32_bf16 v[96:99], v[72:75], v[208:211], v[96:99]\n"
    "ds_read_b128 v[64:67], v3 offset:49152\n"
    "ds_read_b128 v[68:71], v3 offset:50176\n"
    "buffer_load_dwordx4 v9, s[24:27], s56 offen offset:3072 sc1 lds\n"
    "v_rcp_f32 v19, v19\n"
    "v_mfma_f32_16x16x32_bf16 v[96:99], v[76:79], v[160:163], v[96:99]\n"
    "s_add_u32 m0, s79, 0xc000\n"
    "v_mul_f32 v16, v16, v112\n"
    "v_mul_f32 v17, v17, v113\n"
    "v_mfma_f32_16x16x32_bf16 v[96:99], v[80:83], v[192:195], v[96:99]\n"
    "s_add_u32 s56, s76, 0x43000\n"
    "ds_read_b128 v[72:75], v3 offset:51200\n"
    "ds_read_b128 v[76:79], v3 offset:52224\n"
    "v_mul_f32 v18, v18, v114\n"
    "v_mul_f32 v19, v19, v115\n"
    "v_mfma_f32_16x16x32_bf16 v[96:99], v[84:87], v[128:131], v[96:99]\n"
    "s_waitcnt lgkmcnt(4)\n"
    "buffer_load_dwordx4 v9, s[24:27], s56 offen offset:0 sc1 lds\n"
    "v_cvt_pk_bf16_f32 v112, v16, v17\n"
    "v_mfma_f32_16x16x32_bf16 v[100:103], v[88:91], v[132:135], 0\n"
    "Lhop8:\n"
    "s_cbranch_execz LhopB\n"
    "ds_read_b128 v[80:83], v3 offset:53248\n"
    "ds_read_b128 v[84:87], v3 offset:54272\n"
    "v_cvt_pk_bf16_f32 v113, v18, v19\n"
    "v_mul_f32 v20, v116, v116\n"
    "v_mfma_f32_16x16x32_bf16 v[100:103], v[92:95], v[164:167], v[100:103]\n"
    "v_mul_f32 v21, v117, v117\n"
    "v_mul_f32 v22, v118, v118\n"
    "v_mul_f32 v23, v119, v119\n"
    "v_mfma_f32_16x16x32_bf16 v[100:103], v[56:59], v[196:199], v[100:103]\n"
    "ds_read_b128 v[88:91], v3 offset:55296\n"
    "ds_read_b128 v[92:95], v3 offset:56320\n"
    "buffer_load_dwordx4 v9, s[24:27], s56 offen offset:1024 sc1 lds\n"
    "v_fma_f32 v20, v20, v126, v125\n"
    "v_fma_f32 v21, v21, v126, v125\n"
    "v_mfma_f32_16x16x32_bf16 v[100:103], v[60:63], v[136:139], v[100:103]\n"
    "s_waitcnt lgkmcnt(4)\n"
    "v_fma_f32 v22, v22, v126, v125\n"
    "v_fma_f32 v23, v23, v126, v125\n"
    "v_mul_f32 v20, v20, v116\n"
    "v_mfma_f32_16x16x32_bf16 v[100:103], v[64:67], v[168:171], v[100:103]\n"
    "ds_read_b128 v[56:59], v3 offset:57344\n"
    "ds_read_b128 v[60:63], v3 offset:58368\n"
    "buffer_load_dwordx4 v9, s[24:27], s56 offen offset:2048 sc1 lds\n"
    "v_mul_f32 v21, v21, v117\n"
    "v_mul_f32 v22, v22, v118\n"
    "v_mfma_f32_16x16x32_bf16 v[100:103], v[68:71], v[140:143], v[100:103]\n"
    "v_mul_f32 v23, v23, v119\n"
    "v_exp_f32 v20, v20\n"
    "v_mfma_f32_16x16x32_bf16 v[100:103], v[72:75], v[172:175], v[100:103]\n"
    "ds_read_b128 v[64:67], v3 offset:59392\n"
    "ds_read_b128 v[68:71], v3 offset:60416\n"
    "v_exp_f32 v21, v21\n"
    "v_mfma_f32_16x16x32_bf16 v[100:103], v[76:79], v[200:203], v[100:103]\n"
    "s_waitcnt lgkmcnt(4)\n"
    "buffer_load_dwordx4 v9, s[24:27], s56 offen offset:3072 sc1 lds\n"
    "v_exp_f32 v22, v22\n"
    "v_mfma_f32_16x16x32_bf16 v[100:103], v[80:83], v[144:147], v[100:103]\n"
    "s_mul_i32 s56, s41, 0xc00\n"
    "ds_read_b128 v[72:75], v3 offset:61440\n"
    "ds_read_b128 v[76:79], v3 offset:62464\n"
    "v_exp_f32 v23, v23\n"
    "v_mfma_f32_16x16x32_bf16 v[100:103], v[84:87], v[176:179], v[100:103]\n"
    "s_add_u32 m0, s56, 0x10000\n"
    "v_add_f32 v20, 1.0, v20\n"
    "v_add_f32 v21, 1.0, v21\n"
    "v_mfma_f32_16x16x32_bf16 v[100:103], v[88:91], v[148:151], v[100:103]\n"
    "s_sub_u32 s56, s56, s79\n"
    "ds_read_b128 v[80:83], v3 offset:63488\n"
    "ds_read_b128 v[84:87], v3 offset:64512\n"
    "v_add_f32 v22, 1.0, v22\n"
    "v_add_f32 v23, 1.0, v23\n"
    "v_mfma_f32_16x16x32_bf16 v[100:103], v[92:95], v[180:183], v[100:103]\n"
    "s_waitcnt lgkmcnt(4)\n"
    "v_rcp_f32 v20, v20\n"
    "v_rcp_f32 v21, v21\n"
    "v_mfma_f32_16x16x32_bf16 v[100:103], v[56:59], v[204:207], v[100:103]\n"
    "s_add_u32 s56, s56, s76\n"
    "ds_read_b128 v[88:91], v4 offset:0\n"
    "ds_read_b128 v[92:95], v4 offset:1024\n"
    "v_rcp_f32 v22, v22\n"
    "v_mfma_f32_16x16x32_bf16 v[100:103], v[60:63], v[152:155], v[100:103]\n"
    "s_add_u32 s56, s56, 0x47000\n"
    "v_rcp_f32 v23, v23\n"
    "v_mfma_f32_16x16x32_bf16 v[100:103], v[64:67], v[184:187], v[100:103]\n"
    "ds_read_b128 v[56:59], v4 offset:2048\n"
    "ds_read_b128 v[60:63], v4 offset:3072\n"
    "buffer_load_dwordx4 v9, s[24:27], s56 offen offset:0 sc1 lds\n"
    "v_mul_f32 v20, v20, v116\n"
    "v_mul_f32 v21, v21, v117\n"
    "v_mfma_f32_16x16x32_bf16 v[100:103], v[68:71], v[156:159], v[100:103]\n"
    "s_waitcnt lgkmcnt(4)\n"
    "buffer_load_dwordx4 v9, s[24:27], s56 offen offset:1024 sc1 lds\n"
    "v_mul_f32 v22, v22, v118\n"
    "v_mfma_f32_16x16x32_bf16 v[100:103], v[72:75], v[188:191], v[100:103]\n"
    "ds_read_b128 v[64:67], v4 offset:4096\n"
    "ds_read_b128 v[68:71], v4 offset:5120\n"
    "v_mul_f32 v23, v23, v119\n"
    "v_cvt_pk_bf16_f32 v114, v20, v21\n"
    "v_mfma_f32_16x16x32_bf16 v[100:103], v[76:79], v[208:211], v[100:103]\n"
    "v_cvt_pk_bf16_f32 v115, v22, v23\n"
    "v_mul_f32 v16, v120, v120\n"
    "v_mul_f32 v17, v121, v121\n"
    "v_mfma_f32_16x16x32_bf16 v[100:103], v[80:83], v[160:163], v[100:103]\n"
    "s_add_u32 s54, s73, 0x1000\n"
    "ds_read_b128 v[72:75], v4 offset:6144\n"
    "ds_read_b128 v[76:79], v4 offset:7168\n"
    "buffer_store_dwordx4 v[112:115], v1, s[24:27], s54 offen offset:0 sc1\n"
    "v_mul_f32 v18, v122, v122\n"
    "v_mul_f32 v19, v123, v123\n"
    "v_mfma_f32_16x16x32_bf16 v[100:103], v[84:87], v[192:195], v[100:103]\n"
    "s_waitcnt lgkmcnt(4)\n"
    "v_fma_f32 v16, v16, v126, v125\n"
    "v_fma_f32 v17, v17, v126, v125\n"
    "v_fma_f32 v18, v18, v126, v125\n"
    "v_mfma_f32_16x16x32_bf16 v[100:103], v[88:91], v[128:131], v[100:103]\n"
    "ds_read_b128 v[80:83], v4 offset:8192\n"
    "ds_read_b128 v[84:87], v4 offset:9216\n"
    "buffer_load_dwordx4 v9, s[24:27], s56 offen offset:2048 sc1 lds\n"
    "v_fma_f32 v19, v19, v126, v125\n"
    "v_mul_f32 v16, v16, v120\n"
    "s_waitcnt vmcnt(14)\n"
    "s_barrier\n"
    "v_mfma_f32_16x16x32_bf16 v[104:107], v[92:95], v[132:135], 0\n"
    "s_add_u32 s57, s78, 0xd000\n"
    "v_mul_f32 v17, v17, v121\n"
    "v_mul_f32 v18, v18, v122\n"
    "v_mul_f32 v19, v19, v123\n"
    "v_mfma_f32_16x16x32_bf16 v[104:107], v[56:59], v[164:167], v[104:107]\n"
    "ds_read_b128 v[88:91], v4 offset:10240\n"
    "ds_read_b128 v[92:95], v4 offset:11264\n"
    "buffer_load_dwordx4 a[208:211], v1, s[24:27], s57 offen offset:0 sc1\n"
    "v_exp_f32 v16, v16\n"
    "v_mfma_f32_16x16x32_bf16 v[104:107], v[60:63], v[196:199], v[104:107]\n"
    "s_waitcnt lgkmcnt(4)\n"
    "buffer_load_dwordx4 a[212:215], v1, s[24:27], s57 offen offset:1024 sc1\n"
    "v_exp_f32 v17, v17\n"
    "v_mfma_f32_16x16x32_bf16 v[104:107], v[64:67], v[136:139], v[104:107]\n"
    "ds_read_b128 v[56:59], v4 offset:12288\n"
    "ds_read_b128 v[60:63], v4 offset:13312\n"
    "v_exp_f32 v18, v18\n"
    "v_mfma_f32_16x16x32_bf16 v[104:107], v[68:71], v[168:171], v[104:107]\n"
    "v_exp_f32 v19, v19\n"
    "v_mfma_f32_16x16x32_bf16 v[104:107], v[72:75], v[140:143], v[104:107]\n"
    "ds_read_b128 v[64:67], v4 offset:14336\n"
    "ds_read_b128 v[68:71], v4 offset:15360\n"
    "buffer_load_dwordx4 a[216:219], v1, s[24:27], s57 offen offset:2048 sc1\n"
    "v_add_f32 v16, 1.0, v16\n"
    "v_add_f32 v17, 1.0, v17\n"
    "v_mfma_f32_16x16x32_bf16 v[104:107], v[76:79], v[172:175], v[104:107]\n"
    "s_waitcnt lgkmcnt(4)\n"
    "v_add_f32 v18, 1.0, v18\n"
    "v_add_f32 v19, 1.0, v19\n"
    "v_rcp_f32 v16, v16\n"
    "v_mfma_f32_16x16x32_bf16 v[104:107], v[80:83], v[200:203], v[104:107]\n"
    "ds_read_b128 v[72:75], v4 offset:16384\n"
    "ds_read_b128 v[76:79], v4 offset:17408\n"
    "v_rcp_f32 v17, v17\n"
    "v_mfma_f32_16x16x32_bf16 v[104:107], v[84:87], v[144:147], v[104:107]\n"
    "v_rcp_f32 v18, v18\n"
    "v_mfma_f32_16x16x32_bf16 v[104:107], v[88:91], v[176:179], v[104:107]\n"
    "ds_read_b128 v[80:83], v4 offset:18432\n"
    "ds_read_b128 v[84:87], v4 offset:19456\n"
    "v_rcp_f32 v19, v19\n"
    "v_mfma_f32_16x16x32_bf16 v[104:107], v[92:95], v[148:151], v[104:107]\n"
    "s_waitcnt lgkmcnt(4)\n"
    "v_mul_f32 v16, v16, v120\n"
    "v_mul_f32 v17, v17, v121\n"
    "v_mul_f32 v18, v18, v122\n"
    "v_mfma_f32_16x16x32_bf16 v[104:107], v[56:59], v[180:183], v[104:107]\n"
    "ds_read_b128 v[88:91], v4 offset:20480\n"
    "ds_read_b128 v[92:95], v4 offset:21504\n"
    "v_mul_f32 v19, v19, v123\n"
    "v_cvt_pk_bf16_f32 v120, v16, v17\n"
    "v_mfma_f32_16x16x32_bf16 v[104:107], v[60:63], v[204:207], v[104:107]\n"
    "v_cvt_pk_bf16_f32 v121, v18, v19\n"
    "v_mul_f32 v20, v96, v96\n"
    "v_mul_f32 v21, v97, v97\n"
    "v_mfma_f32_16x16x32_bf16 v[104:107], v[64:67], v[152:155], v[104:107]\n"
    "ds_read_b128 v[56:59], v4 offset:22528\n"
    "ds_read_b128 v[60:63], v4 offset:23552\n"
    "v_mul_f32 v22, v98, v98\n"
    "v_mul_f32 v23, v99, v99\n"
    "v_mfma_f32_16x16x32_bf16 v[104:107], v[68:71], v[184:187], v[104:107]\n"
    "s_waitcnt lgkmcnt(4)\n"
    "v_fma_f32 v20, v20, v126, v125\n"
    "v_fma_f32 v21, v21, v126, v125\n"
    "v_fma_f32 v22, v22, v126, v125\n"
    "v_mfma_f32_16x16x32_bf16 v[104:107], v[72:75], v[156:159], v[104:107]\n"
    "ds_read_b128 v[64:67], v4 offset:24576\n"
    "ds_read_b128 v[68:71], v4 offset:25600\n"
    "v_fma_f32 v23, v23, v126, v125\n"
    "v_mul_f32 v20, v20, v96\n"
    "v_mfma_f32_16x16x32_bf16 v[104:107], v[76:79], v[188:191], v[104:107]\n"
    "v_mul_f32 v21, v21, v97\n"
    "v_mul_f32 v22, v22, v98\n"
    "v_mul_f32 v23, v23, v99\n"
    "v_mfma_f32_16x16x32_bf16 v[104:107], v[80:83], v[208:211], v[104:107]\n"
    "ds_read_b128 v[72:75], v4 offset:26624\n"
    "ds_read_b128 v[76:79], v4 offset:27648\n"
    "v_exp_f32 v20, v20\n"
    "v_mfma_f32_16x16x32_bf16 v[104:107], v[84:87], v[160:163], v[104:107]\n"
    "s_waitcnt lgkmcnt(4)\n"
    "v_exp_f32 v21, v21\n"
    "v_exp_f32 v22, v22\n"
    "v_mfma_f32_16x16x32_bf16 v[104:107], v[88:91], v[192:195], v[104:107]\n"
    "ds_read_b128 v[80:83], v4 offset:28672\n"
    "ds_read_b128 v[84:87], v4 offset:29696\n"
    "v_exp_f32 v23, v23\n"
    "v_mfma_f32_16x16x32_bf16 v[104:107], v[92:95], v[128:131], v[104:107]\n"
    "v_add_f32 v20, 1.0, v20\n"
    "v_add_f32 v21, 1.0, v21\n"
    "v_mfma_f32_16x16x32_bf16 v[108:111], v[56:59], v[132:135], 0\n"
    "ds_read_b128 v[88:91], v4 offset:30720\n"
    "ds_read_b128 v[92:95], v4 offset:31744\n"
    "v_add_f32 v22, 1.0, v22\n"
    "v_add_f32 v23, 1.0, v23\n"
    "v_mfma_f32_16x16x32_bf16 v[108:111], v[60:63], v[164:167], v[108:111]\n"
    "s_waitcnt lgkmcnt(4)\n"
    "v_rcp_f32 v20, v20\n"
    "v_rcp_f32 v21, v21\n"
    "v_mfma_f32_16x16x32_bf16 v[108:111], v[64:67], v[196:199], v[108:111]\n"
    "ds_read_b128 v[56:59], v1 offset:0\n"
    "ds_read_b128 v[60:63], v1 offset:1024\n"
    "v_rcp_f32 v22, v22\n"
    "v_mfma_f32_16x16x32_bf16 v[108:111], v[68:71], v[136:139], v[108:111]\n"
    "v_rcp_f32 v23, v23\n"
    "v_mfma_f32_16x16x32_bf16 v[108:111], v[72:75], v[168:171], v[108:111]\n"
    "ds_read_b128 v[64:67], v1 offset:2048\n"
    "ds_read_b128 v[68:71], v1 offset:3072\n"
    "v_mul_f32 v20, v20, v96\n"
    "v_mul_f32 v21, v21, v97\n"
    "v_mfma_f32_16x16x32_bf16 v[108:111], v[76:79], v[140:143], v[108:111]\n"
    "s_waitcnt lgkmcnt(4)\n"
    "v_mul_f32 v22, v22, v98\n"
    "v_mul_f32 v23, v23, v99\n"
    "v_cvt_pk_bf16_f32 v122, v20, v21\n"
    "v_mfma_f32_16x16x32_bf16 v[108:111], v[80:83], v[172:175], v[108:111]\n"
    "ds_read_b128 v[72:75], v1 offset:4096\n"
    "ds_read_b128 v[76:79], v1 offset:5120\n"
    "v_cvt_pk_bf16_f32 v123, v22, v23\n"
    "v_mul_f32 v16, v100, v100\n"
    "v_mfma_f32_16x16x32_bf16 v[108:111], v[84:87], v[200:203], v[108:111]\n"
    "s_add_u32 s54, s73, 0x1000\n"
    "v_mul_f32 v17, v101, v101\n"
    "v_mul_f32 v18, v102, v102\n"
    "v_mul_f32 v19, v103, v103\n"
    "v_mfma_f32_16x16x32_bf16 v[108:111], v[88:91], v[144:147], v[108:111]\n"
    "ds_read_b128 v[80:83], v1 offset:6144\n"
    "ds_read_b128 v[84:87], v1 offset:7168\n"
    "buffer_store_dwordx4 v[120:123], v1, s[24:27], s54 offen offset:2048 sc1\n"
    "v_fma_f32 v16, v16, v126, v125\n"
    "v_fma_f32 v17, v17, v126, v125\n"
    "v_mfma_f32_16x16x32_bf16 v[108:111], v[92:95], v[176:179], v[108:111]\n"
    "s_waitcnt lgkmcnt(4)\n"
    "v_fma_f32 v18, v18, v126, v125\n"
    "v_fma_f32 v19, v19, v126, v125\n"
    "v_mul_f32 v16, v16, v100\n"
    "v_mfma_f32_16x16x32_bf16 v[108:111], v[56:59], v[148:151], v[108:111]\n"
    "ds_read_b128 v[88:91], v1 offset:8192\n"
    "ds_read_b128 v[92:95], v1 offset:9216\n"
    "v_mul_f32 v17, v17, v101\n"
    "v_mul_f32 v18, v18, v102\n"
    "v_mfma_f32_16x16x32_bf16 v[108:111], v[60:63], v[180:183], v[108:111]\n"
    "v_mul_f32 v19, v19, v103\n"
    "v_exp_f32 v16, v16\n"
    "v_mfma_f32_16x16x32_bf16 v[108:111], v[64:67], v[204:207], v[108:111]\n"
    "ds_read_b128 v[56:59], v1 offset:10240\n"
    "ds_read_b128 v[60:63], v1 offset:11264\n"
    "v_exp_f32 v17, v17\n"
    "v_mfma_f32_16x16x32_bf16 v[108:111], v[68:71], v[152:155], v[108:111]\n"
    "s_waitcnt lgkmcnt(4)\n"
    "v_exp_f32 v18, v18\n"
    "v_exp_f32 v19, v19\n"
    "v_mfma_f32_16x16x32_bf16 v[108:111], v[72:75], v[184:187], v[108:111]\n"
    "ds_read_b128 v[64:67], v1 offset:12288\n"
    "ds_read_b128 v[68:71], v1 offset:13312\n"
    "v_add_f32 v16, 1.0, v16\n"
    "v_mfma_f32_16x16x32_bf16 v[108:111], v[76:79], v[156:159], v[108:111]\n"
    "v_add_f32 v17, 1.0, v17\n"
    "v_add_f32 v18, 1.0, v18\n"
    "v_add_f32 v19, 1.0, v19\n"
    "v_mfma_f32_16x16x32_bf16 v[108:111], v[80:83], v[188:191], v[108:111]\n"
    "ds_read_b128 v[72:75], v1 offset:14336\n"
    "ds_read_b128 v[76:79], v1 offset:15360\n"
    "v_rcp_f32 v16, v16\n"
    "v_mfma_f32_16x16x32_bf16 v[108:111], v[84:87], v[208:211], v[108:111]\n"
    "s_waitcnt lgkmcnt(4)\n"
    "v_rcp_f32 v17, v17\n"
    "v_rcp_f32 v18, v18\n"
    "v_mfma_f32_16x16x32_bf16 v[108:111], v[88:91], v[160:163], v[108:111]\n"
    "ds_read_b128 v[80:83], v1 offset:16384\n"
    "ds_read_b128 v[84:87], v1 offset:17408\n"
    "v_rcp_f32 v19, v19\n"
    "v_mfma_f32_16x16x32_bf16 v[108:111], v[92:95], v[192:195], v[108:111]\n"
    "v_mul_f32 v16, v16, v100\n"
    "v_mul_f32 v17, v17, v101\n"
    "v_mfma_f32_16x16x32_bf16 v[108:111], v[56:59], v[128:131], v[108:111]\n"
    "ds_read_b128 v[88:91], v1 offset:18432\n"
    "ds_read_b128 v[92:95], v1 offset:19456\n"
    "buffer_load_dwordx4 v[24:27], v1, s[24:27], s68 offen offset:0 sc1\n"
    "v_mul_f32 v18, v18, v102\n"
    "v_mul_f32 v19, v19, v103\n"
    "s_waitcnt vmcnt(5)\n"
    "s_barrier\n"
    "v_mfma_f32_16x16x32_bf16 v[112:115], v[60:63], v[132:135], 0\n"
    "s_waitcnt lgkmcnt(4)\n"
    "buffer_load_dwordx4 v[40:43], v1, s[24:27], s68 offen offset:1024 sc1\n"
    "v_cvt_pk_bf16_f32 v100, v16, v17\n"
    "v_mfma_f32_16x16x32_bf16 v[112:115], v[64:67], v[164:167], v[112:115]\n"
    "ds_read_b128 v[56:59], v1 offset:20480\n"
    "ds_read_b128 v[60:63], v1 offset:21504\n"
    "buffer_load_dwordx4 v[28:31], v1, s[24:27], s69 offen offset:0 sc1\n"
    "v_cvt_pk_bf16_f32 v101, v18, v19\n"
    "v_mul_f32 v20, v104, v104\n"
    "v_mfma_f32_16x16x32_bf16 v[112:115], v[68:71], v[196:199], v[112:115]\n"
    "v_mul_f32 v21, v105, v105\n"
    "v_mul_f32 v22, v106, v106\n"
    "v_mul_f32 v23, v107, v107\n"
    "v_mfma_f32_16x16x32_bf16 v[112:115], v[72:75], v[136:139], v[112:115]\n"
    "ds_read_b128 v[64:67], v1 offset:22528\n"
    "ds_read_b128 v[68:71], v1 offset:23552\n"
    "buffer_load_dwordx4 v[44:47], v1, s[24:27], s69 offen offset:1024 sc1\n"
    "v_fma_f32 v20, v20, v126, v125\n"
    "v_fma_f32 v21, v21, v126, v125\n"
    "v_mfma_f32_16x16x32_bf16 v[112:115], v[76:79], v[168:171], v[112:115]\n"
    "s_waitcnt lgkmcnt(4)\n"
    "v_fma_f32 v22, v22, v126, v125\n"
    "v_fma_f32 v23, v23, v126, v125\n"
    "v_mul_f32 v20, v20, v104\n"
    "v_mfma_f32_16x16x32_bf16 v[112:115], v[80:83], v[140:143], v[112:115]\n"
    "ds_read_b128 v[72:75], v1 offset:24576\n"
    "ds_read_b128 v[76:79], v1 offset:25600\n"
    "v_mul_f32 v21, v21, v105\n"
    "v_mul_f32 v22, v22, v106\n"
    "v_mfma_f32_16x16x32_bf16 v[112:115], v[84:87], v[172:175], v[112:115]\n"
    "buffer_load_dwordx4 v[32:35], v1, s[24:27], s70 offen offset:0 sc1\n"
    "v_mul_f32 v23, v23, v107\n"
    "v_mfma_f32_16x16x32_bf16 v[112:115], v[88:91], v[200:203], v[112:115]\n"
    "ds_read_b128 v[80:83], v1 offset:26624\n"
    "ds_read_b128 v[84:87], v1 offset:27648\n"
    "v_exp_f32 v20, v20\n"
    "v_mfma_f32_16x16x32_bf16 v[112:115], v[92:95], v[144:147], v[112:115]\n"
    "s_waitcnt lgkmcnt(4)\n"
    "v_exp_f32 v21, v21\n"
    "v_exp_f32 v22, v22\n"
    "v_mfma_f32_16x16x32_bf16 v[112:115], v[56:59], v[176:179], v[112:115]\n"
    "ds_read_b128 v[88:91], v1 offset:28672\n"
    "ds_read_b128 v[92:95], v1 offset:29696\n"
    "buffer_load_dwordx4 v[48:51], v1, s[24:27], s70 offen offset:1024 sc1\n"
    "v_exp_f32 v23, v23\n"
    "v_mfma_f32_16x16x32_bf16 v[112:115], v[60:63], v[148:151], v[112:115]\n"
    "v_add_f32 v20, 1.0, v20\n"
    "v_add_f32 v21, 1.0, v21\n"
    "v_mfma_f32_16x16x32_bf16 v[112:115], v[64:67], v[180:183], v[112:115]\n"
    "ds_read_b128 v[56:59], v1 offset:30720\n"
    "ds_read_b128 v[60:63], v1 offset:31744\n"
    "buffer_load_dwordx4 v[36:39], v1, s[24:27], s71 offen offset:0 sc1\n"
    "v_add_f32 v22, 1.0, v22\n"
    "v_add_f32 v23, 1.0, v23\n"
    "v_mfma_f32_16x16x32_bf16 v[112:115], v[68:71], v[204:207], v[112:115]\n"
    "s_waitcnt lgkmcnt(4)\n"
    "v_rcp_f32 v20, v20\n"
    "v_rcp_f32 v21, v21\n"
    "v_mfma_f32_16x16x32_bf16 v[112:115], v[72:75], v[152:155], v[112:115]\n"
    "ds_read_b128 v[64:67], v1 offset:32768\n"
    "ds_read_b128 v[68:71], v1 offset:33792\n"
    "v_rcp_f32 v22, v22\n"
    "v_mfma_f32_16x16x32_bf16 v[112:115], v[76:79], v[184:187], v[112:115]\n"
    "buffer_load_dwordx4 v[52:55], v1, s[24:27], s71 offen offset:1024 sc1\n"
    "v_mfma_f32_16x16x32_bf16 v[112:115], v[80:83], v[156:159], v[112:115]\n"
    "ds_read_b128 v[72:75], v1 offset:34816\n"
    "ds_read_b128 v[76:79], v1 offset:35840\n"
    "v_rcp_f32 v23, v23\n"
    "v_mfma_f32_16x16x32_bf16 v[112:115], v[84:87], v[188:191], v[112:115]\n"
    "s_waitcnt lgkmcnt(4)\n"
    "v_mul_f32 v20, v20, v104\n"
    "v_mul_f32 v21, v21, v105\n"
    "v_mul_f32 v22, v22, v106\n"
    "v_mfma_f32_16x16x32_bf16 v[112:115], v[88:91], v[208:211], v[112:115]\n"
    "ds_read_b128 v[80:83], v1 offset:36864\n"
    "ds_read_b128 v[84:87], v1 offset:37888\n"
    "v_mul_f32 v23, v23, v107\n"
    "v_cvt_pk_bf16_f32 v102, v20, v21\n"
    "v_mfma_f32_16x16x32_bf16 v[112:115], v[92:95], v[160:163], v[112:115]\n"
    "v_cvt_pk_bf16_f32 v103, v22, v23\n"
    "v_mul_f32 v16, v108, v108\n"
    "v_mul_f32 v17, v109, v109\n"
    "v_mfma_f32_16x16x32_bf16 v[112:115], v[56:59], v[192:195], v[112:115]\n"
    "s_add_u32 s54, s73, 0x2000\n"
    "ds_read_b128 v[88:91], v1 offset:38912\n"
    "ds_read_b128 v[92:95], v1 offset:39936\n"
    "buffer_store_dwordx4 v[100:103], v1, s[24:27], s54 offen offset:0 sc1\n"
    "v_mul_f32 v18, v110, v110\n"
    "v_mul_f32 v19, v111, v111\n"
    "v_mfma_f32_16x16x32_bf16 v[112:115], v[60:63], v[128:131], v[112:115]\n"
    "s_waitcnt lgkmcnt(4)\n"
    "v_fma_f32 v16, v16, v126, v125\n"
    "v_fma_f32 v17, v17, v126, v125\n"
    "v_fma_f32 v18, v18, v126, v125\n"
    "v_mfma_f32_16x16x32_bf16 v[116:119], v[64:67], v[132:135], 0\n"
    "ds_read_b128 v[56:59], v1 offset:40960\n"
    "ds_read_b128 v[60:63], v1 offset:41984\n"
    "v_fma_f32 v19, v19, v126, v125\n"
    "v_mul_f32 v16, v16, v108\n"
    "v_mfma_f32_16x16x32_bf16 v[116:119], v[68:71], v[164:167], v[116:119]\n"
    "v_mul_f32 v17, v17, v109\n"
    "v_mul_f32 v18, v18, v110\n"
    "v_mul_f32 v19, v19, v111\n"
    "v_mfma_f32_16x16x32_bf16 v[116:119], v[72:75], v[196:199], v[116:119]\n"
    "ds_read_b128 v[64:67], v1 offset:43008\n"
    "ds_read_b128 v[68:71], v1 offset:44032\n"
    "v_exp_f32 v16, v16\n"
    "v_mfma_f32_16x16x32_bf16 v[116:119], v[76:79], v[136:139], v[116:119]\n"
    "s_waitcnt lgkmcnt(4)\n"
    "v_exp_f32 v17, v17\n"
    "v_exp_f32 v18, v18\n"
    "v_mfma_f32_16x16x32_bf16 v[116:119], v[80:83], v[168:171], v[116:119]\n"
    "ds_read_b128 v[72:75], v1 offset:45056\n"
    "ds_read_b128 v[76:79], v1 offset:46080\n"
    "v_exp_f32 v19, v19\n"
    "v_mfma_f32_16x16x32_bf16 v[116:119], v[84:87], v[140:143], v[116:119]\n"
    "v_add_f32 v16, 1.0, v16\n"
    "v_add_f32 v17, 1.0, v17\n"
    "v_mfma_f32_16x16x32_bf16 v[116:119], v[88:91], v[172:175], v[116:119]\n"
    "ds_read_b128 v[80:83], v1 offset:47104\n"
    "ds_read_b128 v[84:87], v1 offset:48128\n"
    "v_add_f32 v18, 1.0, v18\n"
    "v_add_f32 v19, 1.0, v19\n"
    "v_mfma_f32_16x16x32_bf16 v[116:119], v[92:95], v[200:203], v[116:119]\n"
    "s_waitcnt lgkmcnt(4)\n"
    "v_rcp_f32 v16, v16\n"
    "v_rcp_f32 v17, v17\n"
    "v_mfma_f32_16x16x32_bf16 v[116:119], v[56:59], v[144:147], v[116:119]\n"
    "ds_read_b128 v[88:91], v1 offset:49152\n"
    "ds_read_b128 v[92:95], v1 offset:50176\n"
    "v_rcp_f32 v18, v18\n"
    "v_mfma_f32_16x16x32_bf16 v[116:119], v[60:63], v[176:179], v[116:119]\n"
    "v_rcp_f32 v19, v19\n"
    "v_mfma_f32_16x16x32_bf16 v[116:119], v[64:67], v[148:151], v[116:119]\n"
    "ds_read_b128 v[56:59], v1 offset:51200\n"
    "ds_read_b128 v[60:63], v1 offset:52224\n"
    "v_mul_f32 v16, v16, v108\n"
    "v_mul_f32 v17, v17, v109\n"
    "v_mfma_f32_16x16x32_bf16 v[116:119], v[68:71], v[180:183], v[116:119]\n"
    "s_waitcnt lgkmcnt(4)\n"
    "v_mul_f32 v18, v18, v110\n"
    "v_mul_f32 v19, v19, v111\n"
    "v_cvt_pk_bf16_f32 v108, v16, v17\n"
    "v_mfma_f32_16x16x32_bf16 v[116:119], v[72:75], v[204:207], v[116:119]\n"
    "ds_read_b128 v[64:67], v1 offset:53248\n"
    "ds_read_b128 v[68:71], v1 offset:54272\n"
    "v_cvt_pk_bf16_f32 v109, v18, v19\n"
    "v_mul_f32 v20, v112, v112\n"
    "v_mfma_f32_16x16x32_bf16 v[116:119], v[76:79], v[152:155], v[116:119]\n"
    "v_mul_f32 v21, v113, v113\n"
    "v_mul_f32 v22, v114, v114\n"
    "v_mul_f32 v23, v115, v115\n"
    "v_mfma_f32_16x16x32_bf16 v[116:119], v[80:83], v[184:187], v[116:119]\n"
    "ds_read_b128 v[72:75], v1 offset:55296\n"
    "ds_read_b128 v[76:79], v1 offset:56320\n"
    "v_fma_f32 v20, v20, v126, v125\n"
    "v_fma_f32 v21, v21, v126, v125\n"
    "v_mfma_f32_16x16x32_bf16 v[116:119], v[84:87], v[156:159], v[116:119]\n"
    "s_waitcnt lgkmcnt(4)\n"
    "v_fma_f32 v22, v22, v126, v125\n"
    "v_fma_f32 v23, v23, v126, v125\n"
    "v_mul_f32 v20, v20, v112\n"
    "v_mfma_f32_16x16x32_bf16 v[116:119], v[88:91], v[188:191], v[116:119]\n"
    "ds_read_b128 v[80:83], v1 offset:57344\n"
    "ds_read_b128 v[84:87], v1 offset:58368\n"
    "v_mul_f32 v21, v21, v113\n"
    "v_mul_f32 v22, v22, v114\n"
    "v_mfma_f32_16x16x32_bf16 v[116:119], v[92:95], v[208:211], v[116:119]\n"
    "v_mul_f32 v23, v23, v115\n"
    "v_exp_f32 v20, v20\n"
    "v_mfma_f32_16x16x32_bf16 v[116:119], v[56:59], v[160:163], v[116:119]\n"
    "ds_read_b128 v[88:91], v1 offset:59392\n"
    "ds_read_b128 v[92:95], v1 offset:60416\n"
    "v_exp_f32 v21, v21\n"
    "v_mfma_f32_16x16x32_bf16 v[116:119], v[60:63], v[192:195], v[116:119]\n"
    "s_waitcnt lgkmcnt(4)\n"
    "v_exp_f32 v22, v22\n"
    "v_exp_f32 v23, v23\n"
    "v_mfma_f32_16x16x32_bf16 v[116:119], v[64:67], v[128:131], v[116:119]\n"
    "ds_read_b128 v[56:59], v1 offset:61440\n"
    "ds_read_b128 v[60:63], v1 offset:62464\n"
    "v_add_f32 v20, 1.0, v20\n"
    "v_mfma_f32_16x16x32_bf16 v[120:123], v[68:71], v[132:135], 0\n"
    "v_add_f32 v21, 1.0, v21\n"
    "v_add_f32 v22, 1.0, v22\n"
    "v_add_f32 v23, 1.0, v23\n"
    "v_mfma_f32_16x16x32_bf16 v[120:123], v[72:75], v[164:167], v[120:123]\n"
    "ds_read_b128 v[64:67], v1 offset:63488\n"
    "ds_read_b128 v[68:71], v1 offset:64512\n"
    "v_rcp_f32 v20, v20\n"
    "v_mfma_f32_16x16x32_bf16 v[120:123], v[76:79], v[196:199], v[120:123]\n"
    "s_waitcnt lgkmcnt(4)\n"
    "v_rcp_f32 v21, v21\n"
    "v_rcp_f32 v22, v22\n"
    "v_mfma_f32_16x16x32_bf16 v[120:123], v[80:83], v[136:139], v[120:123]\n"
    "ds_read_b128 v[72:75], v3 offset:0\n"
    "ds_read_b128 v[76:79], v3 offset:1024\n"
    "v_rcp_f32 v23, v23\n"
    "v_mfma_f32_16x16x32_bf16 v[120:123], v[84:87], v[168:171], v[120:123]\n"
    "v_mul_f32 v20, v20, v112\n"
    "v_mul_f32 v21, v21, v113\n"
    "v_mfma_f32_16x16x32_bf16 v[120:123], v[88:91], v[140:143], v[120:123]\n"
    "ds_read_b128 v[80:83], v3 offset:2048\n"
    "ds_read_b128 v[84:87], v3 offset:3072\n"
    "v_mul_f32 v22, v22, v114\n"
    "v_mul_f32 v23, v23, v115\n"
    "v_mfma_f32_16x16x32_bf16 v[120:123], v[92:95], v[172:175], v[120:123]\n"
    "s_waitcnt lgkmcnt(4)\n"
    "v_cvt_pk_bf16_f32 v110, v20, v21\n"
    "v_cvt_pk_bf16_f32 v111, v22, v23\n"
    "v_mul_f32 v16, v116, v116\n"
    "v_mfma_f32_16x16x32_bf16 v[120:123], v[56:59], v[200:203], v[120:123]\n"
    "s_add_u32 s54, s73, 0x2000\n"
    "ds_read_b128 v[88:91], v3 offset:4096\n"
    "ds_read_b128 v[92:95], v3 offset:5120\n"
    "buffer_store_dwordx4 v[108:111], v1, s[24:27], s54 offen offset:2048 sc1\n"
    "v_mul_f32 v17, v117, v117\n"
    "v_mul_f32 v18, v118, v118\n"
    "v_mfma_f32_16x16x32_bf16 v[120:123], v[60:63], v[144:147], v[120:123]\n"
    "v_mul_f32 v19, v119, v119\n"
    "v_fma_f32 v16, v16, v126, v125\n"
    "v_fma_f32 v17, v17, v126, v125\n"
    "v_mfma_f32_16x16x32_bf16 v[120:123], v[64:67], v[176:179], v[120:123]\n"
    "ds_read_b128 v[56:59], v3 offset:6144\n"
    "ds_read_b128 v[60:63], v3 offset:7168\n"
    "v_fma_f32 v18, v18, v126, v125\n"
    "v_fma_f32 v19, v19, v126, v125\n"
    "v_mfma_f32_16x16x32_bf16 v[120:123], v[68:71], v[148:151], v[120:123]\n"
    "s_waitcnt lgkmcnt(4)\n"
    "v_mul_f32 v16, v16, v116\n"
    "v_mul_f32 v17, v17, v117\n"
    "v_mul_f32 v18, v18, v118\n"
    "v_mfma_f32_16x16x32_bf16 v[120:123], v[72:75], v[180:183], v[120:123]\n"
    "ds_read_b128 v[64:67], v3 offset:8192\n"
    "ds_read_b128 v[68:71], v3 offset:9216\n"
    "v_mul_f32 v19, v19, v119\n"
    "v_exp_f32 v16, v16\n"
    "v_mfma_f32_16x16x32_bf16 v[120:123], v[76:79], v[204:207], v[120:123]\n"
    "v_exp_f32 v17, v17\n"
    "v_mfma_f32_16x16x32_bf16 v[120:123], v[80:83], v[152:155], v[120:123]\n"
    "v_exp_f32 v18, v18\n"
    "v_exp_f32 v19, v19\n"
    "v_mfma_f32_16x16x32_bf16 v[120:123], v[84:87], v[184:187], v[120:123]\n"
    "s_waitcnt lgkmcnt(2)\n"
    "v_add_f32 v16, 1.0, v16\n"
    "v_add_f32 v17, 1.0, v17\n"
    "v_add_f32 v18, 1.0, v18\n"
    "v_mfma_f32_16x16x32_bf16 v[120:123], v[88:91], v[156:159], v[120:123]\n"
    "v_add_f32 v19, 1.0, v19\n"
    "v_rcp_f32 v16, v16\n"
    "v_rcp_f32 v17, v17\n"
    "v_mfma_f32_16x16x32_bf16 v[120:123], v[92:95], v[188:191], v[120:123]\n"
    "v_rcp_f32 v18, v18\n"
    "v_mfma_f32_16x16x32_bf16 v[120:123], v[56:59], v[208:211], v[120:123]\n"
    "v_rcp_f32 v19, v19\n"
    "v_mul_f32 v16, v16, v116\n"
    "v_mul_f32 v17, v17, v117\n"
    "v_mfma_f32_16x16x32_bf16 v[120:123], v[60:63], v[160:163], v[120:123]\n"
    "s_waitcnt lgkmcnt(0)\n"
    "v_mul_f32 v18, v18, v118\n"
    "v_mul_f32 v19, v19, v119\n"
    "v_cvt_pk_bf16_f32 v116, v16, v17\n"
    "v_mfma_f32_16x16x32_bf16 v[120:123], v[64:67], v[192:195], v[120:123]\n"
    "v_cvt_pk_bf16_f32 v117, v18, v19\n"
    "v_mfma_f32_16x16x32_bf16 v[120:123], v[68:71], v[128:131], v[120:123]\n"
    "s_waitcnt vmcnt(8)\n"
    "v_max3_u32 v127, v24, v25, v26\n"
    "v_max3_u32 v127, v127, v27, v40\n"
    "v_max3_u32 v127, v127, v41, v42\n"
    "v_max3_u32 v127, v127, v43, v43\n"
    "v_cmp_eq_u32 vcc, -1, v127\n"
    "s_nop 0\n"
    "s_cbranch_vccnz Lrty63\n"
    "Lok64:\n"
    "v_mfma_f32_16x16x32_bf16 v[132:135], a[0:3], v[24:27], 0\n"
    "buffer_load_dwordx4 v[56:59], v1, s[24:27], s68 offen offset:2048 sc1\n"
    "v_mfma_f32_16x16x32_bf16 v[132:135], a[20:23], v[40:43], v[132:135]\n"
    "s_waitcnt vmcnt(7)\n"
    "buffer_load_dwordx4 v[72:75], v1, s[24:27], s68 offen offset:3072 sc1\n"
    "v_mfma_f32_16x16x32_bf16 v[136:139], a[4:7], v[24:27], 0\n"
    "v_max3_u32 v127, v28, v29, v30\n"
    "v_max3_u32 v127, v127, v31, v44\n"
    "v_mfma_f32_16x16x32_bf16 v[136:139], a[24:27], v[40:43], v[136:139]\n"
    "v_max3_u32 v127, v127, v45, v46\n"
    "v_max3_u32 v127, v127, v47, v47\n"
    "v_mfma_f32_16x16x32_bf16 v[140:143], a[8:11], v[24:27], 0\n"
    "v_cmp_eq_u32 vcc, -1, v127\n"
    "v_mul_f32 v20, v120, v120\n"
    "v_mfma_f32_16x16x32_bf16 v[140:143], a[28:31], v[40:43], v[140:143]\n"
    "buffer_load_dwordx4 v[60:63], v1, s[24:27], s69 offen offset:2048 sc1\n"
    "v_mfma_f32_16x16x32_bf16 v[144:147], a[12:15], v[24:27], 0\n"
    "s_cbranch_vccnz Lrty65\n"
    "Lok66:\n"
    "v_mul_f32 v21, v121, v121\n"
    "v_mul_f32 v22, v122, v122\n"
    "v_mfma_f32_16x16x32_bf16 v[144:147], a[32:35], v[40:43], v[144:147]\n"
    "v_mul_f32 v23, v123, v123\n"
    "v_fma_f32 v20, v20, v126, v125\n"
    "v_mfma_f32_16x16x32_bf16 v[148:151], a[16:19], v[24:27], 0\n"
    "v_fma_f32 v21, v21, v126, v125\n"
    "v_fma_f32 v22, v22, v126, v125\n"
    "v_mfma_f32_16x16x32_bf16 v[148:151], a[36:39], v[40:43], v[148:151]\n"
    "buffer_load_dwordx4 v[76:79], v1, s[24:27], s69 offen offset:3072 sc1\n"
    "v_mfma_f32_16x16x32_bf16 v[152:155], a[0:3], v[28:31], 0\n"
    "v_fma_f32 v23, v23, v126, v125\n"
    "v_mul_f32 v20, v20, v120\n"
    "v_mfma_f32_16x16x32_bf16 v[152:155], a[20:23], v[44:47], v[152:155]\n"
    "s_waitcnt vmcnt(8)\n"
    "v_max3_u32 v127, v32, v33, v34\n"
    "v_max3_u32 v127, v127, v35, v48\n"
    "v_mfma_f32_16x16x32_bf16 v[156:159], a[4:7], v[28:31], 0\n"
    "v_max3_u32 v127, v127, v49, v50\n"
    "v_max3_u32 v127, v127, v51, v51\n"
    "v_mfma_f32_16x16x32_bf16 v[156:159], a[24:27], v[44:47], v[156:159]\n"
    "v_cmp_eq_u32 vcc, -1, v127\n"
    "v_mul_f32 v21, v21, v121\n"
    "v_mfma_f32_16x16x32_bf16 v[160:163], a[8:11], v[28:31], 0\n"
    "buffer_load_dwordx4 v[64:67], v1, s[24:27], s70 offen offset:2048 sc1\n"
    "v_mfma_f32_16x16x32_bf16 v[160:163], a[28:31], v[44:47], v[160:163]\n"
    "s_cbranch_vccnz Lrty67\n"
    "Lok68:\n"
    "v_mul_f32 v22, v22, v122\n"
    "v_mul_f32 v23, v23, v123\n"
    "v_mfma_f32_16x16x32_bf16 v[164:167], a[12:15], v[28:31], 0\n"
    "v_exp_f32 v20, v20\n"
    "v_mfma_f32_16x16x32_bf16 v[164:167], a[32:35], v[44:47], v[164:167]\n"
    "v_exp_f32 v21, v21\n"
    "v_mfma_f32_16x16x32_bf16 v[168:171], a[16:19], v[28:31], 0\n"
    "buffer_load_dwordx4 v[80:83], v1, s[24:27], s70 offen offset:3072 sc1\n"
    "v_mfma_f32_16x16x32_bf16 v[168:171], a[36:39], v[44:47], v[168:171]\n"
    "v_exp_f32 v22, v22\n"
    "v_mfma_f32_16x16x32_bf16 v[172:175], a[0:3], v[32:35], 0\n"
    "v_exp_f32 v23, v23\n"
    "v_mfma_f32_16x16x32_bf16 v[172:175], a[20:23], v[48:51], v[172:175]\n"
    "s_waitcnt vmcnt(8)\n"
    "v_max3_u32 v127, v36, v37, v38\n"
    "v_max3_u32 v127, v127, v39, v52\n"
    "v_mfma_f32_16x16x32_bf16 v[176:179], a[4:7], v[32:35], 0\n"
    "buffer_load_dwordx4 v[68:71], v1, s[24:27], s71 offen offset:2048 sc1\n"
    "v_mfma_f32_16x16x32_bf16 v[176:179], a[24:27], v[48:51], v[176:179]\n"
    "v_max3_u32 v127, v127, v53, v54\n"
    "v_max3_u32 v127, v127, v55, v55\n"
    "v_mfma_f32_16x16x32_bf16 v[180:183], a[8:11], v[32:35], 0\n"
    "v_cmp_eq_u32 vcc, -1, v127\n"
    "v_add_f32 v20, 1.0, v20\n"
    "v_mfma_f32_16x16x32_bf16 v[180:183], a[28:31], v[48:51], v[180:183]\n"
    "v_add_f32 v21, 1.0, v21\n"
    "v_add_f32 v22, 1.0, v22\n"
    "v_mfma_f32_16x16x32_bf16 v[184:187], a[12:15], v[32:35], 0\n"
    "s_cbranch_vccnz Lrty69\n"
    "Lok70:\n"
    "buffer_load_dwordx4 v[84:87], v1, s[24:27], s71 offen offset:3072 sc1\n"
    "v_mfma_f32_16x16x32_bf16 v[184:187], a[32:35], v[48:51], v[184:187]\n"
    "s_add_u32 s44, s68, 0x1000\n"
    "v_add_f32 v23, 1.0, v23\n"
    "v_mfma_f32_16x16x32_bf16 v[188:191], a[16:19], v[32:35], 0\n"
    "v_rcp_f32 v20, v20\n"
    "v_mfma_f32_16x16x32_bf16 v[188:191], a[36:39], v[48:51], v[188:191]\n"
    "v_rcp_f32 v21, v21\n"
    "v_mfma_f32_16x16x32_bf16 v[192:195], a[0:3], v[36:39], 0\n"
    "Lhop9:\n"
    "s_cbranch_execz LhopB\n"
    "buffer_load_dwordx4 v[24:27], v1, s[24:27], s44 offen offset:0 sc1\n"
    "v_mfma_f32_16x16x32_bf16 v[192:195], a[20:23], v[52:55], v[192:195]\n"
    "s_waitcnt vmcnt(7)\n"
    "v_max3_u32 v127, v56, v57, v58\n"
    "v_max3_u32 v127, v127, v59, v72\n"
    "v_mfma_f32_16x16x32_bf16 v[196:199], a[4:7], v[36:39], 0\n"
    "v_max3_u32 v127, v127, v73, v74\n"
    "v_max3_u32 v127, v127, v75, v75\n"
    "v_mfma_f32_16x16x32_bf16 v[196:199], a[24:27], v[52:55], v[196:199]\n"
    "v_cmp_eq_u32 vcc, -1, v127\n"
    "v_mfma_f32_16x16x32_bf16 v[200:203], a[8:11], v[36:39], 0\n"
    "buffer_load_dwordx4 v[40:43], v1, s[24:27], s44 offen offset:1024 sc1\n"
    "v_mfma_f32_16x16x32_bf16 v[200:203], a[28:31], v[52:55], v[200:203]\n"
    "s_cbranch_vccnz Lrty71\n"
    "Lok72:\n"
    "v_rcp_f32 v22, v22\n"
    "v_mfma_f32_16x16x32_bf16 v[204:207], a[12:15], v[36:39], 0\n"
    "s_add_u32 s45, s69, 0x1000\n"
    "v_rcp_f32 v23, v23\n"
    "v_mfma_f32_16x16x32_bf16 v[204:207], a[32:35], v[52:55], v[204:207]\n"
    "v_mul_f32 v20, v20, v120\n"
    "v_mul_f32 v21, v21, v121\n"
    "v_mfma_f32_16x16x32_bf16 v[208:211], a[16:19], v[36:39], 0\n"
    "buffer_load_dwordx4 v[28:31], v1, s[24:27], s45 offen offset:0 sc1\n"
    "v_mfma_f32_16x16x32_bf16 v[208:211], a[36:39], v[52:55], v[208:211]\n"
    "v_mul_f32 v22, v22, v122\n"
    "v_mul_f32 v23, v23, v123\n"
    "v_mfma_f32_16x16x32_bf16 v[132:135], a[40:43], v[56:59], v[132:135]\n"
    "v_cvt_pk_bf16_f32 v118, v20, v21\n"
    "v_cvt_pk_bf16_f32 v119, v22, v23\n"
    "v_mfma_f32_16x16x32_bf16 v[132:135], a[60:63], v[72:75], v[132:135]\n"
    "s_waitcnt vmcnt(7)\n"
    "v_max3_u32 v127, v60, v61, v62\n"
    "v_max3_u32 v127, v127, v63, v76\n"
    "v_mfma_f32_16x16x32_bf16 v[136:139], a[44:47], v[56:59], v[136:139]\n"
    "s_add_u32 s54, s73, 0x3000\n"
    "v_max3_u32 v127, v127, v77, v78\n"
    "v_max3_u32 v127, v127, v79, v79\n"
    "v_mfma_f32_16x16x32_bf16 v[136:139], a[64:67], v[72:75], v[136:139]\n"
    "buffer_store_dwordx4 v[116:119], v9, s[24:27], s54 offen offset:0 sc1\n"
    "v_mfma_f32_16x16x32_bf16 v[140:143], a[48:51], v[56:59], v[140:143]\n"
    "v_cmp_eq_u32 vcc, -1, v127\n"
    "v_mfma_f32_16x16x32_bf16 v[140:143], a[68:71], v[72:75], v[140:143]\n"
    "v_mfma_f32_16x16x32_bf16 v[144:147], a[52:55], v[56:59], v[144:147]\n"
    "s_cbranch_vccnz Lrty73\n"
    "Lok74:\n"
    "buffer_load_dwordx4 v[44:47], v1, s[24:27], s45 offen offset:1024 sc1\n"
    "v_mfma_f32_16x16x32_bf16 v[144:147], a[72:75], v[72:75], v[144:147]\n"
    "s_add_u32 s46, s70, 0x1000\n"
    "v_mfma_f32_16x16x32_bf16 v[148:151], a[56:59], v[56:59], v[148:151]\n"
    "v_mfma_f32_16x16x32_bf16 v[148:151], a[76:79], v[72:75], v[148:151]\n"
    "v_mfma_f32_16x16x32_bf16 v[152:155], a[40:43], v[60:63], v[152:155]\n"
    "buffer_load_dwordx4 v[32:35], v1, s[24:27], s46 offen offset:0 sc1\n"
    "v_mfma_f32_16x16x32_bf16 v[152:155], a[60:63], v[76:79], v[152:155]\n"
    "s_waitcnt vmcnt(8)\n"
    "v_max3_u32 v127, v64, v65, v66\n"
    "v_max3_u32 v127, v127, v67, v80\n"
    "v_mfma_f32_16x16x32_bf16 v[156:159], a[44:47], v[60:63], v[156:159]\n"
    "v_max3_u32 v127, v127, v81, v82\n"
    "v_max3_u32 v127, v127, v83, v83\n"
    "v_mfma_f32_16x16x32_bf16 v[156:159], a[64:67], v[76:79], v[156:159]\n"
    "v_cmp_eq_u32 vcc, -1, v127\n"
    "v_mfma_f32_16x16x32_bf16 v[160:163], a[48:51], v[60:63], v[160:163]\n"
    "buffer_load_dwordx4 v[48:51], v1, s[24:27], s46 offen offset:1024 sc1\n"
    "v_mfma_f32_16x16x32_bf16 v[160:163], a[68:71], v[76:79], v[160:163]\n"
    "s_cbranch_vccnz Lrty75\n"
    "Lok76:\n"
    "v_mfma_f32_16x16x32_bf16 v[164:167], a[52:55], v[60:63], v[164:167]\n"
    "s_add_u32 s47, s71, 0x1000\n"
    "v_mfma_f32_16x16x32_bf16 v[164:167], a[72:75], v[76:79], v[164:167]\n"
    "v_mfma_f32_16x16x32_bf16 v[168:171], a[56:59], v[60:63], v[168:171]\n"
    "buffer_load_dwordx4 v[36:39], v1, s[24:27], s47 offen offset:0 sc1\n"
    "v_mfma_f32_16x16x32_bf16 v[168:171], a[76:79], v[76:79], v[168:171]\n"
    "v_mfma_f32_16x16x32_bf16 v[172:175], a[40:43], v[64:67], v[172:175]\n"
    "v_mfma_f32_16x16x32_bf16 v[172:175], a[60:63], v[80:83], v[172:175]\n"
    "s_waitcnt vmcnt(8)\n"
    "v_max3_u32 v127, v68, v69, v70\n"
    "v_max3_u32 v127, v127, v71, v84\n"
    "v_mfma_f32_16x16x32_bf16 v[176:179], a[44:47], v[64:67], v[176:179]\n"
    "buffer_load_dwordx4 v[52:55], v1, s[24:27], s47 offen offset:1024 sc1\n"
    "v_mfma_f32_16x16x32_bf16 v[176:179], a[64:67], v[80:83], v[176:179]\n"
    "v_max3_u32 v127, v127, v85, v86\n"
    "v_max3_u32 v127, v127, v87, v87\n"
    "v_mfma_f32_16x16x32_bf16 v[180:183], a[48:51], v[64:67], v[180:183]\n"
    "v_cmp_eq_u32 vcc, -1, v127\n"
    "v_mfma_f32_16x16x32_bf16 v[180:183], a[68:71], v[80:83], v[180:183]\n"
    "v_mfma_f32_16x16x32_bf16 v[184:187], a[52:55], v[64:67], v[184:187]\n"
    "s_cbranch_vccnz Lrty77\n"
    "Lok78:\n"
    "buffer_load_dwordx4 v[56:59], v1, s[24:27], s44 offen offset:2048 sc1\n"
    "v_mfma_f32_16x16x32_bf16 v[184:187], a[72:75], v[80:83], v[184:187]\n"
    "v_mfma_f32_16x16x32_bf16 v[188:191], a[56:59], v[64:67], v[188:191]\n"
    "v_mfma_f32_16x16x32_bf16 v[188:191], a[76:79], v[80:83], v[188:191]\n"
    "v_mfma_f32_16x16x32_bf16 v[192:195], a[40:43], v[68:71], v[192:195]\n"
    "buffer_load_dwordx4 v[72:75], v1, s[24:27], s44 offen offset:3072 sc1\n"
    "v_mfma_f32_16x16x32_bf16 v[192:195], a[60:63], v[84:87], v[192:195]\n"
    "s_waitcnt vmcnt(9)\n"
    "v_max3_u32 v127, v24, v25, v26\n"
    "v_max3_u32 v127, v127, v27, v40\n"
    "v_mfma_f32_16x16x32_bf16 v[196:199], a[44:47], v[68:71], v[196:199]\n"
    "v_max3_u32 v127, v127, v41, v42\n"
    "v_max3_u32 v127, v127, v43, v43\n"
    "v_mfma_f32_16x16x32_bf16 v[196:199], a[64:67], v[84:87], v[196:199]\n"
    "v_cmp_eq_u32 vcc, -1, v127\n"
    "v_mfma_f32_16x16x32_bf16 v[200:203], a[48:51], v[68:71], v[200:203]\n"
    "buffer_load_dwordx4 v[60:63], v1, s[24:27], s45 offen offset:2048 sc1\n"
    "v_mfma_f32_16x16x32_bf16 v[200:203], a[68:71], v[84:87], v[200:203]\n"
    "s_cbranch_vccnz Lrty79\n"
    "Lok80:\n"
    "v_mfma_f32_16x16x32_bf16 v[204:207], a[52:55], v[68:71], v[204:207]\n"
    "v_mfma_f32_16x16x32_bf16 v[204:207], a[72:75], v[84:87], v[204:207]\n"
    "v_mfma_f32_16x16x32_bf16 v[208:211], a[56:59], v[68:71], v[208:211]\n"
    "v_mfma_f32_16x16x32_bf16 v[208:211], a[76:79], v[84:87], v[208:211]\n"
    "buffer_load_dwordx4 v[76:79], v1, s[24:27], s45 offen offset:3072 sc1\n"
    "v_mfma_f32_16x16x32_bf16 v[132:135], a[80:83], v[24:27], v[132:135]\n"
    "v_mfma_f32_16x16x32_bf16 v[132:135], a[100:103], v[40:43], v[132:135]\n"
    "s_waitcnt vmcnt(8)\n"
    "v_max3_u32 v127, v28, v29, v30\n"
    "v_max3_u32 v127, v127, v31, v44\n"
    "v_mfma_f32_16x16x32_bf16 v[136:139], a[84:87], v[24:27], v[136:139]\n"
    "v_max3_u32 v127, v127, v45, v46\n"
    "v_max3_u32 v127, v127, v47, v47\n"
    "v_mfma_f32_16x16x32_bf16 v[136:139], a[104:107], v[40:43], v[136:139]\n"
    "buffer_load_dwordx4 v[64:67], v1, s[24:27], s46 offen offset:2048 sc1\n"
    "v_mfma_f32_16x16x32_bf16 v[140:143], a[88:91], v[24:27], v[140:143]\n"
    "v_cmp_eq_u32 vcc, -1, v127\n"
    "v_mfma_f32_16x16x32_bf16 v[140:143], a[108:111], v[40:43], v[140:143]\n"
    "v_mfma_f32_16x16x32_bf16 v[144:147], a[92:95], v[24:27], v[144:147]\n"
    "s_cbranch_vccnz Lrty81\n"
    "Lok82:\n"
    "v_mfma_f32_16x16x32_bf16 v[144:147], a[112:115], v[40:43], v[144:147]\n"
    "buffer_load_dwordx4 v[80:83], v1, s[24:27], s46 offen offset:3072 sc1\n"
    "v_mfma_f32_16x16x32_bf16 v[148:151], a[96:99], v[24:27], v[148:151]\n"
    "v_mfma_f32_16x16x32_bf16 v[148:151], a[116:119], v[40:43], v[148:151]\n"
    "v_mfma_f32_16x16x32_bf16 v[152:155], a[80:83], v[28:31], v[152:155]\n"
    "v_mfma_f32_16x16x32_bf16 v[152:155], a[100:103], v[44:47], v[152:155]\n"
    "s_waitcnt vmcnt(8)\n"
    "buffer_load_dwordx4 v[68:71], v1, s[24:27], s47 offen offset:2048 sc1\n"
    "v_mfma_f32_16x16x32_bf16 v[156:159], a[84:87], v[28:31], v[156:159]\n"
    "v_max3_u32 v127, v32, v33, v34\n"
    "v_max3_u32 v127, v127, v35, v48\n"
    "v_mfma_f32_16x16x32_bf16 v[156:159], a[104:107], v[44:47], v[156:159]\n"
    "v_max3_u32 v127, v127, v49, v50\n"
    "v_max3_u32 v127, v127, v51, v51\n"
    "v_mfma_f32_16x16x32_bf16 v[160:163], a[88:91], v[28:31], v[160:163]\n"
    "v_cmp_eq_u32 vcc, -1, v127\n"
    "v_mfma_f32_16x16x32_bf16 v[160:163], a[108:111], v[44:47], v[160:163]\n"
    "buffer_load_dwordx4 v[84:87], v1, s[24:27], s47 offen offset:3072 sc1\n"
    "v_mfma_f32_16x16x32_bf16 v[164:167], a[92:95], v[28:31], v[164:167]\n"
    "s_cbranch_vccnz Lrty83\n"
    "Lok84:\n"
    "v_mfma_f32_16x16x32_bf16 v[164:167], a[112:115], v[44:47], v[164:167]\n"
    "s_add_u32 s44, s68, 0x2000\n"
    "v_mfma_f32_16x16x32_bf16 v[168:171], a[96:99], v[28:31], v[168:171]\n"
    "v_mfma_f32_16x16x32_bf16 v[168:171], a[116:119], v[44:47], v[168:171]\n"
    "buffer_load_dwordx4 v[24:27], v1, s[24:27], s44 offen offset:0 sc1\n"
    "v_mfma_f32_16x16x32_bf16 v[172:175], a[80:83], v[32:35], v[172:175]\n"
    "v_mfma_f32_16x16x32_bf16 v[172:175], a[100:103], v[48:51], v[172:175]\n"
    "s_waitcnt vmcnt(9)\n"
    "v_max3_u32 v127, v36, v37, v38\n"
    "v_max3_u32 v127, v127, v39, v52\n"
    "v_mfma_f32_16x16x32_bf16 v[176:179], a[84:87], v[32:35], v[176:179]\n"
    "v_max3_u32 v127, v127, v53, v54\n"
    "v_max3_u32 v127, v127, v55, v55\n"
    "v_mfma_f32_16x16x32_bf16 v[176:179], a[104:107], v[48:51], v[176:179]\n"
    "buffer_load_dwordx4 v[40:43], v1, s[24:27], s44 offen offset:1024 sc1\n"
    "v_mfma_f32_16x16x32_bf16 v[180:183], a[88:91], v[32:35], v[180:183]\n"
    "s_add_u32 s45, s69, 0x2000\n"
    "v_cmp_eq_u32 vcc, -1, v127\n"
    "v_mfma_f32_16x16x32_bf16 v[180:183], a[108:111], v[48:51], v[180:183]\n"
    "v_mfma_f32_16x16x32_bf16 v[184:187], a[92:95], v[32:35], v[184:187]\n"
    "s_cbranch_vccnz Lrty85\n"
    "Lok86:\n"
    "v_mfma_f32_16x16x32_bf16 v[184:187], a[112:115], v[48:51], v[184:187]\n"
    "buffer_load_dwordx4 v[28:31], v1, s[24:27], s45 offen offset:0 sc1\n"
    "v_mfma_f32_16x16x32_bf16 v[188:191], a[96:99], v[32:35], v[188:191]\n"
    "v_mfma_f32_16x16x32_bf16 v[188:191], a[116:119], v[48:51], v[188:191]\n"
    "v_mfma_f32_16x16x32_bf16 v[192:195], a[80:83], v[36:39], v[192:195]\n"
    "v_mfma_f32_16x16x32_bf16 v[192:195], a[100:103], v[52:55], v[192:195]\n"
    "s_waitcnt vmcnt(9)\n"
    "buffer_load_dwordx4 v[44:47], v1, s[24:27], s45 offen offset:1024 sc1\n"
    "v_mfma_f32_16x16x32_bf16 v[196:199], a[84:87], v[36:39], v[196:199]\n"
    "s_add_u32 s46, s70, 0x2000\n"
    "v_max3_u32 v127, v56, v57, v58\n"
    "v_max3_u32 v127, v127, v59, v72\n"
    "v_mfma_f32_16x16x32_bf16 v[196:199], a[104:107], v[52:55], v[196:199]\n"
    "v_max3_u32 v127, v127, v73, v74\n"
    "v_max3_u32 v127, v127, v75, v75\n"
    "v_mfma_f32_16x16x32_bf16 v[200:203], a[88:91], v[36:39], v[200:203]\n"
    "v_cmp_eq_u32 vcc, -1, v127\n"
    "v_mfma_f32_16x16x32_bf16 v[200:203], a[108:111], v[52:55], v[200:203]\n"
    "v_mfma_f32_16x16x32_bf16 v[204:207], a[92:95], v[36:39], v[204:207]\n"
    "s_cbranch_vccnz Lrty87\n"
    "Lok88:\n"
    "buffer_load_dwordx4 v[32:35], v1, s[24:27], s46 offen offset:0 sc1\n"
    "v_mfma_f32_16x16x32_bf16 v[204:207], a[112:115], v[52:55], v[204:207]\n"
    "v_mfma_f32_16x16x32_bf16 v[208:211], a[96:99], v[36:39], v[208:211]\n"
    "v_mfma_f32_16x16x32_bf16 v[208:211], a[116:119], v[52:55], v[208:211]\n"
    "v_mfma_f32_16x16x32_bf16 v[132:135], a[120:123], v[56:59], v[132:135]\n"
    "buffer_load_dwordx4 v[48:51], v1, s[24:27], s46 offen offset:1024 sc1\n"
    "v_mfma_f32_16x16x32_bf16 v[132:135], a[140:143], v[72:75], v[132:135]\n"
    "s_waitcnt vmcnt(10)\n"
    "v_max3_u32 v127, v60, v61, v62\n"
    "v_max3_u32 v127, v127, v63, v76\n"
    "v_mfma_f32_16x16x32_bf16 v[136:139], a[124:127], v[56:59], v[136:139]\n"
    "s_add_u32 s47, s71, 0x2000\n"
    "v_max3_u32 v127, v127, v77, v78\n"
    "v_max3_u32 v127, v127, v79, v79\n"
    "v_mfma_f32_16x16x32_bf16 v[136:139], a[144:147], v[72:75], v[136:139]\n"
    "v_cmp_eq_u32 vcc, -1, v127\n"
    "v_mfma_f32_16x16x32_bf16 v[140:143], a[128:131], v[56:59], v[140:143]\n"
    "buffer_load_dwordx4 v[36:39], v1, s[24:27], s47 offen offset:0 sc1\n"
    "v_mfma_f32_16x16x32_bf16 v[140:143], a[148:151], v[72:75], v[140:143]\n"
    "s_cbranch_vccnz Lrty89\n"
    "Lok90:\n"
    "v_mfma_f32_16x16x32_bf16 v[144:147], a[132:135], v[56:59], v[144:147]\n"
    "v_mfma_f32_16x16x32_bf16 v[144:147], a[152:155], v[72:75], v[144:147]\n"
    "v_mfma_f32_16x16x32_bf16 v[148:151], a[136:139], v[56:59], v[148:151]\n"
    "buffer_load_dwordx4 v[52:55], v1, s[24:27], s47 offen offset:1024 sc1\n"
    "v_mfma_f32_16x16x32_bf16 v[148:151], a[156:159], v[72:75], v[148:151]\n"
    "v_mfma_f32_16x16x32_bf16 v[152:155], a[120:123], v[60:63], v[152:155]\n"
    "v_mfma_f32_16x16x32_bf16 v[152:155], a[140:143], v[76:79], v[152:155]\n"
    "s_waitcnt vmcnt(10)\n"
    "v_max3_u32 v127, v64, v65, v66\n"
    "v_max3_u32 v127, v127, v67, v80\n"
    "v_mfma_f32_16x16x32_bf16 v[156:159], a[124:127], v[60:63], v[156:159]\n"
    "buffer_load_dwordx4 v[56:59], v1, s[24:27], s44 offen offset:2048 sc1\n"
    "v_mfma_f32_16x16x32_bf16 v[156:159], a[144:147], v[76:79], v[156:159]\n"
    "v_max3_u32 v127, v127, v81, v82\n"
    "v_max3_u32 v127, v127, v83, v83\n"
    "v_mfma_f32_16x16x32_bf16 v[160:163], a[128:131], v[60:63], v[160:163]\n"
    "v_cmp_eq_u32 vcc, -1, v127\n"
    "v_mfma_f32_16x16x32_bf16 v[160:163], a[148:151], v[76:79], v[160:163]\n"
    "v_mfma_f32_16x16x32_bf16 v[164:167], a[132:135], v[60:63], v[164:167]\n"
    "s_cbranch_vccnz Lrty91\n"
    "Lok92:\n"
    "buffer_load_dwordx4 v[72:75], v1, s[24:27], s44 offen offset:3072 sc1\n"
    "v_mfma_f32_16x16x32_bf16 v[164:167], a[152:155], v[76:79], v[164:167]\n"
    "s_add_u32 s48, s68, 0x3000\n"
    "v_mfma_f32_16x16x32_bf16 v[168:171], a[136:139], v[60:63], v[168:171]\n"
    "v_mfma_f32_16x16x32_bf16 v[168:171], a[156:159], v[76:79], v[168:171]\n"
    "v_mfma_f32_16x16x32_bf16 v[172:175], a[120:123], v[64:67], v[172:175]\n"
    "buffer_load_dwordx4 v[88:91], v1, s[24:27], s48 offen offset:0 sc1\n"
    "v_mfma_f32_16x16x32_bf16 v[172:175], a[140:143], v[80:83], v[172:175]\n"
    "s_waitcnt vmcnt(11)\n"
    "v_max3_u32 v127, v68, v69, v70\n"
    "v_max3_u32 v127, v127, v71, v84\n"
    "v_mfma_f32_16x16x32_bf16 v[176:179], a[124:127], v[64:67], v[176:179]\n"
    "v_max3_u32 v127, v127, v85, v86\n"
    "v_max3_u32 v127, v127, v87, v87\n"
    "v_mfma_f32_16x16x32_bf16 v[176:179], a[144:147], v[80:83], v[176:179]\n"
    "v_cmp_eq_u32 vcc, -1, v127\n"
    "v_mfma_f32_16x16x32_bf16 v[180:183], a[128:131], v[64:67], v[180:183]\n"
    "v_mfma_f32_16x16x32_bf16 v[180:183], a[148:151], v[80:83], v[180:183]\n"
    "s_cbranch_vccnz Lrty93\n"
    "Lok94:\n"
    "buffer_load_dwordx4 v[104:107], v10, s[24:27], s48 offen offset:1024 sc1\n"
    "v_mfma_f32_16x16x32_bf16 v[184:187], a[132:135], v[64:67], v[184:187]\n"
    "v_mfma_f32_16x16x32_bf16 v[184:187], a[152:155], v[80:83], v[184:187]\n"
    "v_mfma_f32_16x16x32_bf16 v[188:191], a[136:139], v[64:67], v[188:191]\n"
    "v_mfma_f32_16x16x32_bf16 v[188:191], a[156:159], v[80:83], v[188:191]\n"
    "buffer_load_dwordx4 v[60:63], v1, s[24:27], s45 offen offset:2048 sc1\n"
    "v_mfma_f32_16x16x32_bf16 v[192:195], a[120:123], v[68:71], v[192:195]\n"
    "v_mfma_f32_16x16x32_bf16 v[192:195], a[140:143], v[84:87], v[192:195]\n"
    "s_waitcnt vmcnt(11)\n"
    "v_max3_u32 v127, v24, v25, v26\n"
    "v_max3_u32 v127, v127, v27, v40\n"
    "v_mfma_f32_16x16x32_bf16 v[196:199], a[124:127], v[68:71], v[196:199]\n"
    "v_max3_u32 v127, v127, v41, v42\n"
    "v_max3_u32 v127, v127, v43, v43\n"
    "v_mfma_f32_16x16x32_bf16 v[196:199], a[144:147], v[84:87], v[196:199]\n"
    "buffer_load_dwordx4 v[76:79], v1, s[24:27], s45 offen offset:3072 sc1\n"
    "v_mfma_f32_16x16x32_bf16 v[200:203], a[128:131], v[68:71], v[200:203]\n"
    "s_add_u32 s48, s69, 0x3000\n"
    "v_cmp_eq_u32 vcc, -1, v127\n"
    "v_mfma_f32_16x16x32_bf16 v[200:203], a[148:151], v[84:87], v[200:203]\n"
    "v_mfma_f32_16x16x32_bf16 v[204:207], a[132:135], v[68:71], v[204:207]\n"
    "s_cbranch_vccnz Lrty95\n"
    "Lok96:\n"
    "v_mfma_f32_16x16x32_bf16 v[204:207], a[152:155], v[84:87], v[204:207]\n"
    "buffer_load_dwordx4 v[92:95], v1, s[24:27], s48 offen offset:0 sc1\n"
    "v_mfma_f32_16x16x32_bf16 v[208:211], a[136:139], v[68:71], v[208:211]\n"
    "v_mfma_f32_16x16x32_bf16 v[208:211], a[156:159], v[84:87], v[208:211]\n"
    "v_mfma_f32_16x16x32_bf16 v[132:135], a[160:163], v[24:27], v[132:135]\n"
    "s_mul_i32 s51, s41, 0x3c00\n"
    "v_add_u32 v16, s51, v1\n"
    "v_mfma_f32_16x16x32_bf16 v[132:135], a[180:183], v[40:43], v[132:135]\n"
    "s_waitcnt vmcnt(11)\n"
    "buffer_load_dwordx4 v[108:111], v10, s[24:27], s48 offen offset:1024 sc1\n"
    "v_mfma_f32_16x16x32_bf16 v[136:139], a[164:167], v[24:27], v[136:139]\n"
    "s_add_u32 s51, s79, 0x10000\n"
    "v_max3_u32 v127, v28, v29, v30\n"
    "v_max3_u32 v127, v127, v31, v44\n"
    "v_mfma_f32_16x16x32_bf16 v[136:139], a[184:187], v[40:43], v[136:139]\n"
    "v_max3_u32 v127, v127, v45, v46\n"
    "v_max3_u32 v127, v127, v47, v47\n"
    "v_mfma_f32_16x16x32_bf16 v[140:143], a[168:171], v[24:27], v[140:143]\n"
    "v_cmp_eq_u32 vcc, -1, v127\n"
    "v_bfe_u32 v17, v1, 4, 4\n"
    "v_mfma_f32_16x16x32_bf16 v[140:143], a[188:191], v[40:43], v[140:143]\n"
    "buffer_load_dwordx4 v[64:67], v1, s[24:27], s46 offen offset:2048 sc1\n"
    "v_mfma_f32_16x16x32_bf16 v[144:147], a[172:175], v[24:27], v[144:147]\n"
    "s_cbranch_vccnz Lrty97\n"
    "Lok98:\n"
    "v_lshrrev_b32 v18, 8, v1\n"
    "v_lshl_add_u32 v18, v17, 2, v18\n"
    "v_mfma_f32_16x16x32_bf16 v[144:147], a[192:195], v[40:43], v[144:147]\n"
    "v_lshl_add_u32 v17, v18, 4, s51\n"
    "v_lshl_add_u32 v18, v18, 3, s51\n"
    "v_mfma_f32_16x16x32_bf16 v[148:151], a[176:179], v[24:27], v[148:151]\n"
    "v_add_u32 v19, s51, v1\n"
    "v_lshrrev_b32 v23, 1, v1\n"
    "v_mfma_f32_16x16x32_bf16 v[148:151], a[196:199], v[40:43], v[148:151]\n"
    "buffer_load_dwordx4 v[80:83], v1, s[24:27], s46 offen offset:3072 sc1\n"
    "v_mfma_f32_16x16x32_bf16 v[152:155], a[160:163], v[28:31], v[152:155]\n"
    "s_add_u32 s48, s70, 0x3000\n"
    "v_add_u32 v23, s51, v23\n"
    "v_mfma_f32_16x16x32_bf16 v[152:155], a[180:183], v[44:47], v[152:155]\n"
    "s_waitcnt vmcnt(12)\n"
    "v_max3_u32 v127, v32, v33, v34\n"
    "v_max3_u32 v127, v127, v35, v48\n"
    "v_mfma_f32_16x16x32_bf16 v[156:159], a[164:167], v[28:31], v[156:159]\n"
    "v_max3_u32 v127, v127, v49, v50\n"
    "v_max3_u32 v127, v127, v51, v51\n"
    "v_mfma_f32_16x16x32_bf16 v[156:159], a[184:187], v[44:47], v[156:159]\n"
    "buffer_load_dwordx4 v[96:99], v1, s[24:27], s48 offen offset:0 sc1\n"
    "v_mfma_f32_16x16x32_bf16 v[160:163], a[168:171], v[28:31], v[160:163]\n"
    "v_cmp_eq_u32 vcc, -1, v127\n"
    "v_mfma_f32_16x16x32_bf16 v[160:163], a[188:191], v[44:47], v[160:163]\n"
    "v_mfma_f32_16x16x32_bf16 v[164:167], a[172:175], v[28:31], v[164:167]\n"
    "s_cbranch_vccnz Lrty99\n"
    "Lok100:\n"
    "v_mfma_f32_16x16x32_bf16 v[164:167], a[192:195], v[44:47], v[164:167]\n"
    "v_mfma_f32_16x16x32_bf16 v[168:171], a[176:179], v[28:31], v[168:171]\n"
    "buffer_load_dwordx4 v[112:115], v10, s[24:27], s48 offen offset:1024 sc1\n"
    "v_mfma_f32_16x16x32_bf16 v[168:171], a[196:199], v[44:47], v[168:171]\n"
    "v_mfma_f32_16x16x32_bf16 v[172:175], a[160:163], v[32:35], v[172:175]\n"
    "v_mfma_f32_16x16x32_bf16 v[172:175], a[180:183], v[48:51], v[172:175]\n"
    "s_waitcnt vmcnt(12)\n"
    "buffer_load_dwordx4 v[68:71], v1, s[24:27], s47 offen offset:2048 sc1\n"
    "v_mfma_f32_16x16x32_bf16 v[176:179], a[164:167], v[32:35], v[176:179]\n"
    "v_max3_u32 v127, v36, v37, v38\n"
    "v_max3_u32 v127, v127, v39, v52\n"
    "v_mfma_f32_16x16x32_bf16 v[176:179], a[184:187], v[48:51], v[176:179]\n"
    "v_max3_u32 v127, v127, v53, v54\n"
    "v_max3_u32 v127, v127, v55, v55\n"
    "v_mfma_f32_16x16x32_bf16 v[180:183], a[168:171], v[32:35], v[180:183]\n"
    "v_cmp_eq_u32 vcc, -1, v127\n"
    "v_mfma_f32_16x16x32_bf16 v[180:183], a[188:191], v[48:51], v[180:183]\n"
    "v_mfma_f32_16x16x32_bf16 v[184:187], a[172:175], v[32:35], v[184:187]\n"
    "s_cbranch_vccnz Lrty101\n"
    "Lok102:\n"
    "buffer_load_dwordx4 v[84:87], v1, s[24:27], s47 offen offset:3072 sc1\n"
    "v_mfma_f32_16x16x32_bf16 v[184:187], a[192:195], v[48:51], v[184:187]\n"
    "s_add_u32 s48, s71, 0x3000\n"
    "v_mfma_f32_16x16x32_bf16 v[188:191], a[176:179], v[32:35], v[188:191]\n"
    "v_mfma_f32_16x16x32_bf16 v[188:191], a[196:199], v[48:51], v[188:191]\n"
    "v_mfma_f32_16x16x32_bf16 v[192:195], a[160:163], v[36:39], v[192:195]\n"
    "buffer_load_dwordx4 v[100:103], v1, s[24:27], s48 offen offset:0 sc1\n"
    "v_mfma_f32_16x16x32_bf16 v[192:195], a[180:183], v[52:55], v[192:195]\n"
    "s_waitcnt vmcnt(11)\n"
    "v_max3_u32 v127, v56, v57, v58\n"
    "v_max3_u32 v127, v127, v59, v72\n"
    "v_mfma_f32_16x16x32_bf16 v[196:199], a[164:167], v[36:39], v[196:199]\n"
    "v_max3_u32 v127, v127, v73, v74\n"
    "v_max3_u32 v127, v127, v75, v88\n"
    "v_mfma_f32_16x16x32_bf16 v[196:199], a[184:187], v[52:55], v[196:199]\n"
    "v_max3_u32 v127, v127, v89, v90\n"
    "v_max3_u32 v127, v127, v91, v104\n"
    "v_mfma_f32_16x16x32_bf16 v[200:203], a[168:171], v[36:39], v[200:203]\n"
    "buffer_load_dwordx4 v[116:119], v10, s[24:27], s48 offen offset:1024 sc1\n"
    "v_mfma_f32_16x16x32_bf16 v[200:203], a[188:191], v[52:55], v[200:203]\n"
    "s_barrier\n"
    "v_mfma_f32_16x16x32_bf16 v[204:207], a[172:175], v[36:39], v[204:207]\n"
    "s_xor_b32 s50, s75, 0\n"
    "v_max3_u32 v127, v127, v105, v106\n"
    "v_max3_u32 v127, v127, v107, v107\n"
    "v_mfma_f32_16x16x32_bf16 v[204:207], a[192:195], v[52:55], v[204:207]\n"
    "s_mul_i32 s50, s50, 0x3c00\n"
    "v_cmp_eq_u32 vcc, -1, v127\n"
    "v_mfma_f32_16x16x32_bf16 v[208:211], a[176:179], v[36:39], v[208:211]\n"
    "s_add_u32 s50, s50, 0\n"
    "v_mfma_f32_16x16x32_bf16 v[208:211], a[196:199], v[52:55], v[208:211]\n"
    "s_cbranch_vccnz Lrty103\n"
    "Lok104:\n"
    "v_mfma_f32_16x16x32_bf16 v[132:135], a[200:203], v[56:59], v[132:135]\n"
    "v_add_u32 v20, s50, v1\n"
    "v_mfma_f32_16x16x32_bf16 v[132:135], a[220:223], v[72:75], v[132:135]\n"
    "s_xor_b32 s50, s75, 1\n"
    "v_mfma_f32_16x16x32_bf16 v[132:135], a[240:243], v[88:91], v[132:135]\n"
    "s_mul_i32 s50, s50, 0x3c00\n"
    "v_mfma_f32_16x16x32_bf16 v[132:135], v[236:239], v[104:107], v[132:135]\n"
    "s_add_u32 s50, s50, 0x1400\n"
    "v_mfma_f32_16x16x32_bf16 v[136:139], a[204:207], v[56:59], v[136:139]\n"
    "v_add_u32 v21, s50, v1\n"
    "v_mfma_f32_16x16x32_bf16 v[136:139], a[224:227], v[72:75], v[136:139]\n"
    "s_xor_b32 s50, s75, 2\n"
    "v_mfma_f32_16x16x32_bf16 v[136:139], a[244:247], v[88:91], v[136:139]\n"
    "s_waitcnt vmcnt(8)\n"
    "v_max3_u32 v127, v60, v61, v62\n"
    "v_max3_u32 v127, v127, v63, v76\n"
    "v_mfma_f32_16x16x32_bf16 v[136:139], v[240:243], v[104:107], v[136:139]\n"
    "s_mul_i32 s50, s50, 0x3c00\n"
    "v_max3_u32 v127, v127, v77, v78\n"
    "v_max3_u32 v127, v127, v79, v92\n"
    "v_mfma_f32_16x16x32_bf16 v[140:143], a[208:211], v[56:59], v[140:143]\n"
    "s_add_u32 s50, s50, 0x2800\n"
    "v_max3_u32 v127, v127, v93, v94\n"
    "v_max3_u32 v127, v127, v95, v108\n"
    "v_mfma_f32_16x16x32_bf16 v[140:143], a[228:231], v[72:75], v[140:143]\n"
    "v_add_u32 v22, s50, v1\n"
    "v_max3_u32 v127, v127, v109, v110\n"
    "v_mfma_f32_16x16x32_bf16 v[140:143], a[248:251], v[88:91], v[140:143]\n"
    "v_max3_u32 v127, v127, v111, v111\n"
    "v_cmp_eq_u32 vcc, -1, v127\n"
    "v_mfma_f32_16x16x32_bf16 v[140:143], v[244:247], v[104:107], v[140:143]\n"
    "v_mfma_f32_16x16x32_bf16 v[144:147], a[212:215], v[56:59], v[144:147]\n"
    "s_cbranch_vccnz Lrty105\n"
    "Lok106:\n"
    "v_mfma_f32_16x16x32_bf16 v[144:147], a[232:235], v[72:75], v[144:147]\n"
    "v_mfma_f32_16x16x32_bf16 v[144:147], a[252:255], v[88:91], v[144:147]\n"
    "v_mfma_f32_16x16x32_bf16 v[144:147], v[248:251], v[104:107], v[144:147]\n"
    "v_mfma_f32_16x16x32_bf16 v[148:151], a[216:219], v[56:59], v[148:151]\n"
    "v_mfma_f32_16x16x32_bf16 v[148:151], a[236:239], v[72:75], v[148:151]\n"
    "v_mfma_f32_16x16x32_bf16 v[148:151], v[232:235], v[88:91], v[148:151]\n"
    "v_mfma_f32_16x16x32_bf16 v[148:151], v[252:255], v[104:107], v[148:151]\n"
    "v_mfma_f32_16x16x32_bf16 v[152:155], a[200:203], v[60:63], v[152:155]\n"
    "v_mfma_f32_16x16x32_bf16 v[152:155], a[220:223], v[76:79], v[152:155]\n"
    "ds_write_b128 v20, v[132:135] offset:0\n"
    "v_mfma_f32_16x16x32_bf16 v[152:155], a[240:243], v[92:95], v[152:155]\n"
    "v_mfma_f32_16x16x32_bf16 v[152:155], v[236:239], v[108:111], v[152:155]\n"
    "v_mfma_f32_16x16x32_bf16 v[156:159], a[204:207], v[60:63], v[156:159]\n"
    "v_mfma_f32_16x16x32_bf16 v[156:159], a[224:227], v[76:79], v[156:159]\n"
    "ds_write_b128 v20, v[136:139] offset:1024\n"
    "v_mfma_f32_16x16x32_bf16 v[156:159], a[244:247], v[92:95], v[156:159]\n"
    "s_waitcnt vmcnt(4)\n"
    "v_max3_u32 v127, v64, v65, v66\n"
    "v_max3_u32 v127, v127, v67, v80\n"
    "v_mfma_f32_16x16x32_bf16 v[156:159], v[240:243], v[108:111], v[156:159]\n"
    "v_max3_u32 v127, v127, v81, v82\n"
    "v_max3_u32 v127, v127, v83, v96\n"
    "v_mfma_f32_16x16x32_bf16 v[160:163], a[208:211], v[60:63], v[160:163]\n"
    "v_max3_u32 v127, v127, v97, v98\n"
    "v_max3_u32 v127, v127, v99, v112\n"
    "v_mfma_f32_16x16x32_bf16 v[160:163], a[228:231], v[76:79], v[160:163]\n"
    "ds_write_b128 v20, v[140:143] offset:2048\n"
    "v_mfma_f32_16x16x32_bf16 v[160:163], a[248:251], v[92:95], v[160:163]\n"
    "v_max3_u32 v127, v127, v113, v114\n"
    "v_max3_u32 v127, v127, v115, v115\n"
    "v_mfma_f32_16x16x32_bf16 v[160:163], v[244:247], v[108:111], v[160:163]\n"
    "v_cmp_eq_u32 vcc, -1, v127\n"
    "v_mfma_f32_16x16x32_bf16 v[164:167], a[212:215], v[60:63], v[164:167]\n"
    "v_mfma_f32_16x16x32_bf16 v[164:167], a[232:235], v[76:79], v[164:167]\n"
    "ds_write_b128 v20, v[144:147] offset:3072\n"
    "v_mfma_f32_16x16x32_bf16 v[164:167], a[252:255], v[92:95], v[164:167]\n"
    "s_cbranch_vccnz Lrty107\n"
    "Lok108:\n"
    "v_mfma_f32_16x16x32_bf16 v[164:167], v[248:251], v[108:111], v[164:167]\n"
    "v_mfma_f32_16x16x32_bf16 v[168:171], a[216:219], v[60:63], v[168:171]\n"
    "v_mfma_f32_16x16x32_bf16 v[168:171], a[236:239], v[76:79], v[168:171]\n"
    "ds_write_b128 v20, v[148:151] offset:4096\n"
    "v_mfma_f32_16x16x32_bf16 v[168:171], v[232:235], v[92:95], v[168:171]\n"
    "v_mfma_f32_16x16x32_bf16 v[168:171], v[252:255], v[108:111], v[168:171]\n"
    "v_mfma_f32_16x16x32_bf16 v[172:175], a[200:203], v[64:67], v[172:175]\n"
    "v_mfma_f32_16x16x32_bf16 v[172:175], a[220:223], v[80:83], v[172:175]\n"
    "ds_write_b128 v21, v[152:155] offset:0\n"
    "v_mfma_f32_16x16x32_bf16 v[172:175], a[240:243], v[96:99], v[172:175]\n"
    "v_mfma_f32_16x16x32_bf16 v[172:175], v[236:239], v[112:115], v[172:175]\n"
    "v_mfma_f32_16x16x32_bf16 v[176:179], a[204:207], v[64:67], v[176:179]\n"
    "v_mfma_f32_16x16x32_bf16 v[176:179], a[224:227], v[80:83], v[176:179]\n"
    "ds_write_b128 v21, v[156:159] offset:1024\n"
    "v_mfma_f32_16x16x32_bf16 v[176:179], a[244:247], v[96:99], v[176:179]\n"
    "s_waitcnt vmcnt(0)\n"
    "v_max3_u32 v127, v68, v69, v70\n"
    "v_max3_u32 v127, v127, v71, v84\n"
    "v_mfma_f32_16x16x32_bf16 v[176:179], v[240:243], v[112:115], v[176:179]\n"
    "v_max3_u32 v127, v127, v85, v86\n"
    "v_max3_u32 v127, v127, v87, v100\n"
    "v_mfma_f32_16x16x32_bf16 v[180:183], a[208:211], v[64:67], v[180:183]\n"
    "v_max3_u32 v127, v127, v101, v102\n"
    "v_max3_u32 v127, v127, v103, v116\n"
    "v_mfma_f32_16x16x32_bf16 v[180:183], a[228:231], v[80:83], v[180:183]\n"
    "ds_write_b128 v21, v[160:163] offset:2048\n"
    "v_mfma_f32_16x16x32_bf16 v[180:183], a[248:251], v[96:99], v[180:183]\n"
    "v_max3_u32 v127, v127, v117, v118\n"
    "v_max3_u32 v127, v127, v119, v119\n"
    "v_mfma_f32_16x16x32_bf16 v[180:183], v[244:247], v[112:115], v[180:183]\n"
    "v_cmp_eq_u32 vcc, -1, v127\n"
    "v_mfma_f32_16x16x32_bf16 v[184:187], a[212:215], v[64:67], v[184:187]\n"
    "v_mfma_f32_16x16x32_bf16 v[184:187], a[232:235], v[80:83], v[184:187]\n"
    "ds_write_b128 v21, v[164:167] offset:3072\n"
    "v_mfma_f32_16x16x32_bf16 v[184:187], a[252:255], v[96:99], v[184:187]\n"
    "s_cbranch_vccnz Lrty109\n"
    "Lok110:\n"
    "v_mfma_f32_16x16x32_bf16 v[184:187], v[248:251], v[112:115], v[184:187]\n"
    "v_mfma_f32_16x16x32_bf16 v[188:191], a[216:219], v[64:67], v[188:191]\n"
    "v_mfma_f32_16x16x32_bf16 v[188:191], a[236:239], v[80:83], v[188:191]\n"
    "ds_write_b128 v21, v[168:171] offset:4096\n"
    "v_mfma_f32_16x16x32_bf16 v[188:191], v[232:235], v[96:99], v[188:191]\n"
    "v_mfma_f32_16x16x32_bf16 v[188:191], v[252:255], v[112:115], v[188:191]\n"
    "s_waitcnt lgkmcnt(0)\n"
    "s_barrier\n"
    "v_mfma_f32_16x16x32_bf16 v[192:195], a[200:203], v[68:71], v[192:195]\n"
    "ds_read_b128 v[132:135], v16 offset:0\n"
    "ds_read_b128 v[136:139], v16 offset:5120\n"
    "v_mfma_f32_16x16x32_bf16 v[192:195], a[220:223], v[84:87], v[192:195]\n"
    "ds_read_b128 v[140:143], v16 offset:1024\n"
    "ds_read_b128 v[144:147], v16 offset:6144\n"
    "v_mfma_f32_16x16x32_bf16 v[192:195], a[240:243], v[100:103], v[192:195]\n"
    "ds_read_b128 v[148:151], v16 offset:2048\n"
    "ds_read_b128 v[152:155], v16 offset:7168\n"
    "v_mfma_f32_16x16x32_bf16 v[192:195], v[236:239], v[116:119], v[192:195]\n"
    "ds_read_b128 v[156:159], v16 offset:3072\n"
    "ds_read_b128 v[160:163], v16 offset:8192\n"
    "v_mfma_f32_16x16x32_bf16 v[196:199], a[204:207], v[68:71], v[196:199]\n"
    "ds_read_b128 v[164:167], v16 offset:4096\n"
    "ds_read_b128 v[168:171], v16 offset:9216\n"
    "v_mfma_f32_16x16x32_bf16 v[196:199], a[224:227], v[84:87], v[196:199]\n"
    "ds_write_b128 v22, v[172:175] offset:0\n"
    "v_mfma_f32_16x16x32_bf16 v[196:199], a[244:247], v[100:103], v[196:199]\n"
    "s_waitcnt lgkmcnt(9)\n"
    "v_fmac_f32 v212, v124, v132\n"
    "v_fmac_f32 v213, v124, v133\n"
    "v_mfma_f32_16x16x32_bf16 v[196:199], v[240:243], v[116:119], v[196:199]\n"
    "ds_write_b128 v22, v[176:179] offset:1024\n"
    "v_mfma_f32_16x16x32_bf16 v[200:203], a[208:211], v[68:71], v[200:203]\n"
    "v_fmac_f32 v214, v124, v134\n"
    "v_fmac_f32 v215, v124, v135\n"
    "v_mfma_f32_16x16x32_bf16 v[200:203], a[228:231], v[84:87], v[200:203]\n"
    "ds_write_b128 v22, v[180:183] offset:2048\n"
    "v_mfma_f32_16x16x32_bf16 v[200:203], a[248:251], v[100:103], v[200:203]\n"
    "v_fmac_f32 v212, v124, v136\n"
    "v_fmac_f32 v213, v124, v137\n"
    "v_mfma_f32_16x16x32_bf16 v[200:203], v[244:247], v[116:119], v[200:203]\n"
    "ds_write_b128 v22, v[184:187] offset:3072\n"
    "v_mfma_f32_16x16x32_bf16 v[204:207], a[212:215], v[68:71], v[204:207]\n"
    "v_fmac_f32 v214, v124, v138\n"
    "v_fmac_f32 v215, v124, v139\n"
    "v_mfma_f32_16x16x32_bf16 v[204:207], a[232:235], v[84:87], v[204:207]\n"
    "ds_write_b128 v22, v[188:191] offset:4096\n"
    "v_mfma_f32_16x16x32_bf16 v[204:207], a[252:255], v[100:103], v[204:207]\n"
    "s_waitcnt lgkmcnt(11)\n"
    "v_fmac_f32 v216, v124, v140\n"
    "v_fmac_f32 v217, v124, v141\n"
    "v_mfma_f32_16x16x32_bf16 v[204:207], v[248:251], v[116:119], v[204:207]\n"
    "v_fmac_f32 v218, v124, v142\n"
    "v_fmac_f32 v219, v124, v143\n"
    "v_mfma_f32_16x16x32_bf16 v[208:211], a[216:219], v[68:71], v[208:211]\n"
    "v_fmac_f32 v216, v124, v144\n"
    "v_fmac_f32 v217, v124, v145\n"
    "v_mfma_f32_16x16x32_bf16 v[208:211], a[236:239], v[84:87], v[208:211]\n"
    "v_fmac_f32 v218, v124, v146\n"
    "v_fmac_f32 v219, v124, v147\n"
    "v_mfma_f32_16x16x32_bf16 v[208:211], v[232:235], v[100:103], v[208:211]\n"
    "s_waitcnt lgkmcnt(9)\n"
    "v_fmac_f32 v220, v124, v148\n"
    "v_fmac_f32 v221, v124, v149\n"
    "v_mfma_f32_16x16x32_bf16 v[208:211], v[252:255], v[116:119], v[208:211]\n"
    "v_fmac_f32 v222, v124, v150\n"
    "v_fmac_f32 v223, v124, v151\n"
    "s_waitcnt lgkmcnt(0)\n"
    "s_barrier\n"
    "ds_read_b128 v[24:27], v16 offset:10240\n"
    "ds_read_b128 v[28:31], v16 offset:11264\n"
    "ds_read_b128 v[32:35], v16 offset:12288\n"
    "ds_read_b128 v[36:39], v16 offset:13312\n"
    "ds_read_b128 v[40:43], v16 offset:14336\n"
    "v_fmac_f32 v220, v124, v152\n"
    "v_fmac_f32 v221, v124, v153\n"
    "v_fmac_f32 v222, v124, v154\n"
    "v_fmac_f32 v223, v124, v155\n"
    "v_fmac_f32 v224, v124, v156\n"
    "v_fmac_f32 v225, v124, v157\n"
    "v_fmac_f32 v226, v124, v158\n"
    "v_fmac_f32 v227, v124, v159\n"
    "v_fmac_f32 v224, v124, v160\n"
    "v_fmac_f32 v225, v124, v161\n"
    "v_fmac_f32 v226, v124, v162\n"
    "v_fmac_f32 v227, v124, v163\n"
    "v_fmac_f32 v228, v124, v164\n"
    "v_fmac_f32 v229, v124, v165\n"
    "v_fmac_f32 v230, v124, v166\n"
    "v_fmac_f32 v231, v124, v167\n"
    "v_fmac_f32 v228, v124, v168\n"
    "v_fmac_f32 v229, v124, v169\n"
    "v_fmac_f32 v230, v124, v170\n"
    "v_fmac_f32 v231, v124, v171\n"
    "v_pk_fma_f32 v[212:213], v[192:193], v[124:125], v[212:213] op_sel_hi:[1,0,1]\n"
    "v_pk_fma_f32 v[214:215], v[194:195], v[124:125], v[214:215] op_sel_hi:[1,0,1]\n"
    "v_pk_fma_f32 v[216:217], v[196:197], v[124:125], v[216:217] op_sel_hi:[1,0,1]\n"
    "v_pk_fma_f32 v[218:219], v[198:199], v[124:125], v[218:219] op_sel_hi:[1,0,1]\n"
    "v_pk_fma_f32 v[220:221], v[200:201], v[124:125], v[220:221] op_sel_hi:[1,0,1]\n"
    "v_pk_fma_f32 v[222:223], v[202:203], v[124:125], v[222:223] op_sel_hi:[1,0,1]\n"
    "v_pk_fma_f32 v[224:225], v[204:205], v[124:125], v[224:225] op_sel_hi:[1,0,1]\n"
    "v_pk_fma_f32 v[226:227], v[206:207], v[124:125], v[226:227] op_sel_hi:[1,0,1]\n"
    "v_pk_fma_f32 v[228:229], v[208:209], v[124:125], v[228:229] op_sel_hi:[1,0,1]\n"
    "v_pk_fma_f32 v[230:231], v[210:211], v[124:125], v[230:231] op_sel_hi:[1,0,1]\n"
    "s_waitcnt lgkmcnt(4)\n"
    "v_pk_fma_f32 v[212:213], v[24:25], v[124:125], v[212:213] op_sel_hi:[1,0,1]\n"
    "Lhop10:\n"
    "s_cbranch_execz LhopB\n"
    "v_pk_fma_f32 v[214:215], v[26:27], v[124:125], v[214:215] op_sel_hi:[1,0,1]\n"
    "v_cvt_pk_bf16_f32 v44, v212, v213\n"
    "v_cvt_pk_bf16_f32 v45, v214, v215\n"
    "s_waitcnt lgkmcnt(3)\n"
    "v_pk_fma_f32 v[216:217], v[28:29], v[124:125], v[216:217] op_sel_hi:[1,0,1]\n"
    "v_pk_fma_f32 v[218:219], v[30:31], v[124:125], v[218:219] op_sel_hi:[1,0,1]\n"
    "v_cvt_pk_bf16_f32 v46, v216, v217\n"
    "v_cvt_pk_bf16_f32 v47, v218, v219\n"
    "ds_write_b128 v17, v[44:47] offset:0\n"
    "s_waitcnt lgkmcnt(3)\n"
    "v_pk_fma_f32 v[220:221], v[32:33], v[124:125], v[220:221] op_sel_hi:[1,0,1]\n"
    "v_pk_fma_f32 v[222:223], v[34:35], v[124:125], v[222:223] op_sel_hi:[1,0,1]\n"
    "v_cvt_pk_bf16_f32 v48, v220, v221\n"
    "v_cvt_pk_bf16_f32 v49, v222, v223\n"
    "ds_read_b128 v[44:47], v19 offset:0\n"
    "s_waitcnt lgkmcnt(3)\n"
    "v_pk_fma_f32 v[224:225], v[36:37], v[124:125], v[224:225] op_sel_hi:[1,0,1]\n"
    "v_pk_fma_f32 v[226:227], v[38:39], v[124:125], v[226:227] op_sel_hi:[1,0,1]\n"
    "v_cvt_pk_bf16_f32 v50, v224, v225\n"
    "v_cvt_pk_bf16_f32 v51, v226, v227\n"
    "ds_write_b128 v17, v[48:51] offset:1024\n"
    "s_waitcnt lgkmcnt(3)\n"
    "v_pk_fma_f32 v[228:229], v[40:41], v[124:125], v[228:229] op_sel_hi:[1,0,1]\n"
    "v_pk_fma_f32 v[230:231], v[42:43], v[124:125], v[230:231] op_sel_hi:[1,0,1]\n"
    "v_cvt_pk_bf16_f32 v52, v228, v229\n"
    "v_cvt_pk_bf16_f32 v53, v230, v231\n"
    "ds_write_b64 v18, v[52:53] offset:2048\n"
    "ds_read_b128 v[48:51], v19 offset:1024\n"
    "ds_read_b64 v[52:53], v23 offset:2048\n"
    "s_waitcnt lgkmcnt(4)\n"
    "buffer_store_dwordx4 v[44:47], v11, s[28:31], s80 offen \n"
    "s_waitcnt lgkmcnt(1)\n"
    "buffer_store_dwordx4 v[48:51], v11, s[28:31], s80 offen offset:64 \n"
    "s_waitcnt lgkmcnt(0)\n"
    "buffer_store_dwordx2 v[52:53], v12, s[28:31], s81 offen \n"
    "s_endpgm\n"
    "Lxsp5:\n"
    "s_cmp_eq_u32 s43, 0\n"
    "s_cbranch_scc1 Lxok6\n"
    "s_sub_u32 s43, s43, 1\n"
    "buffer_load_dwordx4 v[112:115], v117, s[24:27], s55 offen sc1\n"
    "s_waitcnt vmcnt(0)\n"
    "v_cmp_ne_u32 vcc, s6, v113\n"
    "v_cmp_ne_u32 s[16:17], s6, v115\n"
    "s_nop 0\n"
    "s_or_b64 vcc, vcc, s[16:17]\n"
    "s_cbranch_vccnz Lxsp5\n"
    "s_branch Lxok6\n"
    "Lspin7:\n"
    "s_cmp_eq_u32 s43, 0\n"
    "s_cbranch_scc1 Lseen8\n"
    "s_sub_u32 s43, s43, 1\n"
    "v_lshlrev_b32 v23, 3, v1\n"
    "buffer_load_dwordx4 v[104:107], v23, s[24:27], s85 offen offset:0 sc1\n"
    "s_waitcnt vmcnt(0)\n"
    "v_cmp_ne_u64 vcc, s[6:7], v[104:105]\n"
    "v_cmp_ne_u64 s[16:17], s[6:7], v[106:107]\n"
    "s_nop 0\n"
    "s_or_b64 vcc, vcc, s[16:17]\n"
    "s_cbranch_vccnz Lspin7\n"
    "s_branch Lseen8\n"
    "Lspin9:\n"
    "s_cmp_eq_u32 s43, 0\n"
    "s_cbranch_scc1 Lseen10\n"
    "s_sub_u32 s43, s43, 1\n"
    "s_sleep 8\n"
    "v_lshrrev_b32 v23, 1, v1\n"
    "s_lshl_b32 s59, s41, 9\n"
    "s_add_u32 s59, s59, 0x10000\n"
    "buffer_load_dwordx2 v[244:245], v23, s[24:27], s59 offen sc1\n"
    "s_waitcnt vmcnt(0)\n"
    "v_cmp_ne_u64 vcc, s[6:7], v[244:245]\n"
    "s_nop 0\n"
    "s_cbranch_vccnz Lspin9\n"
    "s_branch Lseen10\n"
    "Lqsv11:\n"
    "v_xor_b32 v190, 0xc00, v237\n"
    "v_xor_b32 v191, 0x200, v238\n"
    "ds_read_b128 v[184:187], v190\n"
    "ds_read_b64 v[188:189], v191\n"
    "s_lshl_b32 s59, s40, 2\n"
    "s_add_u32 s59, s59, s41\n"
    "s_lshl_b32 s59, s59, 11\n"
    "s_add_u32 s59, s59, 0x1500000\n"
    "s_waitcnt vmcnt(0) lgkmcnt(0)\n"
    "buffer_store_dwordx4 v[184:187], v1, s[24:27], s59 offen sc1\n"
    "buffer_store_dwordx2 v[188:189], v1, s[24:27], s59 offen offset:1024 sc1\n"
    "s_waitcnt vmcnt(0)\n"
    "s_branch Lqsr12\n"
    "Lspin13:\n"
    "s_cmp_eq_u32 s43, 0\n"
    "s_cbranch_scc1 Lseen14\n"
    "s_sub_u32 s43, s43, 1\n"
    "v_lshlrev_b32 v23, 3, v1\n"
    "v_and_b32 v23, 0x3ff, v23\n"
    "s_and_b32 s59, s84, 0xfffffc00\n"
    "buffer_load_dwordx2 v[244:245], v23, s[24:27], s59 offen offset:72 sc1\n"
    "s_waitcnt vmcnt(0)\n"
    "v_cmp_ne_u64 vcc, s[6:7], v[244:245]\n"
    "s_nop 0\n"
    "s_cbranch_vccnz Lspin13\n"
    "s_branch Lseen14\n"
    "Lfb15:\n"
    "s_nop 7\n"
    "s_bitcmp1_b32 s74, 1\n"
    "s_cbranch_scc0 Lfbs17\n"
    "v_mov_b32 v136, 0\n"
    "v_mov_b32 v137, 0\n"
    "v_mov_b32 v138, 0\n"
    "v_mov_b32 v139, 0\n"
    "v_mov_b32 v140, 0\n"
    "v_mov_b32 v141, 0\n"
    "v_mov_b32 v142, 0\n"
    "v_mov_b32 v143, 0\n"
    "v_mov_b32 v144, 0\n"
    "v_mov_b32 v145, 0\n"
    "v_mov_b32 v146, 0\n"
    "v_mov_b32 v147, 0\n"
    "v_mov_b32 v148, 0\n"
    "v_mov_b32 v149, 0\n"
    "v_mov_b32 v150, 0\n"
    "v_mov_b32 v151, 0\n"
    "v_mov_b32 v20, 0xf149f2ca\n"
    "s_mov_b32 s52, 0x20000\n"
    "Lfbl18:\n"
    "s_add_u32 s53, s52, 0x1000\n"
    "buffer_load_dwordx4 v[56:59], v1, s[24:27], s52 offen offset:0 sc1\n"
    "buffer_load_dwordx4 v[60:63], v1, s[24:27], s52 offen offset:1024 sc1\n"
    "buffer_load_dwordx4 v[64:67], v1, s[24:27], s52 offen offset:2048 sc1\n"
    "buffer_load_dwordx4 v[68:71], v1, s[24:27], s52 offen offset:3072 sc1\n"
    "buffer_load_dwordx4 v[80:83], v13, s[24:27], s53 offen offset:2048 sc1\n"
    "buffer_load_dwordx4 v[72:75], v1, s[24:27], s53 offen offset:0 sc1\n"
    "buffer_load_dwordx4 v[76:79], v1, s[24:27], s53 offen offset:1024 sc1\n"
    "s_waitcnt vmcnt(0)\n"
    "v_mfma_f32_32x32x4_2b_bf16 v[24:55], v[80:81], v[170:171], 0\n"
    "s_nop 15\n"
    "s_nop 1\n"
    "v_mfma_f32_32x32x16_bf16 v[40:55], v[56:59], v[164:167], v[40:55]\n"
    "s_nop 11\n"
    "v_max3_f32 v21, v40, v41, v20\n"
    "v_max3_f32 v21, v21, v42, v43\n"
    "v_max3_f32 v21, v21, v44, v45\n"
    "v_max3_f32 v21, v21, v46, v47\n"
    "v_max3_f32 v21, v21, v48, v49\n"
    "v_max3_f32 v21, v21, v50, v51\n"
    "v_max3_f32 v21, v21, v52, v53\n"
    "v_max3_f32 v21, v21, v54, v55\n"
    "v_mov_b32 v22, v21\n"
    "s_nop 1\n"
    "v_permlane32_swap_b32 v22, v21\n"
    "v_max_f32 v22, v22, v21\n"
    "v_sub_f32 v21, v20, v22\n"
    "v_exp_f32 v21, v21\n"
    "v_mov_b32 v20, v22\n"
    "v_sub_f32 v40, v40, v20\n"
    "v_exp_f32 v40, v40\n"
    "v_sub_f32 v41, v41, v20\n"
    "v_exp_f32 v41, v41\n"
    "v_sub_f32 v42, v42, v20\n"
    "v_exp_f32 v42, v42\n"
    "v_sub_f32 v43, v43, v20\n"
    "v_exp_f32 v43, v43\n"
    "v_sub_f32 v44, v44, v20\n"
    "v_exp_f32 v44, v44\n"
    "v_sub_f32 v45, v45, v20\n"
    "v_exp_f32 v45, v45\n"
    "v_sub_f32 v46, v46, v20\n"
    "v_exp_f32 v46, v46\n"
    "v_sub_f32 v47, v47, v20\n"
    "v_exp_f32 v47, v47\n"
    "v_sub_f32 v48, v48, v20\n"
    "v_exp_f32 v48, v48\n"
    "v_sub_f32 v49, v49, v20\n"
    "v_exp_f32 v49, v49\n"
    "v_sub_f32 v50, v50, v20\n"
    "v_exp_f32 v50, v50\n"
    "v_sub_f32 v51, v51, v20\n"
    "v_exp_f32 v51, v51\n"
    "v_sub_f32 v52, v52, v20\n"
    "v_exp_f32 v52, v52\n"
    "v_sub_f32 v53, v53, v20\n"
    "v_exp_f32 v53, v53\n"
    "v_sub_f32 v54, v54, v20\n"
    "v_exp_f32 v54, v54\n"
    "v_sub_f32 v55, v55, v20\n"
    "v_exp_f32 v55, v55\n"
    "v_mul_f32 v136, v136, v21\n"
    "v_mul_f32 v137, v137, v21\n"
    "v_mul_f32 v138, v138, v21\n"
    "v_mul_f32 v139, v139, v21\n"
    "v_mul_f32 v140, v140, v21\n"
    "v_mul_f32 v141, v141, v21\n"
    "v_mul_f32 v142, v142, v21\n"
    "v_mul_f32 v143, v143, v21\n"
    "v_mul_f32 v144, v144, v21\n"
    "v_mul_f32 v145, v145, v21\n"
    "v_mul_f32 v146, v146, v21\n"
    "v_mul_f32 v147, v147, v21\n"
    "s_nop 1\n"
    "v_smfmac_f32_32x32x32_bf16 v[136:151], v[64:67], v[40:47], v2\n"
    "v_smfmac_f32_32x32x32_bf16 v[136:151], v[68:71], v[48:55], v2\n"
    "v_mfma_f32_32x32x4_2b_bf16 v[24:55], v[82:83], v[170:171], 0\n"
    "s_nop 15\n"
    "s_nop 1\n"
    "v_mfma_f32_32x32x16_bf16 v[40:55], v[60:63], v[164:167], v[40:55]\n"
    "s_nop 11\n"
    "v_max3_f32 v21, v40, v41, v20\n"
    "v_max3_f32 v21, v21, v42, v43\n"
    "v_max3_f32 v21, v21, v44, v45\n"
    "v_max3_f32 v21, v21, v46, v47\n"
    "v_max3_f32 v21, v21, v48, v49\n"
    "v_max3_f32 v21, v21, v50, v51\n"
    "v_max3_f32 v21, v21, v52, v53\n"
    "v_max3_f32 v21, v21, v54, v55\n"
    "v_mov_b32 v22, v21\n"
    "s_nop 1\n"
    "v_permlane32_swap_b32 v22, v21\n"
    "v_max_f32 v22, v22, v21\n"
    "v_sub_f32 v21, v20, v22\n"
    "v_exp_f32 v21, v21\n"
    "v_mov_b32 v20, v22\n"
    "v_sub_f32 v40, v40, v20\n"
    "v_exp_f32 v40, v40\n"
    "v_sub_f32 v41, v41, v20\n"
    "v_exp_f32 v41, v41\n"
    "v_sub_f32 v42, v42, v20\n"
    "v_exp_f32 v42, v42\n"
    "v_sub_f32 v43, v43, v20\n"
    "v_exp_f32 v43, v43\n"
    "v_sub_f32 v44, v44, v20\n"
    "v_exp_f32 v44, v44\n"
    "v_sub_f32 v45, v45, v20\n"
    "v_exp_f32 v45, v45\n"
    "v_sub_f32 v46, v46, v20\n"
    "v_exp_f32 v46, v46\n"
    "v_sub_f32 v47, v47, v20\n"
    "v_exp_f32 v47, v47\n"
    "v_sub_f32 v48, v48, v20\n"
    "v_exp_f32 v48, v48\n"
    "v_sub_f32 v49, v49, v20\n"
    "v_exp_f32 v49, v49\n"
    "v_sub_f32 v50, v50, v20\n"
    "v_exp_f32 v50, v50\n"
    "v_sub_f32 v51, v51, v20\n"
    "v_exp_f32 v51, v51\n"
    "v_sub_f32 v52, v52, v20\n"
    "v_exp_f32 v52, v52\n"
    "v_sub_f32 v53, v53, v20\n"
    "v_exp_f32 v53, v53\n"
    "v_sub_f32 v54, v54, v20\n"
    "v_exp_f32 v54, v54\n"
    "v_sub_f32 v55, v55, v20\n"
    "v_exp_f32 v55, v55\n"
    "v_mul_f32 v136, v136, v21\n"
    "v_mul_f32 v137, v137, v21\n"
    "v_mul_f32 v138, v138, v21\n"
    "v_mul_f32 v139, v139, v21\n"
    "v_mul_f32 v140, v140, v21\n"
    "v_mul_f32 v141, v141, v21\n"
    "v_mul_f32 v142, v142, v21\n"
    "v_mul_f32 v143, v143, v21\n"
    "v_mul_f32 v144, v144, v21\n"
    "v_mul_f32 v145, v145, v21\n"
    "v_mul_f32 v146, v146, v21\n"
    "v_mul_f32 v147, v147, v21\n"
    "s_nop 1\n"
    "v_smfmac_f32_32x32x32_bf16 v[136:151], v[72:75], v[40:47], v2\n"
    "v_smfmac_f32_32x32x32_bf16 v[136:151], v[76:79], v[48:55], v2\n"
    "s_nop 15\n"
    "s_add_u32 s52, s52, 0x1a00\n"
    "s_cmp_lt_u32 s52, 0x54000\n"
    "s_cbranch_scc1 Lfbl18\n"
    "v_mov_b32 v16, v144\n"
    "v_mov_b32 v17, v144\n"
    "s_add_u32 s50, s61, 0x2800\n"
    "s_add_u32 s49, s62, 0x2800\n"
    "v_permlane32_swap_b32 v17, v16\n"
    "v_rcp_f32 v16, v16\n"
    "s_nop 0\n"
    "v_mul_f32 v136, v136, v16\n"
    "v_mul_f32 v137, v137, v16\n"
    "v_mul_f32 v138, v138, v16\n"
    "v_mul_f32 v139, v139, v16\n"
    "v_mul_f32 v140, v140, v16\n"
    "v_mul_f32 v141, v141, v16\n"
    "v_mul_f32 v142, v142, v16\n"
    "v_mul_f32 v143, v143, v16\n"
    "v_mul_f32 v144, v144, v16\n"
    "v_mul_f32 v145, v145, v16\n"
    "v_mul_f32 v146, v146, v16\n"
    "v_mul_f32 v147, v147, v16\n"
    "v_cvt_pk_bf16_f32 v136, v136, v137\n"
    "v_cvt_pk_bf16_f32 v137, v138, v139\n"
    "v_cvt_pk_bf16_f32 v138, v140, v141\n"
    "v_cvt_pk_bf16_f32 v139, v142, v143\n"
    "v_cvt_pk_bf16_f32 v140, v144, v145\n"
    "v_cvt_pk_bf16_f32 v141, v146, v147\n"
    "buffer_store_dwordx4 v[136:139], v5, s[24:27], s50 offen sc1\n"
    "buffer_store_dwordx2 v[140:141], v6, s[24:27], s49 offen sc1\n"
    "s_waitcnt vmcnt(0)\n"
    "Lfbs17:\n"
    "s_bitcmp1_b32 s74, 0\n"
    "s_cbranch_scc0 Lfbs19\n"
    "s_lshl_b32 s59, s40, 2\n"
    "s_add_u32 s59, s59, s41\n"
    "s_lshl_b32 s59, s59, 11\n"
    "s_add_u32 s59, s59, 0x1500000\n"
    "buffer_load_dwordx4 v[164:167], v1, s[24:27], s59 offen sc1\n"
    "buffer_load_dwordx2 v[170:171], v1, s[24:27], s59 offen offset:1024 sc1\n"
    "v_mov_b32 v136, 0\n"
    "v_mov_b32 v137, 0\n"
    "v_mov_b32 v138, 0\n"
    "v_mov_b32 v139, 0\n"
    "v_mov_b32 v140, 0\n"
    "v_mov_b32 v141, 0\n"
    "v_mov_b32 v142, 0\n"
    "v_mov_b32 v143, 0\n"
    "v_mov_b32 v144, 0\n"
    "v_mov_b32 v145, 0\n"
    "v_mov_b32 v146, 0\n"
    "v_mov_b32 v147, 0\n"
    "v_mov_b32 v148, 0\n"
    "v_mov_b32 v149, 0\n"
    "v_mov_b32 v150, 0\n"
    "v_mov_b32 v151, 0\n"
    "v_mov_b32 v20, 0xf149f2ca\n"
    "s_mov_b32 s52, 0x20000\n"
    "s_waitcnt vmcnt(0)\n"
    "Lfbl20:\n"
    "s_add_u32 s53, s52, 0x1000\n"
    "buffer_load_dwordx4 v[56:59], v1, s[24:27], s52 offen offset:0 sc1\n"
    "buffer_load_dwordx4 v[60:63], v1, s[24:27], s52 offen offset:1024 sc1\n"
    "buffer_load_dwordx4 v[64:67], v1, s[24:27], s52 offen offset:2048 sc1\n"
    "buffer_load_dwordx4 v[68:71], v1, s[24:27], s52 offen offset:3072 sc1\n"
    "buffer_load_dwordx4 v[80:83], v13, s[24:27], s53 offen offset:2048 sc1\n"
    "buffer_load_dwordx4 v[72:75], v1, s[24:27], s53 offen offset:0 sc1\n"
    "buffer_load_dwordx4 v[76:79], v1, s[24:27], s53 offen offset:1024 sc1\n"
    "s_waitcnt vmcnt(0)\n"
    "v_mfma_f32_32x32x4_2b_bf16 v[24:55], v[80:81], v[170:171], 0\n"
    "s_nop 15\n"
    "s_nop 1\n"
    "v_mfma_f32_32x32x16_bf16 v[40:55], v[56:59], v[164:167], v[40:55]\n"
    "s_nop 11\n"
    "v_max3_f32 v21, v40, v41, v20\n"
    "v_max3_f32 v21, v21, v42, v43\n"
    "v_max3_f32 v21, v21, v44, v45\n"
    "v_max3_f32 v21, v21, v46, v47\n"
    "v_max3_f32 v21, v21, v48, v49\n"
    "v_max3_f32 v21, v21, v50, v51\n"
    "v_max3_f32 v21, v21, v52, v53\n"
    "v_max3_f32 v21, v21, v54, v55\n"
    "v_mov_b32 v22, v21\n"
    "s_nop 1\n"
    "v_permlane32_swap_b32 v22, v21\n"
    "v_max_f32 v22, v22, v21\n"
    "v_sub_f32 v21, v20, v22\n"
    "v_exp_f32 v21, v21\n"
    "v_mov_b32 v20, v22\n"
    "v_sub_f32 v40, v40, v20\n"
    "v_exp_f32 v40, v40\n"
    "v_sub_f32 v41, v41, v20\n"
    "v_exp_f32 v41, v41\n"
    "v_sub_f32 v42, v42, v20\n"
    "v_exp_f32 v42, v42\n"
    "v_sub_f32 v43, v43, v20\n"
    "v_exp_f32 v43, v43\n"
    "v_sub_f32 v44, v44, v20\n"
    "v_exp_f32 v44, v44\n"
    "v_sub_f32 v45, v45, v20\n"
    "v_exp_f32 v45, v45\n"
    "v_sub_f32 v46, v46, v20\n"
    "v_exp_f32 v46, v46\n"
    "v_sub_f32 v47, v47, v20\n"
    "v_exp_f32 v47, v47\n"
    "v_sub_f32 v48, v48, v20\n"
    "v_exp_f32 v48, v48\n"
    "v_sub_f32 v49, v49, v20\n"
    "v_exp_f32 v49, v49\n"
    "v_sub_f32 v50, v50, v20\n"
    "v_exp_f32 v50, v50\n"
    "v_sub_f32 v51, v51, v20\n"
    "v_exp_f32 v51, v51\n"
    "v_sub_f32 v52, v52, v20\n"
    "v_exp_f32 v52, v52\n"
    "v_sub_f32 v53, v53, v20\n"
    "v_exp_f32 v53, v53\n"
    "v_sub_f32 v54, v54, v20\n"
    "v_exp_f32 v54, v54\n"
    "v_sub_f32 v55, v55, v20\n"
    "v_exp_f32 v55, v55\n"
    "v_mul_f32 v136, v136, v21\n"
    "v_mul_f32 v137, v137, v21\n"
    "v_mul_f32 v138, v138, v21\n"
    "v_mul_f32 v139, v139, v21\n"
    "v_mul_f32 v140, v140, v21\n"
    "v_mul_f32 v141, v141, v21\n"
    "v_mul_f32 v142, v142, v21\n"
    "v_mul_f32 v143, v143, v21\n"
    "v_mul_f32 v144, v144, v21\n"
    "v_mul_f32 v145, v145, v21\n"
    "v_mul_f32 v146, v146, v21\n"
    "v_mul_f32 v147, v147, v21\n"
    "s_nop 1\n"
    "v_smfmac_f32_32x32x32_bf16 v[136:151], v[64:67], v[40:47], v2\n"
    "v_smfmac_f32_32x32x32_bf16 v[136:151], v[68:71], v[48:55], v2\n"
    "v_mfma_f32_32x32x4_2b_bf16 v[24:55], v[82:83], v[170:171], 0\n"
    "s_nop 15\n"
    "s_nop 1\n"
    "v_mfma_f32_32x32x16_bf16 v[40:55], v[60:63], v[164:167], v[40:55]\n"
    "s_nop 11\n"
    "v_max3_f32 v21, v40, v41, v20\n"
    "v_max3_f32 v21, v21, v42, v43\n"
    "v_max3_f32 v21, v21, v44, v45\n"
    "v_max3_f32 v21, v21, v46, v47\n"
    "v_max3_f32 v21, v21, v48, v49\n"
    "v_max3_f32 v21, v21, v50, v51\n"
    "v_max3_f32 v21, v21, v52, v53\n"
    "v_max3_f32 v21, v21, v54, v55\n"
    "v_mov_b32 v22, v21\n"
    "s_nop 1\n"
    "v_permlane32_swap_b32 v22, v21\n"
    "v_max_f32 v22, v22, v21\n"
    "v_sub_f32 v21, v20, v22\n"
    "v_exp_f32 v21, v21\n"
    "v_mov_b32 v20, v22\n"
    "v_sub_f32 v40, v40, v20\n"
    "v_exp_f32 v40, v40\n"
    "v_sub_f32 v41, v41, v20\n"
    "v_exp_f32 v41, v41\n"
    "v_sub_f32 v42, v42, v20\n"
    "v_exp_f32 v42, v42\n"
    "v_sub_f32 v43, v43, v20\n"
    "v_exp_f32 v43, v43\n"
    "v_sub_f32 v44, v44, v20\n"
    "v_exp_f32 v44, v44\n"
    "v_sub_f32 v45, v45, v20\n"
    "v_exp_f32 v45, v45\n"
    "v_sub_f32 v46, v46, v20\n"
    "v_exp_f32 v46, v46\n"
    "v_sub_f32 v47, v47, v20\n"
    "v_exp_f32 v47, v47\n"
    "v_sub_f32 v48, v48, v20\n"
    "v_exp_f32 v48, v48\n"
    "v_sub_f32 v49, v49, v20\n"
    "v_exp_f32 v49, v49\n"
    "v_sub_f32 v50, v50, v20\n"
    "v_exp_f32 v50, v50\n"
    "v_sub_f32 v51, v51, v20\n"
    "v_exp_f32 v51, v51\n"
    "v_sub_f32 v52, v52, v20\n"
    "v_exp_f32 v52, v52\n"
    "v_sub_f32 v53, v53, v20\n"
    "v_exp_f32 v53, v53\n"
    "v_sub_f32 v54, v54, v20\n"
    "v_exp_f32 v54, v54\n"
    "v_sub_f32 v55, v55, v20\n"
    "v_exp_f32 v55, v55\n"
    "v_mul_f32 v136, v136, v21\n"
    "v_mul_f32 v137, v137, v21\n"
    "v_mul_f32 v138, v138, v21\n"
    "v_mul_f32 v139, v139, v21\n"
    "v_mul_f32 v140, v140, v21\n"
    "v_mul_f32 v141, v141, v21\n"
    "v_mul_f32 v142, v142, v21\n"
    "v_mul_f32 v143, v143, v21\n"
    "v_mul_f32 v144, v144, v21\n"
    "v_mul_f32 v145, v145, v21\n"
    "v_mul_f32 v146, v146, v21\n"
    "v_mul_f32 v147, v147, v21\n"
    "s_nop 1\n"
    "v_smfmac_f32_32x32x32_bf16 v[136:151], v[72:75], v[40:47], v2\n"
    "v_smfmac_f32_32x32x32_bf16 v[136:151], v[76:79], v[48:55], v2\n"
    "s_nop 15\n"
    "s_add_u32 s52, s52, 0x1a00\n"
    "s_cmp_lt_u32 s52, 0x54000\n"
    "s_cbranch_scc1 Lfbl20\n"
    "v_mov_b32 v16, v144\n"
    "v_mov_b32 v17, v144\n"
    "s_add_u32 s50, s61, 0\n"
    "s_add_u32 s49, s62, 0\n"
    "v_permlane32_swap_b32 v17, v16\n"
    "v_rcp_f32 v16, v16\n"
    "s_nop 0\n"
    "v_mul_f32 v136, v136, v16\n"
    "v_mul_f32 v137, v137, v16\n"
    "v_mul_f32 v138, v138, v16\n"
    "v_mul_f32 v139, v139, v16\n"
    "v_mul_f32 v140, v140, v16\n"
    "v_mul_f32 v141, v141, v16\n"
    "v_mul_f32 v142, v142, v16\n"
    "v_mul_f32 v143, v143, v16\n"
    "v_mul_f32 v144, v144, v16\n"
    "v_mul_f32 v145, v145, v16\n"
    "v_mul_f32 v146, v146, v16\n"
    "v_mul_f32 v147, v147, v16\n"
    "v_cvt_pk_bf16_f32 v136, v136, v137\n"
    "v_cvt_pk_bf16_f32 v137, v138, v139\n"
    "v_cvt_pk_bf16_f32 v138, v140, v141\n"
    "v_cvt_pk_bf16_f32 v139, v142, v143\n"
    "v_cvt_pk_bf16_f32 v140, v144, v145\n"
    "v_cvt_pk_bf16_f32 v141, v146, v147\n"
    "buffer_store_dwordx4 v[136:139], v5, s[24:27], s50 offen sc1\n"
    "buffer_store_dwordx2 v[140:141], v6, s[24:27], s49 offen sc1\n"
    "s_waitcnt vmcnt(0)\n"
    "Lfbs19:\n"
    "s_branch Lfbret16\n"
    "Lrty21:\n"
    "s_cmp_eq_u32 s43, 0\n"
    "s_cbranch_scc1 Lok22\n"
    "s_sub_u32 s43, s43, 1\n"
    "s_sleep 1\n"
    "s_add_u32 s59, s63, 0x800\n"
    "buffer_load_dwordx4 v[180:183], v1, s[24:27], s59 offen sc1\n"
    "s_add_u32 s59, s63, 0xc00\n"
    "buffer_load_dwordx4 v[184:187], v1, s[24:27], s59 offen sc1\n"
    "s_waitcnt vmcnt(0)\n"
    "v_max3_u32 v16, v180, v181, v182\n"
    "v_max3_u32 v16, v16, v183, v184\n"
    "v_max3_u32 v16, v16, v185, v186\n"
    "v_max3_u32 v16, v16, v187, v187\n"
    "v_cmp_eq_u32 vcc, -1, v16\n"
    "s_nop 0\n"
    "s_cbranch_vccnz Lrty21\n"
    "s_branch Lok22\n"
    "Lrty23:\n"
    "s_cmp_eq_u32 s43, 0\n"
    "s_cbranch_scc1 Lok24\n"
    "s_sub_u32 s43, s43, 1\n"
    "s_sleep 1\n"
    "s_add_u32 s59, s63, 0x1000\n"
    "buffer_load_dwordx4 v[188:191], v1, s[24:27], s59 offen sc1\n"
    "s_add_u32 s59, s63, 0x1400\n"
    "buffer_load_dwordx4 v[192:195], v1, s[24:27], s59 offen sc1\n"
    "s_waitcnt vmcnt(0)\n"
    "v_max3_u32 v16, v188, v189, v190\n"
    "v_max3_u32 v16, v16, v191, v192\n"
    "v_max3_u32 v16, v16, v193, v194\n"
    "v_max3_u32 v16, v16, v195, v195\n"
    "v_cmp_eq_u32 vcc, -1, v16\n"
    "s_nop 0\n"
    "s_cbranch_vccnz Lrty23\n"
    "s_branch Lok24\n"
    "Lrty25:\n"
    "s_cmp_eq_u32 s43, 0\n"
    "s_cbranch_scc1 Lok26\n"
    "s_sub_u32 s43, s43, 1\n"
    "s_sleep 1\n"
    "s_add_u32 s59, s63, 0x1800\n"
    "buffer_load_dwordx4 v[196:199], v1, s[24:27], s59 offen sc1\n"
    "s_add_u32 s59, s63, 0x1c00\n"
    "buffer_load_dwordx4 v[200:203], v1, s[24:27], s59 offen sc1\n"
    "s_waitcnt vmcnt(0)\n"
    "v_max3_u32 v16, v196, v197, v198\n"
    "v_max3_u32 v16, v16, v199, v200\n"
    "v_max3_u32 v16, v16, v201, v202\n"
    "v_max3_u32 v16, v16, v203, v203\n"
    "v_cmp_eq_u32 vcc, -1, v16\n"
    "s_nop 0\n"
    "s_cbranch_vccnz Lrty25\n"
    "s_branch Lok26\n"
    "Lrty27:\n"
    "s_cmp_eq_u32 s43, 0\n"
    "s_cbranch_scc1 Lok28\n"
    "s_sub_u32 s43, s43, 1\n"
    "s_sleep 1\n"
    "s_add_u32 s59, s63, 0x2000\n"
    "buffer_load_dwordx4 v[204:207], v1, s[24:27], s59 offen sc1\n"
    "s_add_u32 s59, s63, 0x2400\n"
    "buffer_load_dwordx4 v[208:211], v1, s[24:27], s59 offen sc1\n"
    "s_waitcnt vmcnt(0)\n"
    "v_max3_u32 v16, v204, v205, v206\n"
    "v_max3_u32 v16, v16, v207, v208\n"
    "v_max3_u32 v16, v16, v209, v210\n"
    "v_max3_u32 v16, v16, v211, v211\n"
    "v_cmp_eq_u32 vcc, -1, v16\n"
    "s_nop 0\n"
    "s_cbranch_vccnz Lrty27\n"
    "s_branch Lok28\n"
    "Lrty29:\n"
    "s_cmp_eq_u32 s43, 0\n"
    "s_cbranch_scc1 Lok30\n"
    "s_sub_u32 s43, s43, 1\n"
    "s_sleep 1\n"
    "s_add_u32 s59, s63, 0x3000\n"
    "buffer_load_dwordx4 v[140:143], v1, s[24:27], s59 offen sc1\n"
    "s_add_u32 s59, s63, 0x3400\n"
    "buffer_load_dwordx4 v[144:147], v1, s[24:27], s59 offen sc1\n"
    "s_waitcnt vmcnt(0)\n"
    "v_max3_u32 v16, v140, v141, v142\n"
    "v_max3_u32 v16, v16, v143, v144\n"
    "v_max3_u32 v16, v16, v145, v146\n"
    "v_max3_u32 v16, v16, v147, v147\n"
    "v_cmp_eq_u32 vcc, -1, v16\n"
    "s_nop 0\n"
    "s_cbranch_vccnz Lrty29\n"
    "s_branch Lok30\n"
    "Lrty31:\n"
    "s_cmp_eq_u32 s43, 0\n"
    "s_cbranch_scc1 Lok32\n"
    "s_sub_u32 s43, s43, 1\n"
    "s_sleep 1\n"
    "s_add_u32 s59, s63, 0x3800\n"
    "buffer_load_dwordx4 v[148:151], v1, s[24:27], s59 offen sc1\n"
    "s_add_u32 s59, s63, 0x3c00\n"
    "buffer_load_dwordx4 v[152:155], v1, s[24:27], s59 offen sc1\n"
    "s_waitcnt vmcnt(0)\n"
    "v_max3_u32 v16, v148, v149, v150\n"
    "v_max3_u32 v16, v16, v151, v152\n"
    "v_max3_u32 v16, v16, v153, v154\n"
    "v_max3_u32 v16, v16, v155, v155\n"
    "v_cmp_eq_u32 vcc, -1, v16\n"
    "s_nop 0\n"
    "s_cbranch_vccnz Lrty31\n"
    "s_branch Lok32\n"
    "Lrty33:\n"
    "s_cmp_eq_u32 s43, 0\n"
    "s_cbranch_scc1 Lok34\n"
    "s_sub_u32 s43, s43, 1\n"
    "s_sleep 1\n"
    "s_add_u32 s59, s63, 0x4000\n"
    "buffer_load_dwordx4 v[156:159], v1, s[24:27], s59 offen sc1\n"
    "s_add_u32 s59, s63, 0x4400\n"
    "buffer_load_dwordx4 v[160:163], v1, s[24:27], s59 offen sc1\n"
    "s_waitcnt vmcnt(0)\n"
    "v_max3_u32 v16, v156, v157, v158\n"
    "v_max3_u32 v16, v16, v159, v160\n"
    "v_max3_u32 v16, v16, v161, v162\n"
    "v_max3_u32 v16, v16, v163, v163\n"
    "v_cmp_eq_u32 vcc, -1, v16\n"
    "s_nop 0\n"
    "s_cbranch_vccnz Lrty33\n"
    "s_branch Lok34\n"
    "Lrty35:\n"
    "s_cmp_eq_u32 s43, 0\n"
    "s_cbranch_scc1 Lok36\n"
    "s_sub_u32 s43, s43, 1\n"
    "s_sleep 1\n"
    "s_add_u32 s59, s63, 0x4800\n"
    "buffer_load_dwordx4 v[164:167], v1, s[24:27], s59 offen sc1\n"
    "s_add_u32 s59, s63, 0x4c00\n"
    "buffer_load_dwordx4 v[168:171], v1, s[24:27], s59 offen sc1\n"
    "s_waitcnt vmcnt(0)\n"
    "v_max3_u32 v16, v164, v165, v166\n"
    "v_max3_u32 v16, v16, v167, v168\n"
    "v_max3_u32 v16, v16, v169, v170\n"
    "v_max3_u32 v16, v16, v171, v171\n"
    "v_cmp_eq_u32 vcc, -1, v16\n"
    "s_nop 0\n"
    "s_cbranch_vccnz Lrty35\n"
    "s_branch Lok36\n"
    "Lrty37:\n"
    "s_cmp_eq_u32 s43, 0\n"
    "s_cbranch_scc1 Lok38\n"
    "s_sub_u32 s43, s43, 1\n"
    "s_sleep 1\n"
    "s_add_u32 s59, s63, 0\n"
    "buffer_load_dwordx4 v[172:175], v1, s[24:27], s59 offen sc1\n"
    "s_add_u32 s59, s63, 0x400\n"
    "buffer_load_dwordx4 v[176:179], v1, s[24:27], s59 offen sc1\n"
    "s_waitcnt vmcnt(0)\n"
    "v_max3_u32 v16, v172, v173, v174\n"
    "v_max3_u32 v16, v16, v175, v176\n"
    "v_max3_u32 v16, v16, v177, v178\n"
    "v_max3_u32 v16, v16, v179, v179\n"
    "v_cmp_eq_u32 vcc, -1, v16\n"
    "s_nop 0\n"
    "s_cbranch_vccnz Lrty37\n"
    "s_branch Lok38\n"
    "Lrty39:\n"
    "s_cmp_eq_u32 s43, 0\n"
    "s_cbranch_scc1 Lok40\n"
    "s_sub_u32 s43, s43, 1\n"
    "s_sleep 1\n"
    "s_add_u32 s59, s63, 0x2800\n"
    "buffer_load_dwordx4 v[132:135], v1, s[24:27], s59 offen sc1\n"
    "s_add_u32 s59, s63, 0x2c00\n"
    "buffer_load_dwordx4 v[136:139], v1, s[24:27], s59 offen sc1\n"
    "s_waitcnt vmcnt(0)\n"
    "v_max3_u32 v16, v132, v133, v134\n"
    "v_max3_u32 v16, v16, v135, v136\n"
    "v_max3_u32 v16, v16, v137, v138\n"
    "v_max3_u32 v16, v16, v139, v139\n"
    "v_cmp_eq_u32 vcc, -1, v16\n"
    "s_nop 0\n"
    "s_cbranch_vccnz Lrty39\n"
    "s_branch Lok40\n"
    "Lrty41:\n"
    "s_cmp_eq_u32 s43, 0\n"
    "s_cbranch_scc1 Lok42\n"
    "s_sub_u32 s43, s43, 1\n"
    "s_sleep 1\n"
    "s_add_u32 s59, s64, 0x800\n"
    "buffer_load_dwordx4 v[140:143], v1, s[24:27], s59 offen sc1\n"
    "s_add_u32 s59, s64, 0xc00\n"
    "buffer_load_dwordx4 v[144:147], v1, s[24:27], s59 offen sc1\n"
    "s_waitcnt vmcnt(0)\n"
    "v_max3_u32 v23, v140, v141, v142\n"
    "v_max3_u32 v23, v23, v143, v144\n"
    "v_max3_u32 v23, v23, v145, v146\n"
    "v_max3_u32 v23, v23, v147, v147\n"
    "v_cmp_eq_u32 vcc, -1, v23\n"
    "s_nop 0\n"
    "s_cbranch_vccnz Lrty41\n"
    "s_branch Lok42\n"
    "Lrty43:\n"
    "s_cmp_eq_u32 s43, 0\n"
    "s_cbranch_scc1 Lok44\n"
    "s_sub_u32 s43, s43, 1\n"
    "s_sleep 1\n"
    "s_add_u32 s59, s64, 0x1000\n"
    "buffer_load_dwordx4 v[148:151], v1, s[24:27], s59 offen sc1\n"
    "s_add_u32 s59, s64, 0x1400\n"
    "buffer_load_dwordx4 v[152:155], v1, s[24:27], s59 offen sc1\n"
    "s_waitcnt vmcnt(0)\n"
    "v_max3_u32 v23, v148, v149, v150\n"
    "v_max3_u32 v23, v23, v151, v152\n"
    "v_max3_u32 v23, v23, v153, v154\n"
    "v_max3_u32 v23, v23, v155, v155\n"
    "v_cmp_eq_u32 vcc, -1, v23\n"
    "s_nop 0\n"
    "s_cbranch_vccnz Lrty43\n"
    "s_branch Lok44\n"
    "Lrty45:\n"
    "s_cmp_eq_u32 s43, 0\n"
    "s_cbranch_scc1 Lok46\n"
    "s_sub_u32 s43, s43, 1\n"
    "s_sleep 1\n"
    "s_add_u32 s59, s64, 0x1800\n"
    "buffer_load_dwordx4 v[156:159], v1, s[24:27], s59 offen sc1\n"
    "s_add_u32 s59, s64, 0x1c00\n"
    "buffer_load_dwordx4 v[160:163], v1, s[24:27], s59 offen sc1\n"
    "s_waitcnt vmcnt(0)\n"
    "v_max3_u32 v23, v156, v157, v158\n"
    "v_max3_u32 v23, v23, v159, v160\n"
    "v_max3_u32 v23, v23, v161, v162\n"
    "v_max3_u32 v23, v23, v163, v163\n"
    "v_cmp_eq_u32 vcc, -1, v23\n"
    "s_nop 0\n"
    "s_cbranch_vccnz Lrty45\n"
    "s_branch Lok46\n"
    "Lrty47:\n"
    "s_cmp_eq_u32 s43, 0\n"
    "s_cbranch_scc1 Lok48\n"
    "s_sub_u32 s43, s43, 1\n"
    "s_sleep 1\n"
    "s_add_u32 s59, s64, 0x2000\n"
    "buffer_load_dwordx4 v[164:167], v1, s[24:27], s59 offen sc1\n"
    "s_add_u32 s59, s64, 0x2400\n"
    "buffer_load_dwordx4 v[168:171], v1, s[24:27], s59 offen sc1\n"
    "s_waitcnt vmcnt(0)\n"
    "v_max3_u32 v23, v164, v165, v166\n"
    "v_max3_u32 v23, v23, v167, v168\n"
    "v_max3_u32 v23, v23, v169, v170\n"
    "v_max3_u32 v23, v23, v171, v171\n"
    "v_cmp_eq_u32 vcc, -1, v23\n"
    "s_nop 0\n"
    "s_cbranch_vccnz Lrty47\n"
    "s_branch Lok48\n"
    "Lrty49:\n"
    "s_cmp_eq_u32 s43, 0\n"
    "s_cbranch_scc1 Lok50\n"
    "s_sub_u32 s43, s43, 1\n"
    "s_sleep 1\n"
    "s_add_u32 s59, s64, 0x2800\n"
    "buffer_load_dwordx4 v[172:175], v1, s[24:27], s59 offen sc1\n"
    "s_add_u32 s59, s64, 0x2c00\n"
    "buffer_load_dwordx4 v[176:179], v1, s[24:27], s59 offen sc1\n"
    "s_waitcnt vmcnt(0)\n"
    "v_max3_u32 v23, v172, v173, v174\n"
    "v_max3_u32 v23, v23, v175, v176\n"
    "v_max3_u32 v23, v23, v177, v178\n"
    "v_max3_u32 v23, v23, v179, v179\n"
    "v_cmp_eq_u32 vcc, -1, v23\n"
    "s_nop 0\n"
    "s_cbranch_vccnz Lrty49\n"
    "s_branch Lok50\n"
    "Lrty51:\n"
    "s_cmp_eq_u32 s43, 0\n"
    "s_cbranch_scc1 Lok52\n"
    "s_sub_u32 s43, s43, 1\n"
    "s_sleep 1\n"
    "s_add_u32 s59, s64, 0x3000\n"
    "buffer_load_dwordx4 v[180:183], v1, s[24:27], s59 offen sc1\n"
    "s_add_u32 s59, s64, 0x3400\n"
    "buffer_load_dwordx4 v[184:187], v1, s[24:27], s59 offen sc1\n"
    "s_waitcnt vmcnt(0)\n"
    "v_max3_u32 v23, v180, v181, v182\n"
    "v_max3_u32 v23, v23, v183, v184\n"
    "v_max3_u32 v23, v23, v185, v186\n"
    "v_max3_u32 v23, v23, v187, v187\n"
    "v_cmp_eq_u32 vcc, -1, v23\n"
    "s_nop 0\n"
    "s_cbranch_vccnz Lrty51\n"
    "s_branch Lok52\n"
    "Lrty53:\n"
    "s_cmp_eq_u32 s43, 0\n"
    "s_cbranch_scc1 Lok54\n"
    "s_sub_u32 s43, s43, 1\n"
    "s_sleep 1\n"
    "s_add_u32 s59, s64, 0x3800\n"
    "buffer_load_dwordx4 v[188:191], v1, s[24:27], s59 offen sc1\n"
    "s_add_u32 s59, s64, 0x3c00\n"
    "buffer_load_dwordx4 v[192:195], v1, s[24:27], s59 offen sc1\n"
    "s_waitcnt vmcnt(0)\n"
    "v_max3_u32 v23, v188, v189, v190\n"
    "v_max3_u32 v23, v23, v191, v192\n"
    "v_max3_u32 v23, v23, v193, v194\n"
    "v_max3_u32 v23, v23, v195, v195\n"
    "v_cmp_eq_u32 vcc, -1, v23\n"
    "s_nop 0\n"
    "s_cbranch_vccnz Lrty53\n"
    "s_branch Lok54\n"
    "Lrty55:\n"
    "s_cmp_eq_u32 s43, 0\n"
    "s_cbranch_scc1 Lok56\n"
    "s_sub_u32 s43, s43, 1\n"
    "s_sleep 1\n"
    "s_add_u32 s59, s64, 0x4000\n"
    "buffer_load_dwordx4 v[196:199], v1, s[24:27], s59 offen sc1\n"
    "s_add_u32 s59, s64, 0x4400\n"
    "buffer_load_dwordx4 v[200:203], v1, s[24:27], s59 offen sc1\n"
    "s_waitcnt vmcnt(0)\n"
    "v_max3_u32 v23, v196, v197, v198\n"
    "v_max3_u32 v23, v23, v199, v200\n"
    "v_max3_u32 v23, v23, v201, v202\n"
    "v_max3_u32 v23, v23, v203, v203\n"
    "v_cmp_eq_u32 vcc, -1, v23\n"
    "s_nop 0\n"
    "s_cbranch_vccnz Lrty55\n"
    "s_branch Lok56\n"
    "Lrty57:\n"
    "s_cmp_eq_u32 s43, 0\n"
    "s_cbranch_scc1 Lok58\n"
    "s_sub_u32 s43, s43, 1\n"
    "s_sleep 1\n"
    "s_add_u32 s59, s64, 0x4800\n"
    "buffer_load_dwordx4 v[204:207], v1, s[24:27], s59 offen sc1\n"
    "s_add_u32 s59, s64, 0x4c00\n"
    "buffer_load_dwordx4 v[208:211], v1, s[24:27], s59 offen sc1\n"
    "s_waitcnt vmcnt(0)\n"
    "v_max3_u32 v23, v204, v205, v206\n"
    "v_max3_u32 v23, v23, v207, v208\n"
    "v_max3_u32 v23, v23, v209, v210\n"
    "v_max3_u32 v23, v23, v211, v211\n"
    "v_cmp_eq_u32 vcc, -1, v23\n"
    "s_nop 0\n"
    "s_cbranch_vccnz Lrty57\n"
    "s_branch Lok58\n"
    "Lrty59:\n"
    "s_cmp_eq_u32 s43, 0\n"
    "s_cbranch_scc1 Lok60\n"
    "s_sub_u32 s43, s43, 1\n"
    "s_sleep 1\n"
    "s_add_u32 s59, s64, 0\n"
    "buffer_load_dwordx4 v[132:135], v1, s[24:27], s59 offen sc1\n"
    "s_add_u32 s59, s64, 0x400\n"
    "buffer_load_dwordx4 v[136:139], v1, s[24:27], s59 offen sc1\n"
    "s_waitcnt vmcnt(0)\n"
    "v_max3_u32 v23, v132, v133, v134\n"
    "v_max3_u32 v23, v23, v135, v136\n"
    "v_max3_u32 v23, v23, v137, v138\n"
    "v_max3_u32 v23, v23, v139, v139\n"
    "v_cmp_eq_u32 vcc, -1, v23\n"
    "s_nop 0\n"
    "s_cbranch_vccnz Lrty59\n"
    "s_branch Lok60\n"
    "Lrty61:\n"
    "s_cmp_eq_u32 s43, 0\n"
    "s_cbranch_scc1 Lok62\n"
    "s_sub_u32 s43, s43, 1\n"
    "s_sleep 1\n"
    "s_add_u32 s59, s67, 0\n"
    "buffer_load_dwordx4 v[124:127], v1, s[24:27], s59 offen sc1\n"
    "s_waitcnt vmcnt(0)\n"
    "v_max3_u32 v22, v124, v125, v126\n"
    "v_max3_u32 v22, v22, v127, v127\n"
    "v_cmp_eq_u32 vcc, -1, v22\n"
    "s_nop 0\n"
    "s_cbranch_vccnz Lrty61\n"
    "s_branch Lok62\n"
    "Lrty63:\n"
    "s_cmp_eq_u32 s43, 0\n"
    "s_cbranch_scc1 Lok64\n"
    "s_sub_u32 s43, s43, 1\n"
    "s_sleep 1\n"
    "s_add_u32 s59, s68, 0\n"
    "buffer_load_dwordx4 v[24:27], v1, s[24:27], s59 offen sc1\n"
    "s_add_u32 s59, s68, 0x400\n"
    "buffer_load_dwordx4 v[40:43], v1, s[24:27], s59 offen sc1\n"
    "s_waitcnt vmcnt(0)\n"
    "v_max3_u32 v127, v24, v25, v26\n"
    "v_max3_u32 v127, v127, v27, v40\n"
    "v_max3_u32 v127, v127, v41, v42\n"
    "v_max3_u32 v127, v127, v43, v43\n"
    "v_cmp_eq_u32 vcc, -1, v127\n"
    "s_nop 0\n"
    "s_cbranch_vccnz Lrty63\n"
    "s_branch Lok64\n"
    "Lrty65:\n"
    "s_cmp_eq_u32 s43, 0\n"
    "s_cbranch_scc1 Lok66\n"
    "s_sub_u32 s43, s43, 1\n"
    "s_sleep 1\n"
    "s_add_u32 s59, s69, 0\n"
    "buffer_load_dwordx4 v[28:31], v1, s[24:27], s59 offen sc1\n"
    "s_add_u32 s59, s69, 0x400\n"
    "buffer_load_dwordx4 v[44:47], v1, s[24:27], s59 offen sc1\n"
    "s_waitcnt vmcnt(0)\n"
    "v_max3_u32 v127, v28, v29, v30\n"
    "v_max3_u32 v127, v127, v31, v44\n"
    "v_max3_u32 v127, v127, v45, v46\n"
    "v_max3_u32 v127, v127, v47, v47\n"
    "v_cmp_eq_u32 vcc, -1, v127\n"
    "s_nop 0\n"
    "s_cbranch_vccnz Lrty65\n"
    "s_branch Lok66\n"
    "Lrty67:\n"
    "s_cmp_eq_u32 s43, 0\n"
    "s_cbranch_scc1 Lok68\n"
    "s_sub_u32 s43, s43, 1\n"
    "s_sleep 1\n"
    "s_add_u32 s59, s70, 0\n"
    "buffer_load_dwordx4 v[32:35], v1, s[24:27], s59 offen sc1\n"
    "s_add_u32 s59, s70, 0x400\n"
    "buffer_load_dwordx4 v[48:51], v1, s[24:27], s59 offen sc1\n"
    "s_waitcnt vmcnt(0)\n"
    "v_max3_u32 v127, v32, v33, v34\n"
    "v_max3_u32 v127, v127, v35, v48\n"
    "v_max3_u32 v127, v127, v49, v50\n"
    "v_max3_u32 v127, v127, v51, v51\n"
    "v_cmp_eq_u32 vcc, -1, v127\n"
    "s_nop 0\n"
    "s_cbranch_vccnz Lrty67\n"
    "s_branch Lok68\n"
    "Lrty69:\n"
    "s_cmp_eq_u32 s43, 0\n"
    "s_cbranch_scc1 Lok70\n"
    "s_sub_u32 s43, s43, 1\n"
    "s_sleep 1\n"
    "s_add_u32 s59, s71, 0\n"
    "buffer_load_dwordx4 v[36:39], v1, s[24:27], s59 offen sc1\n"
    "s_add_u32 s59, s71, 0x400\n"
    "buffer_load_dwordx4 v[52:55], v1, s[24:27], s59 offen sc1\n"
    "s_waitcnt vmcnt(0)\n"
    "v_max3_u32 v127, v36, v37, v38\n"
    "v_max3_u32 v127, v127, v39, v52\n"
    "v_max3_u32 v127, v127, v53, v54\n"
    "v_max3_u32 v127, v127, v55, v55\n"
    "v_cmp_eq_u32 vcc, -1, v127\n"
    "s_nop 0\n"
    "s_cbranch_vccnz Lrty69\n"
    "s_branch Lok70\n"
    "Lrty71:\n"
    "s_cmp_eq_u32 s43, 0\n"
    "s_cbranch_scc1 Lok72\n"
    "s_sub_u32 s43, s43, 1\n"
    "s_sleep 1\n"
    "s_add_u32 s59, s68, 0x800\n"
    "buffer_load_dwordx4 v[56:59], v1, s[24:27], s59 offen sc1\n"
    "s_add_u32 s59, s68, 0xc00\n"
    "buffer_load_dwordx4 v[72:75], v1, s[24:27], s59 offen sc1\n"
    "s_waitcnt vmcnt(0)\n"
    "v_max3_u32 v127, v56, v57, v58\n"
    "v_max3_u32 v127, v127, v59, v72\n"
    "v_max3_u32 v127, v127, v73, v74\n"
    "v_max3_u32 v127, v127, v75, v75\n"
    "v_cmp_eq_u32 vcc, -1, v127\n"
    "s_nop 0\n"
    "s_cbranch_vccnz Lrty71\n"
    "s_branch Lok72\n"
    "Lrty73:\n"
    "s_cmp_eq_u32 s43, 0\n"
    "s_cbranch_scc1 Lok74\n"
    "s_sub_u32 s43, s43, 1\n"
    "s_sleep 1\n"
    "s_add_u32 s59, s69, 0x800\n"
    "buffer_load_dwordx4 v[60:63], v1, s[24:27], s59 offen sc1\n"
    "s_add_u32 s59, s69, 0xc00\n"
    "buffer_load_dwordx4 v[76:79], v1, s[24:27], s59 offen sc1\n"
    "s_waitcnt vmcnt(0)\n"
    "v_max3_u32 v127, v60, v61, v62\n"
    "v_max3_u32 v127, v127, v63, v76\n"
    "v_max3_u32 v127, v127, v77, v78\n"
    "v_max3_u32 v127, v127, v79, v79\n"
    "v_cmp_eq_u32 vcc, -1, v127\n"
    "s_nop 0\n"
    "s_cbranch_vccnz Lrty73\n"
    "s_branch Lok74\n"
    "Lrty75:\n"
    "s_cmp_eq_u32 s43, 0\n"
    "s_cbranch_scc1 Lok76\n"
    "s_sub_u32 s43, s43, 1\n"
    "s_sleep 1\n"
    "s_add_u32 s59, s70, 0x800\n"
    "buffer_load_dwordx4 v[64:67], v1, s[24:27], s59 offen sc1\n"
    "s_add_u32 s59, s70, 0xc00\n"
    "buffer_load_dwordx4 v[80:83], v1, s[24:27], s59 offen sc1\n"
    "s_waitcnt vmcnt(0)\n"
    "v_max3_u32 v127, v64, v65, v66\n"
    "v_max3_u32 v127, v127, v67, v80\n"
    "v_max3_u32 v127, v127, v81, v82\n"
    "v_max3_u32 v127, v127, v83, v83\n"
    "v_cmp_eq_u32 vcc, -1, v127\n"
    "s_nop 0\n"
    "s_cbranch_vccnz Lrty75\n"
    "s_branch Lok76\n"
    "Lrty77:\n"
    "s_cmp_eq_u32 s43, 0\n"
    "s_cbranch_scc1 Lok78\n"
    "s_sub_u32 s43, s43, 1\n"
    "s_sleep 1\n"
    "s_add_u32 s59, s71, 0x800\n"
    "buffer_load_dwordx4 v[68:71], v1, s[24:27], s59 offen sc1\n"
    "s_add_u32 s59, s71, 0xc00\n"
    "buffer_load_dwordx4 v[84:87], v1, s[24:27], s59 offen sc1\n"
    "s_waitcnt vmcnt(0)\n"
    "v_max3_u32 v127, v68, v69, v70\n"
    "v_max3_u32 v127, v127, v71, v84\n"
    "v_max3_u32 v127, v127, v85, v86\n"
    "v_max3_u32 v127, v127, v87, v87\n"
    "v_cmp_eq_u32 vcc, -1, v127\n"
    "s_nop 0\n"
    "s_cbranch_vccnz Lrty77\n"
    "s_branch Lok78\n"
    "Lrty79:\n"
    "s_cmp_eq_u32 s43, 0\n"
    "s_cbranch_scc1 Lok80\n"
    "s_sub_u32 s43, s43, 1\n"
    "s_sleep 1\n"
    "s_add_u32 s59, s68, 0x1000\n"
    "buffer_load_dwordx4 v[24:27], v1, s[24:27], s59 offen sc1\n"
    "s_add_u32 s59, s68, 0x1400\n"
    "buffer_load_dwordx4 v[40:43], v1, s[24:27], s59 offen sc1\n"
    "s_waitcnt vmcnt(0)\n"
    "v_max3_u32 v127, v24, v25, v26\n"
    "v_max3_u32 v127, v127, v27, v40\n"
    "v_max3_u32 v127, v127, v41, v42\n"
    "v_max3_u32 v127, v127, v43, v43\n"
    "v_cmp_eq_u32 vcc, -1, v127\n"
    "s_nop 0\n"
    "s_cbranch_vccnz Lrty79\n"
    "s_branch Lok80\n"
    "Lrty81:\n"
    "s_cmp_eq_u32 s43, 0\n"
    "s_cbranch_scc1 Lok82\n"
    "s_sub_u32 s43, s43, 1\n"
    "s_sleep 1\n"
    "s_add_u32 s59, s69, 0x1000\n"
    "buffer_load_dwordx4 v[28:31], v1, s[24:27], s59 offen sc1\n"
    "s_add_u32 s59, s69, 0x1400\n"
    "buffer_load_dwordx4 v[44:47], v1, s[24:27], s59 offen sc1\n"
    "s_waitcnt vmcnt(0)\n"
    "v_max3_u32 v127, v28, v29, v30\n"
    "v_max3_u32 v127, v127, v31, v44\n"
    "v_max3_u32 v127, v127, v45, v46\n"
    "v_max3_u32 v127, v127, v47, v47\n"
    "v_cmp_eq_u32 vcc, -1, v127\n"
    "s_nop 0\n"
    "s_cbranch_vccnz Lrty81\n"
    "s_branch Lok82\n"
    "Lrty83:\n"
    "s_cmp_eq_u32 s43, 0\n"
    "s_cbranch_scc1 Lok84\n"
    "s_sub_u32 s43, s43, 1\n"
    "s_sleep 1\n"
    "s_add_u32 s59, s70, 0x1000\n"
    "buffer_load_dwordx4 v[32:35], v1, s[24:27], s59 offen sc1\n"
    "s_add_u32 s59, s70, 0x1400\n"
    "buffer_load_dwordx4 v[48:51], v1, s[24:27], s59 offen sc1\n"
    "s_waitcnt vmcnt(0)\n"
    "v_max3_u32 v127, v32, v33, v34\n"
    "v_max3_u32 v127, v127, v35, v48\n"
    "v_max3_u32 v127, v127, v49, v50\n"
    "v_max3_u32 v127, v127, v51, v51\n"
    "v_cmp_eq_u32 vcc, -1, v127\n"
    "s_nop 0\n"
    "s_cbranch_vccnz Lrty83\n"
    "s_branch Lok84\n"
    "Lrty85:\n"
    "s_cmp_eq_u32 s43, 0\n"
    "s_cbranch_scc1 Lok86\n"
    "s_sub_u32 s43, s43, 1\n"
    "s_sleep 1\n"
    "s_add_u32 s59, s71, 0x1000\n"
    "buffer_load_dwordx4 v[36:39], v1, s[24:27], s59 offen sc1\n"
    "s_add_u32 s59, s71, 0x1400\n"
    "buffer_load_dwordx4 v[52:55], v1, s[24:27], s59 offen sc1\n"
    "s_waitcnt vmcnt(0)\n"
    "v_max3_u32 v127, v36, v37, v38\n"
    "v_max3_u32 v127, v127, v39, v52\n"
    "v_max3_u32 v127, v127, v53, v54\n"
    "v_max3_u32 v127, v127, v55, v55\n"
    "v_cmp_eq_u32 vcc, -1, v127\n"
    "s_nop 0\n"
    "s_cbranch_vccnz Lrty85\n"
    "s_branch Lok86\n"
    "Lrty87:\n"
    "s_cmp_eq_u32 s43, 0\n"
    "s_cbranch_scc1 Lok88\n"
    "s_sub_u32 s43, s43, 1\n"
    "s_sleep 1\n"
    "s_add_u32 s59, s68, 0x1800\n"
    "buffer_load_dwordx4 v[56:59], v1, s[24:27], s59 offen sc1\n"
    "s_add_u32 s59, s68, 0x1c00\n"
    "buffer_load_dwordx4 v[72:75], v1, s[24:27], s59 offen sc1\n"
    "s_waitcnt vmcnt(0)\n"
    "v_max3_u32 v127, v56, v57, v58\n"
    "v_max3_u32 v127, v127, v59, v72\n"
    "v_max3_u32 v127, v127, v73, v74\n"
    "v_max3_u32 v127, v127, v75, v75\n"
    "v_cmp_eq_u32 vcc, -1, v127\n"
    "s_nop 0\n"
    "s_cbranch_vccnz Lrty87\n"
    "s_branch Lok88\n"
    "Lrty89:\n"
    "s_cmp_eq_u32 s43, 0\n"
    "s_cbranch_scc1 Lok90\n"
    "s_sub_u32 s43, s43, 1\n"
    "s_sleep 1\n"
    "s_add_u32 s59, s69, 0x1800\n"
    "buffer_load_dwordx4 v[60:63], v1, s[24:27], s59 offen sc1\n"
    "s_add_u32 s59, s69, 0x1c00\n"
    "buffer_load_dwordx4 v[76:79], v1, s[24:27], s59 offen sc1\n"
    "s_waitcnt vmcnt(0)\n"
    "v_max3_u32 v127, v60, v61, v62\n"
    "v_max3_u32 v127, v127, v63, v76\n"
    "v_max3_u32 v127, v127, v77, v78\n"
    "v_max3_u32 v127, v127, v79, v79\n"
    "v_cmp_eq_u32 vcc, -1, v127\n"
    "s_nop 0\n"
    "s_cbranch_vccnz Lrty89\n"
    "s_branch Lok90\n"
    "Lrty91:\n"
    "s_cmp_eq_u32 s43, 0\n"
    "s_cbranch_scc1 Lok92\n"
    "s_sub_u32 s43, s43, 1\n"
    "s_sleep 1\n"
    "s_add_u32 s59, s70, 0x1800\n"
    "buffer_load_dwordx4 v[64:67], v1, s[24:27], s59 offen sc1\n"
    "s_add_u32 s59, s70, 0x1c00\n"
    "buffer_load_dwordx4 v[80:83], v1, s[24:27], s59 offen sc1\n"
    "s_waitcnt vmcnt(0)\n"
    "v_max3_u32 v127, v64, v65, v66\n"
    "v_max3_u32 v127, v127, v67, v80\n"
    "v_max3_u32 v127, v127, v81, v82\n"
    "v_max3_u32 v127, v127, v83, v83\n"
    "v_cmp_eq_u32 vcc, -1, v127\n"
    "s_nop 0\n"
    "s_cbranch_vccnz Lrty91\n"
    "s_branch Lok92\n"
    "Lrty93:\n"
    "s_cmp_eq_u32 s43, 0\n"
    "s_cbranch_scc1 Lok94\n"
    "s_sub_u32 s43, s43, 1\n"
    "s_sleep 1\n"
    "s_add_u32 s59, s71, 0x1800\n"
    "buffer_load_dwordx4 v[68:71], v1, s[24:27], s59 offen sc1\n"
    "s_add_u32 s59, s71, 0x1c00\n"
    "buffer_load_dwordx4 v[84:87], v1, s[24:27], s59 offen sc1\n"
    "s_waitcnt vmcnt(0)\n"
    "v_max3_u32 v127, v68, v69, v70\n"
    "v_max3_u32 v127, v127, v71, v84\n"
    "v_max3_u32 v127, v127, v85, v86\n"
    "v_max3_u32 v127, v127, v87, v87\n"
    "v_cmp_eq_u32 vcc, -1, v127\n"
    "s_nop 0\n"
    "s_cbranch_vccnz Lrty93\n"
    "s_branch Lok94\n"
    "Lrty95:\n"
    "s_cmp_eq_u32 s43, 0\n"
    "s_cbranch_scc1 Lok96\n"
    "s_sub_u32 s43, s43, 1\n"
    "s_sleep 1\n"
    "s_add_u32 s59, s68, 0x2000\n"
    "buffer_load_dwordx4 v[24:27], v1, s[24:27], s59 offen sc1\n"
    "s_add_u32 s59, s68, 0x2400\n"
    "buffer_load_dwordx4 v[40:43], v1, s[24:27], s59 offen sc1\n"
    "s_waitcnt vmcnt(0)\n"
    "v_max3_u32 v127, v24, v25, v26\n"
    "v_max3_u32 v127, v127, v27, v40\n"
    "v_max3_u32 v127, v127, v41, v42\n"
    "v_max3_u32 v127, v127, v43, v43\n"
    "v_cmp_eq_u32 vcc, -1, v127\n"
    "s_nop 0\n"
    "s_cbranch_vccnz Lrty95\n"
    "s_branch Lok96\n"
    "Lrty97:\n"
    "s_cmp_eq_u32 s43, 0\n"
    "s_cbranch_scc1 Lok98\n"
    "s_sub_u32 s43, s43, 1\n"
    "s_sleep 1\n"
    "s_add_u32 s59, s69, 0x2000\n"
    "buffer_load_dwordx4 v[28:31], v1, s[24:27], s59 offen sc1\n"
    "s_add_u32 s59, s69, 0x2400\n"
    "buffer_load_dwordx4 v[44:47], v1, s[24:27], s59 offen sc1\n"
    "s_waitcnt vmcnt(0)\n"
    "v_max3_u32 v127, v28, v29, v30\n"
    "v_max3_u32 v127, v127, v31, v44\n"
    "v_max3_u32 v127, v127, v45, v46\n"
    "v_max3_u32 v127, v127, v47, v47\n"
    "v_cmp_eq_u32 vcc, -1, v127\n"
    "s_nop 0\n"
    "s_cbranch_vccnz Lrty97\n"
    "s_branch Lok98\n"
    "Lrty99:\n"
    "s_cmp_eq_u32 s43, 0\n"
    "s_cbranch_scc1 Lok100\n"
    "s_sub_u32 s43, s43, 1\n"
    "s_sleep 1\n"
    "s_add_u32 s59, s70, 0x2000\n"
    "buffer_load_dwordx4 v[32:35], v1, s[24:27], s59 offen sc1\n"
    "s_add_u32 s59, s70, 0x2400\n"
    "buffer_load_dwordx4 v[48:51], v1, s[24:27], s59 offen sc1\n"
    "s_waitcnt vmcnt(0)\n"
    "v_max3_u32 v127, v32, v33, v34\n"
    "v_max3_u32 v127, v127, v35, v48\n"
    "v_max3_u32 v127, v127, v49, v50\n"
    "v_max3_u32 v127, v127, v51, v51\n"
    "v_cmp_eq_u32 vcc, -1, v127\n"
    "s_nop 0\n"
    "s_cbranch_vccnz Lrty99\n"
    "s_branch Lok100\n"
    "Lrty101:\n"
    "s_cmp_eq_u32 s43, 0\n"
    "s_cbranch_scc1 Lok102\n"
    "s_sub_u32 s43, s43, 1\n"
    "s_sleep 1\n"
    "s_add_u32 s59, s71, 0x2000\n"
    "buffer_load_dwordx4 v[36:39], v1, s[24:27], s59 offen sc1\n"
    "s_add_u32 s59, s71, 0x2400\n"
    "buffer_load_dwordx4 v[52:55], v1, s[24:27], s59 offen sc1\n"
    "s_waitcnt vmcnt(0)\n"
    "v_max3_u32 v127, v36, v37, v38\n"
    "v_max3_u32 v127, v127, v39, v52\n"
    "v_max3_u32 v127, v127, v53, v54\n"
    "v_max3_u32 v127, v127, v55, v55\n"
    "v_cmp_eq_u32 vcc, -1, v127\n"
    "s_nop 0\n"
    "s_cbranch_vccnz Lrty101\n"
    "s_branch Lok102\n"
    "Lrty103:\n"
    "s_cmp_eq_u32 s43, 0\n"
    "s_cbranch_scc1 Lok104\n"
    "s_sub_u32 s43, s43, 1\n"
    "s_sleep 1\n"
    "s_add_u32 s59, s68, 0x2800\n"
    "buffer_load_dwordx4 v[56:59], v1, s[24:27], s59 offen sc1\n"
    "s_add_u32 s59, s68, 0x2c00\n"
    "buffer_load_dwordx4 v[72:75], v1, s[24:27], s59 offen sc1\n"
    "s_add_u32 s59, s68, 0x3000\n"
    "buffer_load_dwordx4 v[88:91], v1, s[24:27], s59 offen sc1\n"
    "s_add_u32 s59, s68, 0x3400\n"
    "buffer_load_dwordx4 v[104:107], v10, s[24:27], s59 offen sc1\n"
    "s_waitcnt vmcnt(0)\n"
    "v_max3_u32 v127, v56, v57, v58\n"
    "v_max3_u32 v127, v127, v59, v72\n"
    "v_max3_u32 v127, v127, v73, v74\n"
    "v_max3_u32 v127, v127, v75, v88\n"
    "v_max3_u32 v127, v127, v89, v90\n"
    "v_max3_u32 v127, v127, v91, v104\n"
    "v_max3_u32 v127, v127, v105, v106\n"
    "v_max3_u32 v127, v127, v107, v107\n"
    "v_cmp_eq_u32 vcc, -1, v127\n"
    "s_nop 0\n"
    "s_cbranch_vccnz Lrty103\n"
    "s_branch Lok104\n"
    "Lrty105:\n"
    "s_cmp_eq_u32 s43, 0\n"
    "s_cbranch_scc1 Lok106\n"
    "s_sub_u32 s43, s43, 1\n"
    "s_sleep 1\n"
    "s_add_u32 s59, s69, 0x2800\n"
    "buffer_load_dwordx4 v[60:63], v1, s[24:27], s59 offen sc1\n"
    "s_add_u32 s59, s69, 0x2c00\n"
    "buffer_load_dwordx4 v[76:79], v1, s[24:27], s59 offen sc1\n"
    "s_add_u32 s59, s69, 0x3000\n"
    "buffer_load_dwordx4 v[92:95], v1, s[24:27], s59 offen sc1\n"
    "s_add_u32 s59, s69, 0x3400\n"
    "buffer_load_dwordx4 v[108:111], v10, s[24:27], s59 offen sc1\n"
    "s_waitcnt vmcnt(0)\n"
    "v_max3_u32 v127, v60, v61, v62\n"
    "v_max3_u32 v127, v127, v63, v76\n"
    "v_max3_u32 v127, v127, v77, v78\n"
    "v_max3_u32 v127, v127, v79, v92\n"
    "v_max3_u32 v127, v127, v93, v94\n"
    "v_max3_u32 v127, v127, v95, v108\n"
    "v_max3_u32 v127, v127, v109, v110\n"
    "v_max3_u32 v127, v127, v111, v111\n"
    "v_cmp_eq_u32 vcc, -1, v127\n"
    "s_nop 0\n"
    "s_cbranch_vccnz Lrty105\n"
    "s_branch Lok106\n"
    "Lrty107:\n"
    "s_cmp_eq_u32 s43, 0\n"
    "s_cbranch_scc1 Lok108\n"
    "s_sub_u32 s43, s43, 1\n"
    "s_sleep 1\n"
    "s_add_u32 s59, s70, 0x2800\n"
    "buffer_load_dwordx4 v[64:67], v1, s[24:27], s59 offen sc1\n"
    "s_add_u32 s59, s70, 0x2c00\n"
    "buffer_load_dwordx4 v[80:83], v1, s[24:27], s59 offen sc1\n"
    "s_add_u32 s59, s70, 0x3000\n"
    "buffer_load_dwordx4 v[96:99], v1, s[24:27], s59 offen sc1\n"
    "s_add_u32 s59, s70, 0x3400\n"
    "buffer_load_dwordx4 v[112:115], v10, s[24:27], s59 offen sc1\n"
    "s_waitcnt vmcnt(0)\n"
    "v_max3_u32 v127, v64, v65, v66\n"
    "v_max3_u32 v127, v127, v67, v80\n"
    "v_max3_u32 v127, v127, v81, v82\n"
    "v_max3_u32 v127, v127, v83, v96\n"
    "v_max3_u32 v127, v127, v97, v98\n"
    "v_max3_u32 v127, v127, v99, v112\n"
    "v_max3_u32 v127, v127, v113, v114\n"
    "v_max3_u32 v127, v127, v115, v115\n"
    "v_cmp_eq_u32 vcc, -1, v127\n"
    "s_nop 0\n"
    "s_cbranch_vccnz Lrty107\n"
    "s_branch Lok108\n"
    "Lrty109:\n"
    "s_cmp_eq_u32 s43, 0\n"
    "s_cbranch_scc1 Lok110\n"
    "s_sub_u32 s43, s43, 1\n"
    "s_sleep 1\n"
    "s_add_u32 s59, s71, 0x2800\n"
    "buffer_load_dwordx4 v[68:71], v1, s[24:27], s59 offen sc1\n"
    "s_add_u32 s59, s71, 0x2c00\n"
    "buffer_load_dwordx4 v[84:87], v1, s[24:27], s59 offen sc1\n"
    "s_add_u32 s59, s71, 0x3000\n"
    "buffer_load_dwordx4 v[100:103], v1, s[24:27], s59 offen sc1\n"
    "s_add_u32 s59, s71, 0x3400\n"
    "buffer_load_dwordx4 v[116:119], v10, s[24:27], s59 offen sc1\n"
    "s_waitcnt vmcnt(0)\n"
    "v_max3_u32 v127, v68, v69, v70\n"
    "v_max3_u32 v127, v127, v71, v84\n"
    "v_max3_u32 v127, v127, v85, v86\n"
    "v_max3_u32 v127, v127, v87, v100\n"
    "v_max3_u32 v127, v127, v101, v102\n"
    "v_max3_u32 v127, v127, v103, v116\n"
    "v_max3_u32 v127, v127, v117, v118\n"
    "v_max3_u32 v127, v127, v119, v119\n"
    "v_cmp_eq_u32 vcc, -1, v127\n"
    "s_nop 0\n"
    "s_cbranch_vccnz Lrty109\n"
    "s_branch Lok110\n"
    "Lend:\n"
    :
    : "s"(__builtin_amdgcn_kernarg_segment_ptr()), "s"(blockIdx.x), "{v0}"(threadIdx.x), "s"((unsigned)(size_t)lds), "s"(&kTab)
    : "v1", "v2", "v3", "v4", "v5", "v6", "v7", "v8", "v9", "v10", "v11", "v12", "v13", "v14", "v15", "v16", "v17", "v18", "v19", "v20", "v21", "v22", "v23", "v24", "v25", "v26", "v27", "v28", "v29", "v30", "v31", "v32", "v33", "v34", "v35", "v36", "v37", "v38", "v39", "v40", "v41", "v42", "v43", "v44", "v45", "v46", "v47", "v48", "v49", "v50", "v51", "v52", "v53", "v54", "v55", "v56", "v57", "v58", "v59", "v60", "v61", "v62", "v63", "v64", "v65", "v66", "v67", "v68", "v69", "v70", "v71", "v72", "v73", "v74", "v75", "v76", "v77", "v78", "v79", "v80", "v81", "v82", "v83", "v84", "v85", "v86", "v87", "v88", "v89", "v90", "v91", "v92", "v93", "v94", "v95", "v96", "v97", "v98", "v99", "v100", "v101", "v102", "v103", "v104", "v105", "v106", "v107", "v108", "v109", "v110", "v111", "v112", "v113", "v114", "v115", "v116", "v117", "v118", "v119", "v120", "v121", "v122", "v123", "v124", "v125", "v126", "v127", "v128", "v129", "v130", "v131", "v132", "v133", "v134", "v135", "v136", "v137", "v138", "v139", "v140", "v141", "v142", "v143", "v144", "v145", "v146", "v147", "v148", "v149", "v150", "v151", "v152", "v153", "v154", "v155", "v156", "v157", "v158", "v159", "v160", "v161", "v162", "v163", "v164", "v165", "v166", "v167", "v168", "v169", "v170", "v171", "v172", "v173", "v174", "v175", "v176", "v177", "v178", "v179", "v180", "v181", "v182", "v183", "v184", "v185", "v186", "v187", "v188", "v189", "v190", "v191", "v192", "v193", "v194", "v195", "v196", "v197", "v198", "v199", "v200", "v201", "v202", "v203", "v204", "v205", "v206", "v207", "v208", "v209", "v210", "v211", "v212", "v213", "v214", "v215", "v216", "v217", "v218", "v219", "v220", "v221", "v222", "v223", "v224", "v225", "v226", "v227", "v228", "v229", "v230", "v231", "v232", "v233", "v234", "v235", "v236", "v237", "v238", "v239", "v240", "v241", "v242", "v243", "v244", "v245", "v246", "v247", "v248", "v249", "v250", "v251", "v252", "v253", "v254", "v255", "a0", "a1", "a2", "a3", "a4", "a5", "a6", "a7", "a8", "a9", "a10", "a11", "a12", "a13", "a14", "a15", "a16", "a17", "a18", "a19", "a20", "a21", "a22", "a23", "a24", "a25", "a26", "a27", "a28", "a29", "a30", "a31", "a32", "a33", "a34", "a35", "a36", "a37", "a38", "a39", "a40", "a41", "a42", "a43", "a44", "a45", "a46", "a47", "a48", "a49", "a50", "a51", "a52", "a53", "a54", "a55", "a56", "a57", "a58", "a59", "a60", "a61", "a62", "a63", "a64", "a65", "a66", "a67", "a68", "a69", "a70", "a71", "a72", "a73", "a74", "a75", "a76", "a77", "a78", "a79", "a80", "a81", "a82", "a83", "a84", "a85", "a86", "a87", "a88", "a89", "a90", "a91", "a92", "a93", "a94", "a95", "a96", "a97", "a98", "a99", "a100", "a101", "a102", "a103", "a104", "a105", "a106", "a107", "a108", "a109", "a110", "a111", "a112", "a113", "a114", "a115", "a116", "a117", "a118", "a119", "a120", "a121", "a122", "a123", "a124", "a125", "a126", "a127", "a128", "a129", "a130", "a131", "a132", "a133", "a134", "a135", "a136", "a137", "a138", "a139", "a140", "a141", "a142", "a143", "a144", "a145", "a146", "a147", "a148", "a149", "a150", "a151", "a152", "a153", "a154", "a155", "a156", "a157", "a158", "a159", "a160", "a161", "a162", "a163", "a164", "a165", "a166", "a167", "a168", "a169", "a170", "a171", "a172", "a173", "a174", "a175", "a176", "a177", "a178", "a179", "a180", "a181", "a182", "a183", "a184", "a185", "a186", "a187", "a188", "a189", "a190", "a191", "a192", "a193", "a194", "a195", "a196", "a197", "a198", "a199", "a200", "a201", "a202", "a203", "a204", "a205", "a206", "a207", "a208", "a209", "a210", "a211", "a212", "a213", "a214", "a215", "a216", "a217", "a218", "a219", "a220", "a221", "a222", "a223", "a224", "a225", "a226", "a227", "a228", "a229", "a230", "a231", "a232", "a233", "a234", "a235", "a236", "a237", "a238", "a239", "a240", "a241", "a242", "a243", "a244", "a245", "a246", "a247", "a248", "a249", "a250", "a251", "a252", "a253", "a254", "a255", "s6", "s7", "s8", "s9", "s10", "s11", "s12", "s13", "s14", "s15", "s16", "s17", "s18", "s19", "s20", "s21", "s22", "s23", "s24", "s25", "s26", "s27", "s28", "s29", "s30", "s31", "s33", "s34", "s35", "s36", "s37", "s38", "s39", "s40", "s41", "s42", "s43", "s44", "s45", "s46", "s47", "s48", "s49", "s50", "s51", "s52", "s53", "s54", "s55", "s56", "s57", "s58", "s59", "s60", "s61", "s62", "s63", "s64", "s65", "s66", "s67", "s68", "s69", "s70", "s71", "s72", "s73", "s74", "s75", "s76", "s77", "s78", "s79", "s80", "s81", "s82", "s83", "s84", "s85", "s86", "s87", "s88", "s89", "s90", "s91", "s92", "s93", "s94", "s95", "s96", "s97", "s98", "s99", "vcc", "scc", "memory");
}

extern "C" void kernel_launch(void* const* d_in, const int* in_sizes, int n_in, void* d_out, int out_size, void* d_ws, size_t ws_size,
                              hipStream_t stream) {
  fused_block<<<256, 256, 0, stream>>>(d_in[0], d_in[1], d_in[2], d_in[3], d_in[4], d_in[6], d_in[7], d_in[8], d_in[9], d_out, d_ws);
}
```
